```python
import torch

def _softmax(x, axis=-1):
    return torch.softmax(x, dim=axis)
def _gelu(x):
    return torch.nn.functional.gelu(x, approximate="tanh")
def _silu(x):
    return torch.nn.functional.silu(x)
def _sigmoid(x):
    return torch.sigmoid(x)
def _layernorm(x, axis=-1, eps=1e-5):
    m = x.mean(dim=axis, keepdim=True)
    v = x.var(dim=axis, keepdim=True, unbiased=False)
    return (x - m) / torch.sqrt(v + eps)
def _scatter_add(tgt, idx, src, axis=0):
    return tgt.clone().index_add_(axis, idx, src)
def _segment_sum(src, seg, n):
    out = torch.zeros((n,) + src.shape[1:], dtype=src.dtype)
    return out.index_add_(0, seg, src)
def _pad(x, pw):
    flat = tuple(v for pair in reversed(pw) for v in pair)
    return torch.nn.functional.pad(x, flat)

kernel_name = 'interleave_layers__spectral_gfnet__glu_gated_reglu__prompt'

def setup_inputs():

    _w = (lambda a: (lambda t: t.to(torch.bfloat16) if t.is_floating_point() else t)(torch.as_tensor(a)))
    def _A_setup():
        B, S, D = 1, 2048, 512
        Df = D // 2 + 1
        return {
            "x": _w((lambda *s: torch.randn(*s, dtype=torch.bfloat16) * 0.05)(B, S, D)),
            "w_r": _w((lambda *s: torch.randn(*s, dtype=torch.bfloat16) * 0.4)(S, Df) * 0.02),
            "w_i": _w((lambda *s: torch.randn(*s, dtype=torch.bfloat16) * 0.4)(S, Df) * 0.02),
        }
    _A_in = _A_setup()

    def _B_setup():
        B, S, D, F = 1, 2048, 512, 1376
        return {
            "x": _w((lambda *s: torch.randn(*s, dtype=torch.bfloat16) * 0.05)(B, S, D)),
            "W_gate": _w((lambda *s: torch.randn(*s, dtype=torch.bfloat16) * 0.2)(D, F) * (2.0 / D) ** 0.5),
            "W_up": _w((lambda *s: torch.randn(*s, dtype=torch.bfloat16) * 0.2)(D, F) * (2.0 / D) ** 0.5),
            "W_down": _w((lambda *s: torch.randn(*s, dtype=torch.bfloat16) * 0.2)(F, D) * (1.0 / F) ** 0.5),
        }
    _B_in = _B_setup()

    return {
        "A_x": _A_in["x"],
        "A_w_r": _A_in["w_r"],
        "A_w_i": _A_in["w_i"],
        "B_x": _B_in["x"],
        "B_W_gate": _B_in["W_gate"],
        "B_W_up": _B_in["W_up"],
        "B_W_down": _B_in["W_down"],
    }

def reference(A_x, A_w_r, A_w_i, B_x, B_W_gate, B_W_up, B_W_down):
    def _A_ref(x, w_r, w_i):
        B, S, D = x.shape
        X = (lambda x, axes=(-2,-1): torch.fft.rfft2(x.float(), dim=axes))(x, axes=(-2, -1))
        W = w_r + 1j * w_i
        X = X * W
        y = (lambda x, s, axes=(-2,-1): torch.fft.irfft2(x, s=s, dim=axes).to(torch.bfloat16))(X, (S, D), axes=(-2, -1))
        return y

    def _B_ref(x, W_gate, W_up, W_down):
        gate = torch.einsum("bsd,df->bsf", x, W_gate)
        up = torch.einsum("bsd,df->bsf", x, W_up)
        gate = gate * (lambda x, dt: x.to(dt))(gate > 0, gate.dtype)
        h = gate * up
        return torch.einsum("bsf,fd->bsd", h, W_down)

    x = A_x
    x = x + _A_ref(_layernorm(x, axis=-1), A_w_r, A_w_i)
    x = x + _B_ref(_layernorm(x, axis=-1), B_W_gate, B_W_up, B_W_down)
    x = x + _A_ref(_layernorm(x, axis=-1), A_w_r, A_w_i)
    x = x + _B_ref(_layernorm(x, axis=-1), B_W_gate, B_W_up, B_W_down)
    return x
```

```cpp
#include <hip/hip_runtime.h>
#include <hip/hip_bf16.h>

typedef unsigned u32;
typedef unsigned short u16;
typedef __attribute__((ext_vector_type(8))) __bf16 bf8;
typedef __attribute__((ext_vector_type(2))) __bf16 bf2;
typedef __attribute__((ext_vector_type(2))) float f2;
typedef __attribute__((ext_vector_type(4))) float f4;
typedef __attribute__((ext_vector_type(16))) float f16v;
typedef __attribute__((ext_vector_type(2))) u32 u2;
typedef __attribute__((ext_vector_type(4))) u32 u4;
typedef __attribute__((ext_vector_type(4))) short s4;
typedef u4 __attribute__((aligned(2))) u4_ua;
#define DI __device__ __forceinline__
#define LDSP __attribute__((address_space(3)))
#define GLBP __attribute__((address_space(1)))

constexpr int SS = 2048;
constexpr float EPS = 1e-5f;

struct Part { u16 v[16384]; };
#if defined(__HIP_DEVICE_COMPILE__)
constexpr double kPi = 3.14159265358979323846;
constexpr double csin(double x) { double t = x, s = x; for (int i = 1; i < 14; ++i) { t *= -x * x / ((2 * i) * (2 * i + 1)); s += t; } return s; }
constexpr u16 tobf(double x) { float f = (float)x; u32 u = __builtin_bit_cast(u32, f); u += 0x7fffu + ((u >> 16) & 1u); return (u16)(u >> 16); }
struct CosT { u16 v[512]; };
constexpr CosT mkcos() {
  CosT c{};
  for (int i = 0; i < 512; ++i) {
    int q = (i + 128) & 511;
    int y = q <= 128 ? q : (q <= 384 ? 256 - q : q - 512);
    c.v[i] = y == 0 ? (u16)0 : tobf(csin(2 * kPi * y / 512) / 16.0);
  }
  return c;
}
constexpr CosT kCos = mkcos();
constexpr u16 kDC = tobf(0.04419417382415922);
constexpr Part mkR1(int st) {
  Part p{};
  for (int e = 0; e < 16384; ++e) {
    int d = ((e >> 9) << 4) + ((e >> 5) & 8) + (e & 7), s = 32 * st + ((e >> 3) & 31);
    p.v[e] = s >= 2 ? kCos.v[((s >> 1) * d + ((s & 1) << 7)) & 511] : (u16)(kDC | ((s == 0 && (d & 1)) ? 0x8000 : 0));
  }
  return p;
}
template <int I> struct PR { static constexpr Part v = mkR1(I); };
struct Tab { Part p[16]; };
__device__ const __attribute__((aligned(1024))) Tab R1T = {{PR<0>::v, PR<1>::v, PR<2>::v, PR<3>::v, PR<4>::v, PR<5>::v, PR<6>::v, PR<7>::v,
  PR<8>::v, PR<9>::v, PR<10>::v, PR<11>::v, PR<12>::v, PR<13>::v, PR<14>::v, PR<15>::v}};
#else
struct Tab { u16 p[8]; };
__device__ const Tab R1T = {};
#endif

DI f16v mf32(u4 a, u4 b, f16v c) { return __builtin_amdgcn_mfma_f32_32x32x16_bf16(__builtin_bit_cast(bf8, a), __builtin_bit_cast(bf8, b), c, 0, 0, 0); }
DI f4 mf16(u4 a, u4 b, f4 c) { return __builtin_amdgcn_mfma_f32_16x16x32_bf16(__builtin_bit_cast(bf8, a), __builtin_bit_cast(bf8, b), c, 0, 0, 0); }
DI u32 pk(float a, float b) { f2 v = {a, b}; return __builtin_bit_cast(u32, __builtin_convertvector(v, bf2)); }
DI float lo(u32 u) { return __builtin_bit_cast(float, u << 16); }
DI float hi(u32 u) { return __builtin_bit_cast(float, u & 0xffff0000u); }
DI float dot2(u32 a, u32 b, float c) { return __builtin_amdgcn_fdot2_f32_bf16(__builtin_bit_cast(bf2, a), __builtin_bit_cast(bf2, b), c, false); }
DI void glds(const void* g, char* l) { __builtin_amdgcn_global_load_lds((const GLBP void*)g, (LDSP void*)l, 16, 0, 0); }
DI u4 lds128(const char* p) { return *(const u4*)p; }
DI u4 pack8(const f16v& a, int s) { u4 o = {pk(a[8 * s], a[8 * s + 1]), pk(a[8 * s + 2], a[8 * s + 3]), pk(a[8 * s + 4], a[8 * s + 5]), pk(a[8 * s + 6], a[8 * s + 7])}; return o; }
template <int N> DI void waitv() { asm volatile("s_waitcnt vmcnt(%0)" ::"n"(N) : "memory"); }
DI void bar() { asm volatile("s_waitcnt lgkmcnt(0)" ::: "memory"); __builtin_amdgcn_s_barrier(); asm volatile("" ::: "memory"); }
DI int dperm(int m) { return (m & 3) + 4 * (m >> 3) + 16 * ((m >> 2) & 1); }
DI int fperm(int m) { int i = (m & 3) + 4 * (m >> 3); return 16 * (i >> 3) + 8 * ((m >> 2) & 1) + (i & 7); }
template <int I> struct IC { static constexpr int v = I; };
template <int N, class F> DI void unroll(F&& f) { if constexpr (N > 0) { unroll<N - 1>(f); f(IC<N - 1>{}); } }

template <int JOB>
DI void prep_gemm(const u16* __restrict__ q0, const u16* __restrict__ q1, int qt, int nt, char* lds,
                  u32* __restrict__ o0, u32* __restrict__ o1) {
  const int lane = threadIdx.x & 63, w = __builtin_amdgcn_readfirstlane(threadIdx.x >> 6);
  const int wp = w >> 1, wq = w & 1, r = lane & 31, h = lane >> 5;
  const char* pS = (const char*)&R1T + (size_t)(4 * nt + w) * 32768 + lane * 16;
  const char* qS[4];
#pragma unroll
  for (int i = 0; i < 4; ++i) {
    if (JOB == 1) {
      int d = 16 * w + 4 * i + (lane >> 4), pos = lane & 15;
      int f = 64 * qt + 16 * (((pos >> 1) & 3) ^ (d & 3)) + 8 * (pos & 1);
      f = f > 1368 ? 1368 : f;
      qS[i] = (const char*)((pos >> 3) ? q1 : q0) + ((size_t)d * 1376 + f) * 2;
    } else {
      int u = 32 * w + 8 * i + (lane >> 3), row;
      if (JOB == 0) { int rho = 128 * qt + (u & 64) + 2 * (u & 31) + ((u >> 5) & 1); row = 32 * (rho & 63) + (rho >> 6); }
      else { row = 128 * qt + (u & 96) + fperm(u & 31); row = row > 1375 ? 1375 : row; }
      qS[i] = (const char*)q0 + (size_t)row * 1024 + (((lane & 7) ^ ((u >> 1) & 7)) << 4);
    }
  }
  constexpr int QSTEP = JOB == 1 ? 64 * 1376 * 2 : 128;
  auto issue = [&](int c) {
    char* sl = lds + (c & 3) * 32768;
#pragma unroll
    for (int i = 0; i < 4; ++i) glds(pS + (4 * c + i) * 1024, sl + (4 * w + i) * 1024);
#pragma unroll
    for (int i = 0; i < 4; ++i) glds(qS[i] + (size_t)c * QSTEP, sl + 16384 + (4 * w + i) * 1024);
  };
  f16v acc[2][2] = {}, acc2[2][2] = {};
  float s1 = 0.f, s2 = 0.f;
  const int pOff = wp * 8192 + lane * 16;
  int qOff;
  if (JOB == 1) {
    int g = lane >> 4, qr = (lane >> 2) & 3, run = 4 * (g & 1) + (lane & 3), q = run >> 1;
    qOff = 16384 + (8 * (g >> 1) + qr) * 256 + (q & 1) * 128 + (8 * (run & 1) + 4 * (q >> 1)) * 2;
  } else qOff = 16384 + (64 * wq + r) * 128;
  issue(0); issue(1); issue(2); issue(3);
  unroll<8>([&](auto C) {
    constexpr int c = C.v;
    waitv<c == 0 ? 24 : c <= 5 ? 16 : c == 6 ? 8 : 0>();
    bar();
    if constexpr (c >= 1 && c <= 4) issue(c + 3);
    const char* sl = lds + (c & 3) * 32768;
#pragma unroll
    for (int ks = 0; ks < 4; ++ks) {
      u4 P[2], Q[2];
#pragma unroll
      for (int t = 0; t < 2; ++t) P[t] = lds128(sl + pOff + t * 4096 + ks * 1024);
#pragma unroll
      for (int b = 0; b < 2; ++b) {
        if (JOB == 1) {
          const char* a = sl + qOff + ks * 4096 + (((2 * wq + b) ^ ((lane >> 2) & 3)) << 5);
          u2 x = __builtin_bit_cast(u2, __builtin_amdgcn_ds_read_tr16_b64_v4i16((LDSP s4*)a));
          u2 y = __builtin_bit_cast(u2, __builtin_amdgcn_ds_read_tr16_b64_v4i16((LDSP s4*)(a + 1024)));
          Q[b] = u4{x[0], x[1], y[0], y[1]};
        } else Q[b] = lds128(sl + qOff + b * 4096 + (((2 * ks + h) ^ ((r >> 1) & 7)) << 4));
      }
#pragma unroll
      for (int t = 0; t < 2; ++t)
#pragma unroll
        for (int b = 0; b < 2; ++b) acc[t][b] = JOB == 2 ? mf32(Q[b], P[t], acc[t][b]) : mf32(P[t], Q[b], acc[t][b]);
      if (JOB == 0) {
        u4 v = wp ? Q[1] : Q[0];
#pragma unroll
        for (int j = 0; j < 4; ++j) { s1 = dot2(v[j], 0x3f803f80u, s1); s2 = dot2(v[j], v[j], s2); }
      }
      if (JOB == 2) {
        if (c == 2 * nt + wp) {
          int e = dperm(r) - 16 * (ks & 1) - 8 * h;
          u4 I;
#pragma unroll
          for (int j = 0; j < 4; ++j) I[j] = (e == 2 * j ? 0x3f80u : 0u) | (e == 2 * j + 1 ? 0x3f800000u : 0u);
#pragma unroll
          for (int b = 0; b < 2; ++b) acc2[ks >> 1][b] = mf32(Q[b], I, acc2[ks >> 1][b]);
        }
      }
    }
  });
  if (JOB == 0) {
#pragma unroll
    for (int t = 0; t < 2; ++t)
#pragma unroll
      for (int i = 0; i < 16; i += 2) {
        int col = 64 * nt + 32 * wp + 16 * t + (((i & 3) + 8 * (i >> 2) + 4 * h) >> 1);
        u2 v = {pk(acc[t][0][i], acc[t][0][i + 1]), pk(acc[t][1][i], acc[t][1][i + 1])};
        *(u2*)(o0 + col * SS + 128 * qt + 64 * wq + 2 * r) = v;
      }
    s1 += __shfl_xor(s1, 32); s2 += __shfl_xor(s2, 32);
    float m = s1 * (1.f / 512), var = fmaxf(s2 * (1.f / 512) - m * m, 0.f);
    if (nt == 0 && h == 0) ((float*)o1)[128 * qt + 64 * wq + 2 * r + wp] = rsqrtf(var + EPS);
  } else if (JOB == 1) {
#pragma unroll
    for (int b = 0; b < 2; ++b) {
      int tile = 4 * qt + 2 * wq + b;
      if (tile < 86)
#pragma unroll
        for (int t = 0; t < 2; ++t)
#pragma unroll
          for (int s = 0; s < 2; ++s) {
            int ks = 8 * nt + 4 * wp + 2 * t + s;
            u4 v = pack8(acc[t][b], s);
            if (ks == 0 && h == 0) v[0] &= 0xffffu;
            *(u4*)(o0 + (tile * 32 + ks) * 256 + lane * 4) = v;
          }
    }
  } else {
#pragma unroll
    for (int b = 0; b < 2; ++b)
#pragma unroll
      for (int s = 0; s < 2; ++s) {
        int ks = 8 * qt + 4 * wq + 2 * b + s;
        if (ks < 86)
#pragma unroll
          for (int t = 0; t < 2; ++t) {
            int o = ((4 * nt + 2 * wp + t) * 86 + ks) * 256 + lane * 4;
            *(u4*)(o0 + o) = pack8(acc[t][b], s);
            *(u4*)(o1 + o) = pack8(acc2[t][b], s);
          }
      }
  }
}

__global__ __launch_bounds__(256, 1) void k_prep(const u16* __restrict__ x, const u16* __restrict__ wr, const u16* __restrict__ wi,
    const u16* __restrict__ wg, const u16* __restrict__ wu, const u16* __restrict__ wd,
    u32* __restrict__ xh, u32* __restrict__ rstd, u32* __restrict__ wgu, u32* __restrict__ wd1, u32* __restrict__ wd2,
    u32* __restrict__ filt, u32* __restrict__ r2) {
  __shared__ __attribute__((aligned(1024))) char lds[131072];
  const int b = blockIdx.x, xc = b & 7, j = b >> 3, nt = xc & 3, q = 2 * j + (xc >> 2);
  const int lane = threadIdx.x & 63, w = threadIdx.x >> 6;
  if (q < 16) prep_gemm<0>(x, x, q, nt, lds, xh, rstd);
  else if (q < 38) prep_gemm<1>(wg, wu, q - 16, nt, lds, wgu, wgu);
  else if (q < 49) prep_gemm<2>(wd, wd, q - 38, nt, lds, wd1, wd2);
  else if (q < 57) {
#pragma unroll
    for (int it = 0; it < 2; ++it) {
      int unit = (((q - 49) * 4 + nt) * 4 + w) * 2 + it, cb = unit & 7, wmg = unit >> 3;
      int c0 = 32 * cb + 8 * (lane >> 4), k0 = 16 * (wmg >> 3) + (lane & 15) + 64 * (16 * ((wmg >> 2) & 1) + 4 * (wmg & 3));
      u4 a[4], bb[4];
#pragma unroll
      for (int i = 0; i < 4; ++i) {
        a[i] = *(const u4_ua*)(wr + (k0 + 64 * i) * 257 + 1 + c0);
        bb[i] = *(const u4_ua*)(wi + (k0 + 64 * i) * 257 + 1 + c0);
      }
#pragma unroll
      for (int e = 0; e < 8; ++e) {
        u4 v;
#pragma unroll
        for (int i = 0; i < 4; ++i) v[i] = (e & 1) ? ((a[i][e >> 1] >> 16) | (bb[i][e >> 1] & 0xffff0000u)) : ((a[i][e >> 1] & 0xffffu) | (bb[i][e >> 1] << 16));
        int col = (c0 + e + 1) & 255;
        *(u4*)(filt + col * SS + (wmg >> 2) * 256 + ((wmg & 3) * 16 + (lane & 15)) * 4) = v;
      }
    }
  } else if (q < 61) {
    int dt = (q - 57) * 4 + nt, d = 32 * dt + dperm(lane & 31), h = lane >> 5;
#pragma unroll
    for (int it = 0; it < 8; ++it) {
      int ks = 8 * w + it;
      u4 v;
#pragma unroll
      for (int jj = 0; jj < 4; ++jj) {
        int c = 8 * ks + 4 * (jj >> 1) + 2 * h + (jj & 1);
        float a = (float)((c * d) & 511) * (1.f / 512);
        float re = __builtin_amdgcn_cosf(a) * 0.0625f, im = __builtin_amdgcn_sinf(a) * -0.0625f;
        if (c == 0) { im = 0.04419417382f; re = (d & 1) ? -im : im; }
        v[jj] = pk(re, im);
      }
      *(u4*)(r2 + (dt * 32 + ks) * 256 + lane * 4) = v;
    }
  }
}

DI u4 rot(u4 v) { u4 o; for (int j = 0; j < 4; ++j) o[j] = __builtin_amdgcn_alignbit(v[j], v[j], 16) ^ 0x8000u; return o; }
template <int NP>
__global__ __launch_bounds__(256, 1) void k_fft(const u32* __restrict__ xin, const float* __restrict__ st, const u32* __restrict__ filt,
                                                u32* __restrict__ xout) {
  __shared__ __attribute__((aligned(256))) char lds[16384];
  const int col = blockIdx.x, tid = threadIdx.x, lane = tid & 63, w = tid >> 6, l15 = lane & 15, g = lane >> 4;
  const u32* xc = xin + col * SS;
  u4 xd[2], xr[2], fw[2];
  f4 sv[NP][2];
#pragma unroll
  for (int i = 0; i < 2; ++i) xd[i] = *(const u4*)(xc + 8 * tid + 4 * i);
#pragma unroll
  for (int p = 0; p < NP; ++p)
#pragma unroll
    for (int i = 0; i < 2; ++i) sv[p][i] = *(const f4*)(st + p * SS + 8 * tid + 4 * i);
#pragma unroll
  for (int i = 0; i < 2; ++i) {
    fw[i] = *(const u4*)(filt + col * SS + (2 * w + i) * 256 + lane * 4);
    xr[i] = *(const u4*)(xc + 64 * (16 * i + l15) + 16 * w + 4 * g);
  }
  __builtin_amdgcn_sched_barrier(0);
  const int k1 = 16 * w + l15;
  u4 F64r[4], F64i[4], F32r[2][2], F32i[2][2];
  float t1c[2][4], t1s[2][4], t2c[2][4], t2s[2][4];
#pragma unroll
  for (int s = 0; s < 4; ++s) {
#pragma unroll
    for (int j = 0; j < 4; ++j) { float a = (float)(((16 * s + 4 * g + j) * k1) & 63) * (1.f / 64); F64r[s][j] = pk(__builtin_amdgcn_cosf(a), __builtin_amdgcn_sinf(a)); }
    F64i[s] = rot(F64r[s]);
  }
#pragma unroll
  for (int m = 0; m < 2; ++m)
#pragma unroll
    for (int t = 0; t < 2; ++t) {
#pragma unroll
      for (int j = 0; j < 4; ++j) { float a = (float)(((16 * t + 4 * g + j) * (16 * m + l15)) & 31) * (1.f / 32); F32r[m][t][j] = pk(__builtin_amdgcn_cosf(a), __builtin_amdgcn_sinf(a)); }
      F32i[m][t] = rot(F32r[m][t]);
    }
#pragma unroll
  for (int t = 0; t < 2; ++t)
#pragma unroll
    for (int j = 0; j < 4; ++j) {
      float a = (float)((16 * t + 4 * g + j) * k1) * (1.f / 2048), b = (float)((16 * t + l15) * (16 * w + 4 * g + j)) * (1.f / 2048);
      t1c[t][j] = __builtin_amdgcn_cosf(a); t1s[t][j] = __builtin_amdgcn_sinf(a);
      t2c[t][j] = __builtin_amdgcn_cosf(b) * (1.f / 2048); t2s[t][j] = __builtin_amdgcn_sinf(b) * (1.f / 2048);
    }
  __builtin_amdgcn_sched_barrier(0);
  const u32 msk = col == 0 ? 0xffffu : 0xffffffffu;
#pragma unroll
  for (int i = 0; i < 2; ++i) {
    f4 rs = sv[0][i];
    if (NP > 1) {
#pragma unroll
      for (int p = 1; p < NP; ++p) rs += sv[p][i];
#pragma unroll
      for (int j = 0; j < 4; ++j) rs[j] = rsqrtf(rs[j] * (1.f / 512) + EPS);
    }
    u4 z;
#pragma unroll
    for (int j = 0; j < 4; ++j) z[j] = pk(lo(xd[i][j]) * rs[j], hi(xd[i][j]) * rs[j]) & msk;
    int n2 = tid >> 3;
    *(u4*)(lds + n2 * 256 + (((2 * (tid & 7) + i) ^ (n2 & 15)) << 4)) = z;
  }
  __syncthreads();
  f4 ar[2], ai[2];
  u4 T[2];
#pragma unroll
  for (int t = 0; t < 2; ++t) {
    ar[t] = f4{0, 0, 0, 0}; ai[t] = ar[t];
#pragma unroll
    for (int s = 0; s < 4; ++s) {
      u4 z = lds128(lds + (16 * t + l15) * 256 + (((4 * s + g) ^ l15) << 4));
      ar[t] = mf16(z, F64r[s], ar[t]); ai[t] = mf16(z, F64i[s], ai[t]);
    }
#pragma unroll
    for (int j = 0; j < 4; ++j) T[t][j] = pk(ar[t][j] * t1c[t][j] + ai[t][j] * t1s[t][j], ai[t][j] * t1c[t][j] - ar[t][j] * t1s[t][j]);
  }
  u4 Y[2];
#pragma unroll
  for (int m = 0; m < 2; ++m) {
    f4 xr_ = {0, 0, 0, 0}, xi_ = xr_;
#pragma unroll
    for (int t = 0; t < 2; ++t) { xr_ = mf16(F32r[m][t], T[t], xr_); xi_ = mf16(F32i[m][t], T[t], xi_); }
#pragma unroll
    for (int j = 0; j < 4; ++j) { float a = lo(fw[m][j]), b = hi(fw[m][j]); Y[m][j] = pk(xr_[j] * a - xi_[j] * b, -(xr_[j] * b + xi_[j] * a)); }
  }
#pragma unroll
  for (int t = 0; t < 2; ++t) {
    f4 vr = {0, 0, 0, 0}, vi = vr;
#pragma unroll
    for (int m = 0; m < 2; ++m) { vr = mf16(Y[m], F32r[t][m], vr); vi = mf16(Y[m], F32i[t][m], vi); }
    u4 v;
#pragma unroll
    for (int j = 0; j < 4; ++j) v[j] = pk(vr[j] * t2c[t][j] + vi[j] * t2s[t][j], vi[j] * t2c[t][j] - vr[j] * t2s[t][j]);
    *(u4*)(lds + 8192 + (16 * t + l15) * 256 + (((4 * w + g) ^ l15) << 4)) = v;
  }
  __syncthreads();
#pragma unroll
  for (int t = 0; t < 2; ++t) {
    f4 yr = {0, 0, 0, 0}, yi = yr;
#pragma unroll
    for (int s = 0; s < 4; ++s) {
      u4 v = lds128(lds + 8192 + (16 * t + l15) * 256 + (((4 * s + g) ^ l15) << 4));
      yr = mf16(F64r[s], v, yr); yi = mf16(F64i[s], v, yi);
    }
    u4 o;
#pragma unroll
    for (int j = 0; j < 4; ++j) o[j] = pk(lo(xr[t][j]) + yr[j], hi(xr[t][j]) - (col == 0 ? 0.f : yi[j]));
    *(u4*)(xout + col * SS + 64 * (16 * t + l15) + 16 * w + 4 * g) = o;
  }
}

template <int LAST>
__global__ __launch_bounds__(256, 1) void k_up(const u32* __restrict__ xh, const u32* __restrict__ wgu, const u32* __restrict__ r2,
                                               u32* __restrict__ hb, u32* __restrict__ x3) {
  constexpr int NT = 6 + LAST, CH = NT * 4096 + 16384, NS = LAST ? 3 : 4, NI = NT + 4;
  __shared__ __attribute__((aligned(1024))) char lds[NS * CH];
  const int xc = blockIdx.x & 7, jb = blockIdx.x >> 3, mt = 8 * (xc & 1) + (jb & 7), ng = 4 * (xc >> 1) + (jb >> 3);
  const int t0 = (ng * 43) >> 3, t1 = ((ng + 1) * 43) >> 3;
  const int lane = threadIdx.x & 63, w = __builtin_amdgcn_readfirstlane(threadIdx.x >> 6);
  const int wm = w >> 1, wn = w & 1, r = lane & 31, h = lane >> 5;
  const u32* pS = wgu + t0 * 8192 + lane * 4;
  const u32* rS = r2 + (ng * 32 + w) * 256 + lane * 4;
  const u32* qS = xh + (8 * w + h) * SS + 128 * mt + 4 * r;
  auto issue = [&](int c) {
    char* sl = lds + (c % NS) * CH;
#pragma unroll
    for (int i = 0; i < 4; ++i) glds(qS + (32 * c + 2 * i) * SS, sl + NT * 4096 + (4 * w + i) * 1024);
#pragma unroll
    for (int i = 0; i < 6; ++i) { int e = 6 * w + i; glds(pS + (e >> 2) * 8192 + (4 * c + (e & 3)) * 256, sl + e * 1024); }
    if (LAST) glds(rS + c * 1024, sl + (24 + w) * 1024);
  };
  f16v acc[3][2] = {}, accx = {};
  float ss0 = 0.f, ss1 = 0.f;
  const int pOff = wn * 12288 + lane * 16, qOff = NT * 4096 + 2 * h * 512 + (64 * wm + 2 * r) * 4;
#pragma unroll
  for (int c = 0; c < NS; ++c) issue(c);
  unroll<8>([&](auto C) {
    constexpr int c = C.v;
    constexpr int last = c == 0 ? NS - 1 : (c + NS - 2 > 7 ? 7 : c + NS - 2);
    waitv<(last - c) * NI>();
    bar();
    if constexpr (c >= 1 && c + NS - 1 <= 7) issue(c + NS - 1);
    const char* sl = lds + (c % NS) * CH;
#pragma unroll
    for (int ks = 0; ks < 4; ++ks) {
      u4 P[3], Q0, Q1;
#pragma unroll
      for (int j = 0; j < 4; ++j) { u2 v = *(const u2*)(sl + qOff + (8 * ks + 4 * (j >> 1) + (j & 1)) * 512); Q0[j] = v[0]; Q1[j] = v[1]; }
#pragma unroll
      for (int t = 0; t < 3; ++t) P[t] = lds128(sl + pOff + t * 4096 + ks * 1024);
#pragma unroll
      for (int t = 0; t < 3; ++t) { acc[t][0] = mf32(P[t], Q0, acc[t][0]); acc[t][1] = mf32(P[t], Q1, acc[t][1]); }
      if (LAST) accx = mf32(lds128(sl + 24576 + ks * 1024 + lane * 16), wn ? Q1 : Q0, accx);
      if (c == 0 && ks == 0 && h == 0) { Q0[0] &= 0xffffu; Q1[0] &= 0xffffu; }
#pragma unroll
      for (int j = 0; j < 4; ++j) { ss0 = dot2(Q0[j], Q0[j], ss0); ss1 = dot2(Q1[j], Q1[j], ss1); }
    }
  });
  ss0 += __shfl_xor(ss0, 32); ss1 += __shfl_xor(ss1, 32);
  const float rs[2] = {1.f / (ss0 * (1.f / 512) + EPS), 1.f / (ss1 * (1.f / 512) + EPS)};
#pragma unroll
  for (int t = 0; t < 3; ++t) {
    int tile = t0 + 3 * wn + t;
    if (tile < t1)
#pragma unroll
      for (int b = 0; b < 2; ++b) {
        const f16v& a = acc[t][b];
        u4 o;
#pragma unroll
        for (int j = 0; j < 4; ++j) { int i = 8 * (j >> 1) + 2 * (j & 1); o[j] = pk(fmaxf(a[i], 0.f) * rs[b] * a[i + 4], fmaxf(a[i + 1], 0.f) * rs[b] * a[i + 5]); }
        *(u4*)(hb + (((2 * mt + wm) * 2 + b) * 86 + tile) * 256 + lane * 4) = o;
      }
  }
  if (LAST) {
    u32* o = x3 + (128 * mt + 64 * wm + 2 * r + wn) * 256 + 16 * ng + 8 * h;
    *(u4*)o = pack8(accx, 0); *(u4*)(o + 4) = pack8(accx, 1);
  }
}

template <int LAST>
__global__ __launch_bounds__(256, 1) void k_down(const u32* __restrict__ hb, const u32* __restrict__ wd, const u32* __restrict__ res,
                                                 u32* __restrict__ out, float* __restrict__ ssp) {
  __shared__ __attribute__((aligned(256))) char lds[4 * 3 * 4 * 1024 + 1024];
  const int xc = blockIdx.x & 7, jb = blockIdx.x >> 3, rg = 8 * (xc & 3) + (jb & 7), nt = 4 * (xc >> 2) + (jb >> 3);
  const int lane = threadIdx.x & 63, w = __builtin_amdgcn_readfirstlane(threadIdx.x >> 6), r = lane & 31, h = lane >> 5;
  const u32* pA = wd + (2 * nt * 86 + w) * 256 + lane * 4;
  const u32* pB = hb + (2 * rg * 86 + w) * 256 + lane * 4;
  u4 A[22][2], B[22][2];
#pragma unroll
  for (int i = 0; i < 22; ++i) {
    int o = (i == 21 && w >= 2) ? 80 * 256 : i * 1024;
    A[i][0] = *(const u4*)(pA + o); B[i][0] = *(const u4*)(pB + o);
    A[i][1] = *(const u4*)(pA + o + 86 * 256); B[i][1] = *(const u4*)(pB + o + 86 * 256);
  }
  u4 rv[2];
  if (LAST) {
    const u32* p = res + (64 * rg + 2 * r + (w & 1)) * 256 + 32 * nt + 16 * (w >> 1) + 8 * h;
    rv[0] = *(const u4*)p; rv[1] = *(const u4*)(p + 4);
  } else {
#pragma unroll
    for (int j = 0; j < 4; ++j) {
      u2 v = *(const u2*)(res + (32 * nt + 16 * (w >> 1) + 8 * (w & 1) + 4 * (j >> 1) + 2 * h + (j & 1)) * SS + 64 * rg + 2 * r);
      rv[0][j] = v[0]; rv[1][j] = v[1];
    }
  }
  __builtin_amdgcn_sched_barrier(0);
  f16v acc[2][2] = {};
#pragma unroll
  for (int i = 0; i < 22; ++i) {
    if (i == 21 && w >= 2) { A[i][0] = u4{0, 0, 0, 0}; A[i][1] = A[i][0]; }
#pragma unroll
    for (int t = 0; t < 2; ++t)
#pragma unroll
      for (int b = 0; b < 2; ++b) acc[t][b] = mf32(A[i][t], B[i][b], acc[t][b]);
  }
  f4 own[4];
#pragma unroll
  for (int p = 0; p < 4; ++p) {
#pragma unroll
    for (int q = 0; q < 4; ++q) {
      const f16v& a = LAST ? acc[p >> 1][p & 1] : acc[p >> 1][q >> 1];
      const int i = LAST ? 4 * q : 8 * (p & 1) + 4 * (q & 1);
      f4 v = {a[i], a[i + 1], a[i + 2], a[i + 3]};
      if (p == w) own[q] = v;
      else *(f4*)(lds + ((p * 3 + ((w - p - 1) & 3)) * 4 + q) * 1024 + lane * 16) = v;
    }
  }
  __syncthreads();
#pragma unroll
  for (int s = 0; s < 3; ++s)
#pragma unroll
    for (int q = 0; q < 4; ++q) own[q] += *(const f4*)(lds + ((w * 3 + s) * 4 + q) * 1024 + lane * 16);
  if (LAST) {
    const int rho = 64 * rg + 2 * r + (w & 1), n = 32 * (rho & 63) + (rho >> 6);
    u32* o = out + n * 256 + 32 * nt + 16 * (w >> 1) + 8 * h;
#pragma unroll
    for (int s = 0; s < 2; ++s) {
      u4 v;
#pragma unroll
      for (int j = 0; j < 4; ++j) v[j] = pk(lo(rv[s][j]) + own[2 * s + (j >> 1)][2 * (j & 1)], hi(rv[s][j]) + own[2 * s + (j >> 1)][2 * (j & 1) + 1]);
      *(u4*)(o + 4 * s) = v;
    }
  } else {
    float ss[2] = {0.f, 0.f};
    u4 v[2];
#pragma unroll
    for (int b = 0; b < 2; ++b)
#pragma unroll
      for (int j = 0; j < 4; ++j) {
        v[b][j] = pk(lo(rv[b][j]) + own[2 * b + (j >> 1)][2 * (j & 1)], hi(rv[b][j]) + own[2 * b + (j >> 1)][2 * (j & 1) + 1]);
        u32 m = (j == 0 && nt == 0 && w == 0 && h == 0) ? (v[b][j] & 0xffffu) : v[b][j];
        ss[b] = dot2(m, m, ss[b]);
      }
#pragma unroll
    for (int j = 0; j < 4; ++j)
      *(u2*)(out + (32 * nt + 16 * (w >> 1) + 8 * (w & 1) + 4 * (j >> 1) + 2 * h + (j & 1)) * SS + 64 * rg + 2 * r) = u2{v[0][j], v[1][j]};
    ss[0] += __shfl_xor(ss[0], 32); ss[1] += __shfl_xor(ss[1], 32);
    float* sl = (float*)(lds + 49152);
    sl[w * 64 + 2 * r + h] = h ? ss[1] : ss[0];
    __syncthreads();
    if (w == 0) ssp[nt * SS + 64 * rg + lane] = (sl[lane] + sl[64 + lane]) + (sl[128 + lane] + sl[192 + lane]);
  }
}

extern "C" void kernel_launch(void* const* d_in, const int* in_sizes, int n_in, void* d_out, int out_size, void* d_ws, size_t ws_size,
                              hipStream_t stream) {
  const u16 *x = (const u16*)d_in[0], *wr = (const u16*)d_in[1], *wi = (const u16*)d_in[2];
  const u16 *wg = (const u16*)d_in[4], *wu = (const u16*)d_in[5], *wd = (const u16*)d_in[6];
  char* ws = (char*)d_ws;
  const size_t MB = 1 << 20;
  u32 *xh0 = (u32*)ws, *xh1 = (u32*)(ws + 2 * MB), *xh2 = (u32*)(ws + 4 * MB), *xh3 = (u32*)(ws + 6 * MB);
  u32 *filt = (u32*)(ws + 8 * MB), *x3 = (u32*)(ws + 10 * MB), *wgu = (u32*)(ws + 12 * MB), *wd1 = (u32*)(ws + 15 * MB);
  u32 *wd2 = (u32*)(ws + 17 * MB), *r2 = (u32*)(ws + 19 * MB), *hb = (u32*)(ws + 20 * MB);
  float *rstd = (float*)(ws + 26 * MB), *ssp = (float*)(ws + 27 * MB);
  k_prep<<<248, 256, 0, stream>>>(x, wr, wi, wg, wu, wd, xh0, (u32*)rstd, wgu, wd1, wd2, filt, r2);
  k_fft<1><<<256, 256, 0, stream>>>(xh0, rstd, filt, xh1);
  k_up<0><<<256, 256, 0, stream>>>(xh1, wgu, r2, hb, x3);
  k_down<0><<<256, 256, 0, stream>>>(hb, wd1, xh1, xh2, ssp);
  k_fft<8><<<256, 256, 0, stream>>>(xh2, ssp, filt, xh3);
  k_up<1><<<256, 256, 0, stream>>>(xh3, wgu, r2, hb, x3);
  k_down<1><<<256, 256, 0, stream>>>(hb, wd2, x3, (u32*)d_out, ssp);
}
```

```cpp
#include <hip/hip_runtime.h>
#include <hip/hip_bf16.h>

typedef unsigned u32;
typedef unsigned short u16;
typedef __attribute__((ext_vector_type(8))) __bf16 bf8;
typedef __attribute__((ext_vector_type(2))) __bf16 bf2;
typedef __attribute__((ext_vector_type(2))) float f2;
typedef __attribute__((ext_vector_type(4))) float f4;
typedef __attribute__((ext_vector_type(16))) float f16v;
typedef __attribute__((ext_vector_type(2))) u32 u2;
typedef __attribute__((ext_vector_type(4))) u32 u4;
typedef __attribute__((ext_vector_type(4))) short s4;
typedef u4 __attribute__((aligned(2))) u4_ua;
#define DI __device__ __forceinline__
#define LDSP __attribute__((address_space(3)))
#define GLBP __attribute__((address_space(1)))

#define STAMP(k, j)
#define STAMPW(k, j)
constexpr int SS = 2048;
constexpr float EPS = 1e-5f;

struct Part { u16 v[16384]; };
#if defined(__HIP_DEVICE_COMPILE__)
constexpr double kPi = 3.14159265358979323846;
constexpr double csin(double x) { double t = x, s = x; for (int i = 1; i < 14; ++i) { t *= -x * x / ((2 * i) * (2 * i + 1)); s += t; } return s; }
constexpr u16 tobf(double x) { float f = (float)x; u32 u = __builtin_bit_cast(u32, f); u += 0x7fffu + ((u >> 16) & 1u); return (u16)(u >> 16); }
struct CosT { u16 v[512]; };
constexpr CosT mkcos() {
  CosT c{};
  for (int i = 0; i < 512; ++i) {
    int q = (i + 128) & 511;
    int y = q <= 128 ? q : (q <= 384 ? 256 - q : q - 512);
    c.v[i] = y == 0 ? (u16)0 : tobf(csin(2 * kPi * y / 512) / 16.0);
  }
  return c;
}
constexpr CosT kCos = mkcos();
constexpr u16 kDC = tobf(0.04419417382415922);
constexpr Part mkR1(int st) {
  Part p{};
  for (int e = 0; e < 16384; ++e) {
    int d = ((e >> 9) << 4) + ((e >> 5) & 8) + (e & 7), s = 32 * st + ((e >> 3) & 31);
    p.v[e] = s >= 2 ? kCos.v[((s >> 1) * d + ((s & 1) << 7)) & 511] : (u16)(kDC | ((s == 0 && (d & 1)) ? 0x8000 : 0));
  }
  return p;
}
template <int I> struct PR { static constexpr Part v = mkR1(I); };
struct Tab { Part p[16]; };
__device__ const __attribute__((aligned(1024))) Tab R1T = {{PR<0>::v, PR<1>::v, PR<2>::v, PR<3>::v, PR<4>::v, PR<5>::v, PR<6>::v, PR<7>::v,
  PR<8>::v, PR<9>::v, PR<10>::v, PR<11>::v, PR<12>::v, PR<13>::v, PR<14>::v, PR<15>::v}};
#else
struct Tab { u16 p[8]; };
__device__ const Tab R1T = {};
#endif

DI f16v mf32(u4 a, u4 b, f16v c) { return __builtin_amdgcn_mfma_f32_32x32x16_bf16(__builtin_bit_cast(bf8, a), __builtin_bit_cast(bf8, b), c, 0, 0, 0); }
DI f4 mf16(u4 a, u4 b, f4 c) { return __builtin_amdgcn_mfma_f32_16x16x32_bf16(__builtin_bit_cast(bf8, a), __builtin_bit_cast(bf8, b), c, 0, 0, 0); }
DI u32 pk(float a, float b) { f2 v = {a, b}; return __builtin_bit_cast(u32, __builtin_convertvector(v, bf2)); }
DI float lo(u32 u) { return __builtin_bit_cast(float, u << 16); }
DI float hi(u32 u) { return __builtin_bit_cast(float, u & 0xffff0000u); }
DI float dot2(u32 a, u32 b, float c) { return __builtin_amdgcn_fdot2_f32_bf16(__builtin_bit_cast(bf2, a), __builtin_bit_cast(bf2, b), c, false); }
DI void glds(const void* g, char* l) { __builtin_amdgcn_global_load_lds((const GLBP void*)g, (LDSP void*)l, 16, 0, 0); }
DI u4 lds128(const char* p) { return *(const u4*)p; }
DI u4 pack8(const f16v& a, int s) { u4 o = {pk(a[8 * s], a[8 * s + 1]), pk(a[8 * s + 2], a[8 * s + 3]), pk(a[8 * s + 4], a[8 * s + 5]), pk(a[8 * s + 6], a[8 * s + 7])}; return o; }
template <int N> DI void waitv() { asm volatile("s_waitcnt vmcnt(%0)" ::"n"(N) : "memory"); }
DI void bar() { asm volatile("s_waitcnt lgkmcnt(0)" ::: "memory"); __builtin_amdgcn_s_barrier(); asm volatile("" ::: "memory"); }
DI int dperm(int m) { return (m & 3) + 4 * (m >> 3) + 16 * ((m >> 2) & 1); }
DI int fperm(int m) { int i = (m & 3) + 4 * (m >> 3); return 16 * (i >> 3) + 8 * ((m >> 2) & 1) + (i & 7); }
#define SB() __builtin_amdgcn_sched_barrier(0)
#define SGB(m, n) __builtin_amdgcn_sched_group_barrier(m, n, 0)
template <int NM, int ND, int NV> DI void mix() { if constexpr (NM > 0) { SGB(0x008, 1); SGB(0x100, ND); SGB(0x002, NV); mix<NM - 1, ND, NV>(); } }
template <int I> struct IC { static constexpr int v = I; };
template <int N, class F> DI void unroll(F&& f) { if constexpr (N > 0) { unroll<N - 1>(f); f(IC<N - 1>{}); } }

template <int JOB>
DI void prep_gemm(const u16* __restrict__ q0, const u16* __restrict__ q1, int qt, int nt, char* lds,
                  u32* __restrict__ o0, u32* __restrict__ o1) {
  const int lane = threadIdx.x & 63, w = __builtin_amdgcn_readfirstlane(threadIdx.x >> 6);
  const int wp = w >> 1, wq = w & 1, r = lane & 31, h = lane >> 5;
  const char* pS = (const char*)&R1T + (size_t)(4 * nt + w) * 32768 + lane * 16;
  const char* qS[4];
#pragma unroll
  for (int i = 0; i < 4; ++i) {
    if (JOB == 1) {
      int d = 16 * w + 4 * i + (lane >> 4), pos = lane & 15;
      int f = 64 * qt + 16 * (((pos >> 1) & 3) ^ (d & 3)) + 8 * (pos & 1);
      f = f > 1368 ? 1368 : f;
      qS[i] = (const char*)((pos >> 3) ? q1 : q0) + ((size_t)d * 1376 + f) * 2;
    } else {
      int u = 32 * w + 8 * i + (lane >> 3), row;
      if (JOB == 0) { int rho = 128 * qt + (u & 64) + 2 * (u & 31) + ((u >> 5) & 1); row = 32 * (rho & 63) + (rho >> 6); }
      else { row = 128 * qt + (u & 96) + fperm(u & 31); row = row > 1375 ? 1375 : row; }
      qS[i] = (const char*)q0 + (size_t)row * 1024 + (((lane & 7) ^ ((u >> 1) & 7)) << 4);
    }
  }
  constexpr int QSTEP = JOB == 1 ? 64 * 1376 * 2 : 128;
  auto issue = [&](int c) {
    char* sl = lds + (c & 3) * 32768;
#pragma unroll
    for (int i = 0; i < 4; ++i) glds(pS + (4 * c + i) * 1024, sl + (4 * w + i) * 1024);
#pragma unroll
    for (int i = 0; i < 4; ++i) glds(qS[i] + (size_t)c * QSTEP, sl + 16384 + (4 * w + i) * 1024);
  };
  f16v acc[2][2] = {}, acc2[2][2] = {};
  float s1 = 0.f, s2 = 0.f;
  const int pOff = wp * 8192 + lane * 16;
  int qOff;
  if (JOB == 1) {
    int g = lane >> 4, qr = (lane >> 2) & 3, run = 4 * (g & 1) + (lane & 3), q = run >> 1;
    qOff = 16384 + (8 * (g >> 1) + qr) * 256 + (q & 1) * 128 + (8 * (run & 1) + 4 * (q >> 1)) * 2;
  } else qOff = 16384 + (64 * wq + r) * 128;
  struct Fr { u4 P[2], Q[2]; };
  auto rd = [&](const char* sl, int ks) {
    Fr f;
#pragma unroll
    for (int t = 0; t < 2; ++t) f.P[t] = lds128(sl + pOff + t * 4096 + ks * 1024);
#pragma unroll
    for (int b = 0; b < 2; ++b) {
      if (JOB == 1) {
        const char* a = sl + qOff + ks * 4096 + (((2 * wq + b) ^ ((lane >> 2) & 3)) << 5);
        u2 x = __builtin_bit_cast(u2, __builtin_amdgcn_ds_read_tr16_b64_v4i16((LDSP s4*)a));
        u2 y = __builtin_bit_cast(u2, __builtin_amdgcn_ds_read_tr16_b64_v4i16((LDSP s4*)(a + 1024)));
        f.Q[b] = u4{x[0], x[1], y[0], y[1]};
      } else f.Q[b] = lds128(sl + qOff + b * 4096 + (((2 * ks + h) ^ ((r >> 1) & 7)) << 4));
    }
    return f;
  };
  u4 Id[2];
  if (JOB == 2) {
#pragma unroll
    for (int k = 0; k < 2; ++k) {
      int e = dperm(r) - 16 * k - 8 * h;
#pragma unroll
      for (int j = 0; j < 4; ++j) Id[k][j] = (e == 2 * j ? 0x3f80u : 0u) | (e == 2 * j + 1 ? 0x3f800000u : 0u);
    }
  }
  auto mm = [&](const Fr& f, int c, int ks) {
#pragma unroll
    for (int t = 0; t < 2; ++t)
#pragma unroll
      for (int b = 0; b < 2; ++b) acc[t][b] = JOB == 2 ? mf32(f.Q[b], f.P[t], acc[t][b]) : mf32(f.P[t], f.Q[b], acc[t][b]);
    if (JOB == 0) {
      u4 v = wp ? f.Q[1] : f.Q[0];
#pragma unroll
      for (int j = 0; j < 4; ++j) { s1 = dot2(v[j], 0x3f803f80u, s1); s2 = dot2(v[j], v[j], s2); }
    }
    mix<4, 2, 3>();
    SB();
    if (JOB == 2) {
      if (c == 2 * nt + wp) {
#pragma unroll
        for (int b = 0; b < 2; ++b) acc2[ks >> 1][b] = mf32(f.Q[b], Id[ks & 1], acc2[ks >> 1][b]);
      }
    }
  };
  issue(0); issue(1); issue(2); issue(3);
  waitv<24>(); bar();
  Fr f0 = rd(lds, 0), f1;
#pragma unroll 1
  for (int c = 0; c < 8; ++c) {
    const char* sl = lds + (c & 3) * 32768;
    SB();
    f1 = rd(sl, 1); mm(f0, c, 0);
    f0 = rd(sl, 2); mm(f1, c, 1);
    f1 = rd(sl, 3); mm(f0, c, 2);
    if (c < 5) waitv<16>(); else if (c == 5) waitv<8>(); else waitv<0>();
    bar();
    if (c < 4) issue(c + 4);
    SB();
    f0 = rd(lds + ((c + 1) & 3) * 32768, 0); mm(f1, c, 3);
  }
  STAMP(0, 3);
  if (JOB == 0) {
#pragma unroll
    for (int t = 0; t < 2; ++t)
#pragma unroll
      for (int i = 0; i < 16; i += 2) {
        int col = 64 * nt + 32 * wp + 16 * t + (((i & 3) + 8 * (i >> 2) + 4 * h) >> 1);
        u2 v = {pk(acc[t][0][i], acc[t][0][i + 1]), pk(acc[t][1][i], acc[t][1][i + 1])};
        *(u2*)(o0 + col * SS + 128 * qt + 64 * wq + 2 * r) = v;
      }
    s1 += __shfl_xor(s1, 32); s2 += __shfl_xor(s2, 32);
    float m = s1 * (1.f / 512), var = fmaxf(s2 * (1.f / 512) - m * m, 0.f);
    if (nt == 0 && h == 0) ((float*)o1)[128 * qt + 64 * wq + 2 * r + wp] = __builtin_amdgcn_rsqf(var + EPS) * (1.f / 2048);
  } else if (JOB == 1) {
#pragma unroll
    for (int b = 0; b < 2; ++b) {
      int tile = 4 * qt + 2 * wq + b;
      if (tile < 86)
#pragma unroll
        for (int t = 0; t < 2; ++t)
#pragma unroll
          for (int s = 0; s < 2; ++s) {
            int ks = 8 * nt + 4 * wp + 2 * t + s;
            u4 v = pack8(acc[t][b], s);
            if (ks == 0 && h == 0) v[0] &= 0xffffu;
            *(u4*)(o0 + (tile * 32 + ks) * 256 + lane * 4) = v;
          }
    }
  } else {
#pragma unroll
    for (int b = 0; b < 2; ++b)
#pragma unroll
      for (int s = 0; s < 2; ++s) {
        int ks = 8 * qt + 4 * wq + 2 * b + s;
        if (ks < 86)
#pragma unroll
          for (int t = 0; t < 2; ++t) {
            int o = ((4 * nt + 2 * wp + t) * 86 + ks) * 256 + lane * 4;
            *(u4*)(o0 + o) = pack8(acc[t][b], s);
            *(u4*)(o1 + o) = pack8(acc2[t][b], s);
          }
      }
  }
}

__global__ __launch_bounds__(256, 2) void k_prep(const u16* __restrict__ x, const u16* __restrict__ wr, const u16* __restrict__ wi,
    const u16* __restrict__ wg, const u16* __restrict__ wu, const u16* __restrict__ wd,
    u32* __restrict__ xh, u32* __restrict__ rstd, u32* __restrict__ wgu, u32* __restrict__ wd1, u32* __restrict__ wd2,
    u32* __restrict__ filt, u32* __restrict__ r2) {
  __shared__ __attribute__((aligned(1024))) char lds[131072];
  const int b = blockIdx.x, nt = (b >> 3) & 3, q = 8 * (b >> 5) + (b & 7);
  const int lane = threadIdx.x & 63, w = threadIdx.x >> 6;
  STAMP(0, 0);
  if (q < 16) prep_gemm<0>(x, x, q, nt, lds, xh, rstd);
  else if (q < 38) prep_gemm<1>(wg, wu, q - 16, nt, lds, wgu, wgu);
  else if (q < 49) prep_gemm<2>(wd, wd, q - 38, nt, lds, wd1, wd2);
  else if (q < 57) {
#pragma unroll
    for (int it = 0; it < 2; ++it) {
      int unit = (((q - 49) * 4 + nt) * 4 + w) * 2 + it, cb = unit & 7, wmg = unit >> 3;
      int c0 = 32 * cb + 8 * (lane >> 4), k0 = 16 * (wmg >> 3) + (lane & 15) + 64 * (16 * ((wmg >> 2) & 1) + 4 * (wmg & 3));
      u4 a[4], bb[4];
#pragma unroll
      for (int i = 0; i < 4; ++i) {
        a[i] = *(const u4_ua*)(wr + (k0 + 64 * i) * 257 + 1 + c0);
        bb[i] = *(const u4_ua*)(wi + (k0 + 64 * i) * 257 + 1 + c0);
      }
#pragma unroll
      for (int e = 0; e < 8; ++e) {
        u4 v;
#pragma unroll
        for (int i = 0; i < 4; ++i) v[i] = (e & 1) ? ((a[i][e >> 1] >> 16) | (bb[i][e >> 1] & 0xffff0000u)) : ((a[i][e >> 1] & 0xffffu) | (bb[i][e >> 1] << 16));
        int col = (c0 + e + 1) & 255;
        *(u4*)(filt + col * SS + (wmg >> 2) * 256 + ((wmg & 3) * 16 + (lane & 15)) * 4) = v;
      }
    }
  } else if (q < 61) {
    int dt = (q - 57) * 4 + nt, d = 32 * dt + dperm(lane & 31), h = lane >> 5;
#pragma unroll
    for (int it = 0; it < 8; ++it) {
      int ks = 8 * w + it;
      u4 v;
#pragma unroll
      for (int jj = 0; jj < 4; ++jj) {
        int c = 8 * ks + 4 * (jj >> 1) + 2 * h + (jj & 1);
        float a = (float)((c * d) & 511) * (1.f / 512);
        float re = __builtin_amdgcn_cosf(a) * 0.0625f, im = __builtin_amdgcn_sinf(a) * -0.0625f;
        if (c == 0) { im = 0.04419417382f; re = (d & 1) ? -im : im; }
        v[jj] = pk(re, im);
      }
      *(u4*)(r2 + (dt * 32 + ks) * 256 + lane * 4) = v;
    }
  }
  STAMPW(0, q < 16 ? 4 : q < 38 ? 5 : q < 49 ? 6 : 7);
}

DI u4 rot(u4 v) { u4 o; for (int j = 0; j < 4; ++j) o[j] = __builtin_amdgcn_alignbit(v[j], v[j], 16) ^ 0x8000u; return o; }
template <int NP>
__global__ __launch_bounds__(256, 2) void k_fft(const u32* __restrict__ xin, const float* __restrict__ st, const u32* __restrict__ filt,
                                                u32* __restrict__ xout) {
  __shared__ __attribute__((aligned(256))) char lds[16384];
  const int col = blockIdx.x, tid = threadIdx.x, lane = tid & 63, w = tid >> 6, l15 = lane & 15, g = lane >> 4;
  constexpr int KI = NP == 1 ? 1 : 4;
  STAMP(KI, 0);
  const u32* xc = xin + col * SS;
  u4 xd[2], xr[2], fw[2];
  f4 sv[NP][2];
#pragma unroll
  for (int i = 0; i < 2; ++i) xd[i] = *(const u4*)(xc + 1024 * i + 4 * tid);
#pragma unroll
  for (int p = 0; p < NP; ++p)
#pragma unroll
    for (int i = 0; i < 2; ++i) sv[p][i] = *(const f4*)(st + p * SS + 1024 * i + 4 * tid);
#pragma unroll
  for (int i = 0; i < 2; ++i) {
    fw[i] = *(const u4*)(filt + col * SS + (2 * w + i) * 256 + lane * 4);
    xr[i] = *(const u4*)(xc + 64 * (16 * i + l15) + 16 * w + 4 * g);
  }
  SB();
  const int k1 = 16 * w + l15;
  const float fk1 = (float)k1, fl = (float)l15, fa = (float)(4 * g);
  u4 F64r[4], F64i[4], F32r[2][2], F32i[2][2];
  float t1c[2][4], t1s[2][4], t2c[2][4], t2s[2][4];
#pragma unroll
  for (int j = 0; j < 4; ++j) {
    float a = (fa + j) * fk1 * (1.f / 64), b = (fa + j) * fl * (1.f / 32);
    F64r[0][j] = pk(__builtin_amdgcn_cosf(a), __builtin_amdgcn_sinf(a));
    F32r[0][0][j] = pk(__builtin_amdgcn_cosf(b), __builtin_amdgcn_sinf(b));
  }
  F64i[0] = rot(F64r[0]); F32i[0][0] = rot(F32r[0][0]);
  const u32 NEG = 0x80008000u, so = (lane & 1) ? NEG : 0u, sr = (lane & 2) ? NEG : 0u, si = ((lane + 1) & 2) ? NEG : 0u;
#pragma unroll
  for (int j = 0; j < 4; ++j) {
    F64r[1][j] = ((lane & 1) ? F64i[0][j] : F64r[0][j]) ^ sr;
    F64i[1][j] = ((lane & 1) ? F64r[0][j] : F64i[0][j]) ^ si;
    F64r[2][j] = F64r[0][j] ^ so; F64i[2][j] = F64i[0][j] ^ so;
    F64r[3][j] = F64r[1][j] ^ so; F64i[3][j] = F64i[1][j] ^ so;
    const u32 sj = (j & 1) ? NEG : 0u;
    F32r[0][1][j] = F32r[0][0][j] ^ so; F32i[0][1][j] = F32i[0][0][j] ^ so;
    F32r[1][0][j] = F32r[0][0][j] ^ sj; F32i[1][0][j] = F32i[0][0][j] ^ sj;
    F32r[1][1][j] = F32r[0][1][j] ^ sj; F32i[1][1][j] = F32i[0][1][j] ^ sj;
  }
#pragma unroll
  for (int t = 0; t < 2; ++t)
#pragma unroll
    for (int j = 0; j < 4; ++j) {
      float a = (fa + (16 * t + j)) * (fk1 * (1.f / 2048)), b = ((fa + (float)(16 * w)) + j) * ((fl + 16 * t) * (1.f / 2048));
      t1c[t][j] = __builtin_amdgcn_cosf(a); t1s[t][j] = __builtin_amdgcn_sinf(a);
      t2c[t][j] = __builtin_amdgcn_cosf(b); t2s[t][j] = __builtin_amdgcn_sinf(b);
    }
  SB();
  STAMP(KI, 1);
  const u32 msk = col == 0 ? 0xffffu : 0xffffffffu;
#pragma unroll
  for (int i = 0; i < 2; ++i) {
    f4 rs = sv[0][i];
    if (NP > 1) {
#pragma unroll
      for (int p = 1; p < NP; ++p) rs += sv[p][i];
#pragma unroll
      for (int j = 0; j < 4; ++j) rs[j] = __builtin_amdgcn_rsqf(rs[j] * (1.f / 512) + EPS) * (1.f / 2048);
    }
    u4 z;
#pragma unroll
    for (int j = 0; j < 4; ++j) z[j] = pk(lo(xd[i][j]) * rs[j], hi(xd[i][j]) * rs[j]) & msk;
    *(u4*)(lds + (16 * i + (tid >> 4)) * 256 + ((l15 ^ (tid >> 4)) << 4)) = z;
  }
  __syncthreads();
  STAMP(KI, 2);
  u4 z[2][4], T[2], Y[2];
  f4 ar[2], ai[2];
  const f4 Z4 = {0, 0, 0, 0};
#pragma unroll
  for (int s = 0; s < 4; ++s)
#pragma unroll
    for (int t = 0; t < 2; ++t) z[t][s] = lds128(lds + (16 * t + l15) * 256 + (((4 * s + g) ^ l15) << 4));
  SB();
#pragma unroll
  for (int t = 0; t < 2; ++t)
#pragma unroll
    for (int s = 0; s < 4; ++s) { ar[t] = mf16(z[t][s], F64r[s], s ? ar[t] : Z4); ai[t] = mf16(z[t][s], F64i[s], s ? ai[t] : Z4); }
#pragma unroll
  for (int t = 0; t < 2; ++t)
#pragma unroll
    for (int j = 0; j < 4; ++j) T[t][j] = pk(ar[t][j] * t1c[t][j] + ai[t][j] * t1s[t][j], ai[t][j] * t1c[t][j] - ar[t][j] * t1s[t][j]);
#pragma unroll
  for (int m = 0; m < 2; ++m)
#pragma unroll
    for (int t = 0; t < 2; ++t) { ar[m] = mf16(F32r[m][t], T[t], t ? ar[m] : Z4); ai[m] = mf16(F32i[m][t], T[t], t ? ai[m] : Z4); }
#pragma unroll
  for (int m = 0; m < 2; ++m)
#pragma unroll
    for (int j = 0; j < 4; ++j) { float a = lo(fw[m][j]), b = hi(fw[m][j]); Y[m][j] = pk(ar[m][j] * a - ai[m][j] * b, -(ar[m][j] * b) - ai[m][j] * a); }
#pragma unroll
  for (int t = 0; t < 2; ++t)
#pragma unroll
    for (int m = 0; m < 2; ++m) { ar[t] = mf16(Y[m], F32r[t][m], m ? ar[t] : Z4); ai[t] = mf16(Y[m], F32i[t][m], m ? ai[t] : Z4); }
#pragma unroll
  for (int t = 0; t < 2; ++t) {
    u4 v;
#pragma unroll
    for (int j = 0; j < 4; ++j) v[j] = pk(ar[t][j] * t2c[t][j] + ai[t][j] * t2s[t][j], ai[t][j] * t2c[t][j] - ar[t][j] * t2s[t][j]);
    *(u4*)(lds + 8192 + (16 * t + l15) * 256 + (((4 * w + g) ^ l15) << 4)) = v;
  }
  __syncthreads();
#pragma unroll
  for (int s = 0; s < 4; ++s)
#pragma unroll
    for (int t = 0; t < 2; ++t) z[t][s] = lds128(lds + 8192 + (16 * t + l15) * 256 + (((4 * s + g) ^ l15) << 4));
  SB();
#pragma unroll
  for (int t = 0; t < 2; ++t)
#pragma unroll
    for (int s = 0; s < 4; ++s) { ar[t] = mf16(F64r[s], z[t][s], s ? ar[t] : Z4); ai[t] = mf16(F64i[s], z[t][s], s ? ai[t] : Z4); }
  const float sg = col == 0 ? 0.f : -1.f;
#pragma unroll
  for (int t = 0; t < 2; ++t) {
    u4 o;
#pragma unroll
    for (int j = 0; j < 4; ++j) o[j] = pk(lo(xr[t][j]) + ar[t][j], hi(xr[t][j]) + (col == 0 ? 0.f : sg * ai[t][j]));
    *(u4*)(xout + col * SS + 64 * (16 * t + l15) + 16 * w + 4 * g) = o;
  }
  STAMP(KI, 3); STAMPW(KI, 4);
}

template <int LAST>
__global__ __launch_bounds__(256, 2) void k_up(const u32* __restrict__ xh, const u32* __restrict__ wgu, const u32* __restrict__ r2,
                                               u32* __restrict__ hb, u32* __restrict__ x3, float* __restrict__ rs2) {
  constexpr int NT = 6 + LAST, CH = NT * 4096 + 16384, NS = LAST ? 3 : 4, NI = NT + 4;
  __shared__ __attribute__((aligned(1024))) char lds[NS * CH];
  const int xc = blockIdx.x & 7, jb = blockIdx.x >> 3, mt = 8 * (xc & 1) + (jb & 7), ng = 4 * (xc >> 1) + (jb >> 3);
  const int t0 = (ng * 43) >> 3, t1 = ((ng + 1) * 43) >> 3;
  const int lane = threadIdx.x & 63, w = __builtin_amdgcn_readfirstlane(threadIdx.x >> 6);
  const int wm = w >> 1, wn = w & 1, r = lane & 31, h = lane >> 5;
  constexpr int KI = LAST ? 5 : 2;
  STAMP(KI, 0);
  const u32* pS = wgu + t0 * 8192 + lane * 4;
  const u32* rS = r2 + (ng * 32 + w) * 256 + lane * 4;
  const u32* qS = xh + (8 * w + h) * SS + 128 * mt + 4 * r;
  auto issue = [&](int c, char* sl) {
#pragma unroll
    for (int i = 0; i < 4; ++i) glds(qS + (32 * c + 2 * i) * SS, sl + NT * 4096 + (4 * w + i) * 1024);
#pragma unroll
    for (int i = 0; i < 6; ++i) { int e = 6 * w + i; glds(pS + (e >> 2) * 8192 + (4 * c + (e & 3)) * 256, sl + e * 1024); }
    if (LAST) glds(rS + c * 1024, sl + (24 + w) * 1024);
  };
  f16v acc[3][2] = {}, accx = {};
  float ss = 0.f;
  u32 dcm = h ? 0xffffffffu : 0xffffu;
  const int pOff = wn * 12288 + lane * 16, qOff = NT * 4096 + 2 * h * 512 + (64 * wm + r) * 4;
  struct Fr { u4 P[3], Q[2], X; };
  auto rd = [&](const char* sl, int ks) {
    Fr f;
#pragma unroll
    for (int b = 0; b < 2; ++b)
#pragma unroll
      for (int j = 0; j < 4; ++j) f.Q[b][j] = *(const u32*)(sl + qOff + b * 128 + (8 * ks + 4 * (j >> 1) + (j & 1)) * 512);
#pragma unroll
    for (int t = 0; t < 3; ++t) f.P[t] = lds128(sl + pOff + t * 4096 + ks * 1024);
    if (LAST) f.X = lds128(sl + 24576 + ks * 1024 + lane * 16);
    return f;
  };
  auto mm = [&](const Fr& f, int ks) {
#pragma unroll
    for (int t = 0; t < 3; ++t) { acc[t][0] = mf32(f.P[t], f.Q[0], acc[t][0]); acc[t][1] = mf32(f.P[t], f.Q[1], acc[t][1]); }
    u4 v = wn ? f.Q[1] : f.Q[0];
    if (LAST) accx = mf32(f.X, v, accx);
    if (ks == 0) { v[0] &= dcm; dcm = 0xffffffffu; }
#pragma unroll
    for (int j = 0; j < 4; ++j) ss = dot2(v[j], v[j], ss);
    mix<6 + LAST, 2, 2>();
    SB();
  };
#pragma unroll
  for (int c = 0; c < NS; ++c) issue(c, lds + c * CH);
  waitv<(NS - 1) * NI>(); bar();
  STAMP(KI, 1);
  Fr f0 = rd(lds, 0), f1;
  int so = 0;
#pragma unroll 1
  for (int c = 0; c < 8; ++c) {
    char* sl = lds + so;
    so = so == (NS - 1) * CH ? 0 : so + CH;
    SB();
    f1 = rd(sl, 1); mm(f0, 0);
    f0 = rd(sl, 2); mm(f1, 1);
    f1 = rd(sl, 3); mm(f0, 2);
    if (c < 9 - NS) waitv<(NS - 2) * NI>(); else if (NS == 4 && c == 5) waitv<NI>(); else waitv<0>();
    bar();
    if (c < 8 - NS) issue(c + NS, sl);
    SB();
    f0 = rd(lds + so, 0); mm(f1, 3);
  }
  STAMP(KI, 3);
#pragma unroll
  for (int t = 0; t < 3; ++t) {
    int tile = t0 + 3 * wn + t;
    if (tile < t1)
#pragma unroll
      for (int b = 0; b < 2; ++b) {
        const f16v& a = acc[t][b];
        u4 o;
#pragma unroll
        for (int j = 0; j < 4; ++j) { int i = 8 * (j >> 1) + 2 * (j & 1); o[j] = pk(fmaxf(a[i], 0.f) * a[i + 4], fmaxf(a[i + 1], 0.f) * a[i + 5]); }
        *(u4*)(hb + (((2 * mt + wm) * 2 + b) * 86 + tile) * 256 + lane * 4) = o;
      }
  }
  const int rho = 128 * mt + 64 * wm + 32 * wn + r;
  if (LAST) {
    u32* o = x3 + rho * 256 + 16 * ng + 8 * h;
    *(u4*)o = pack8(accx, 0); *(u4*)(o + 4) = pack8(accx, 1);
  }
  if (ng == 0) {
    ss += __shfl_xor(ss, 32);
    if (h == 0) rs2[rho] = 1.f / (ss * (1.f / 512) + EPS);
  }
  STAMP(KI, 4); STAMPW(KI, 5);
}

template <int LAST>
__global__ __launch_bounds__(256, 2) void k_down(const u32* __restrict__ hb, const u32* __restrict__ wd, const float* __restrict__ rs2,
                                                 const u32* __restrict__ res, u32* __restrict__ out, float* __restrict__ ssp) {
  constexpr int NS = 9;
  __shared__ __attribute__((aligned(1024))) char lds[4 * NS * 4096];
  const int xc = blockIdx.x & 7, jb = blockIdx.x >> 3, rg = 8 * (xc & 3) + (jb & 7), nt = 4 * (xc >> 2) + (jb >> 3);
  const int lane = threadIdx.x & 63, w = __builtin_amdgcn_readfirstlane(threadIdx.x >> 6), r = lane & 31, h = lane >> 5;
  constexpr int KI = LAST ? 6 : 3;
  STAMP(KI, 0);
  u4 rv[2];
  float rs[2];
  const int c0 = 32 * nt + 16 * (w >> 1) + 8 * (w & 1) + 2 * h;
  if (LAST) {
    const u32* p = res + (64 * rg + 32 * (w & 1) + r) * 256 + 32 * nt + 16 * (w >> 1) + 8 * h;
    rv[0] = *(const u4*)p; rv[1] = *(const u4*)(p + 4);
    rs[0] = rs[1] = rs2[64 * rg + 32 * (w & 1) + r];
  } else {
#pragma unroll
    for (int b = 0; b < 2; ++b) {
#pragma unroll
      for (int j = 0; j < 4; ++j) rv[b][j] = res[(c0 + 4 * (j >> 1) + (j & 1)) * SS + 64 * rg + 32 * b + r];
      rs[b] = rs2[64 * rg + 32 * b + r];
    }
  }
  SB();
  const u32* pA = wd + (2 * nt * 86 + w) * 256 + lane * 4;
  const u32* pB = hb + (2 * rg * 86 + w) * 256 + lane * 4;
  char* ring = lds + w * (NS * 4096);
  auto issue = [&](int i, char* sl) {
    glds(pA + i * 1024, sl); glds(pB + i * 1024, sl + 1024);
    glds(pA + i * 1024 + 86 * 256, sl + 2048); glds(pB + i * 1024 + 86 * 256, sl + 3072);
  };
  struct Fr { u4 A[2], B[2]; };
  auto rd = [&](const char* sl) {
    Fr f;
    f.A[0] = lds128(sl + lane * 16); f.B[0] = lds128(sl + 1024 + lane * 16);
    f.A[1] = lds128(sl + 2048 + lane * 16); f.B[1] = lds128(sl + 3072 + lane * 16);
    return f;
  };
  f16v acc[2][2] = {};
  auto mm = [&](const Fr& f) {
#pragma unroll
    for (int t = 0; t < 2; ++t)
#pragma unroll
      for (int b = 0; b < 2; ++b) acc[t][b] = mf32(f.A[t], f.B[b], acc[t][b]);
  };
#pragma unroll
  for (int i = 0; i < NS; ++i) issue(i, ring + i * 4096);
  const int n = w < 2 ? 22 : 21;
  int so = 0;
#pragma unroll 1
  for (int i = NS; i < n; ++i) {
    waitv<4 * (NS - 1)>();
    Fr f = rd(ring + so);
    asm volatile("s_waitcnt lgkmcnt(0)" : "+v"(f.A[0]), "+v"(f.A[1]), "+v"(f.B[0]), "+v"(f.B[1])::"memory");
    issue(i, ring + so);
    SB();
    mm(f);
    so = so == (NS - 1) * 4096 ? 0 : so + 4096;
  }
  unroll<NS>([&](auto I) {
    waitv<4 * (NS - 1 - I.v)>();
    Fr f = rd(ring + so);
    mm(f);
    so = so == (NS - 1) * 4096 ? 0 : so + 4096;
  });
  STAMP(KI, 2);
  __syncthreads();
  f4 own[4];
#pragma unroll
  for (int p = 0; p < 4; ++p) {
    char* dst = lds + (p * 3 + ((w - p - 1) & 3)) * 4096 + lane * 16;
  #pragma unroll
    for (int q = 0; q < 4; ++q) {
      const f16v& a = LAST ? acc[p >> 1][p & 1] : acc[p >> 1][q >> 1];
      const int i = LAST ? 4 * q : 8 * (p & 1) + 4 * (q & 1);
      f4 v = {a[i], a[i + 1], a[i + 2], a[i + 3]};
      if (p == w) own[q] = v; else *(f4*)(dst + q * 1024) = v;
    }
  }
  __syncthreads();
#pragma unroll
  for (int s = 0; s < 3; ++s)
#pragma unroll
    for (int q = 0; q < 4; ++q) own[q] += *(const f4*)(lds + ((w * 3 + s) * 4 + q) * 1024 + lane * 16);
  u4 v[2];
#pragma unroll
  for (int b = 0; b < 2; ++b)
#pragma unroll
    for (int j = 0; j < 4; ++j) v[b][j] = pk(lo(rv[b][j]) + rs[b] * own[2 * b + (j >> 1)][2 * (j & 1)], hi(rv[b][j]) + rs[b] * own[2 * b + (j >> 1)][2 * (j & 1) + 1]);
  if (LAST) {
    u32* o = out + (32 * (32 * (w & 1) + r) + rg) * 256 + 32 * nt + 16 * (w >> 1) + 8 * h;
    *(u4*)o = v[0]; *(u4*)(o + 4) = v[1];
  } else {
    float ss[2] = {0.f, 0.f};
#pragma unroll
    for (int b = 0; b < 2; ++b)
#pragma unroll
      for (int j = 0; j < 4; ++j) {
        out[(c0 + 4 * (j >> 1) + (j & 1)) * SS + 64 * rg + 32 * b + r] = v[b][j];
        u32 m = (j == 0 && c0 == 0) ? (v[b][j] & 0xffffu) : v[b][j];
        ss[b] = dot2(m, m, ss[b]);
      }
    ss[0] += __shfl_xor(ss[0], 32); ss[1] += __shfl_xor(ss[1], 32);
    float* sl = (float*)(lds + 49152);
    sl[w * 64 + lane] = h ? ss[1] : ss[0];
    __syncthreads();
    if (w == 0) ssp[nt * SS + 64 * rg + lane] = (sl[lane] + sl[64 + lane]) + (sl[128 + lane] + sl[192 + lane]);
  }
  STAMP(KI, 3); STAMPW(KI, 4);
}

extern "C" void kernel_launch(void* const* d_in, const int* in_sizes, int n_in, void* d_out, int out_size, void* d_ws, size_t ws_size,
                              hipStream_t stream) {
  const u16 *x = (const u16*)d_in[0], *wr = (const u16*)d_in[1], *wi = (const u16*)d_in[2];
  const u16 *wg = (const u16*)d_in[4], *wu = (const u16*)d_in[5], *wd = (const u16*)d_in[6];
  char* ws = (char*)d_ws;
  const size_t MB = 1 << 20;
  u32 *xh0 = (u32*)ws, *xh1 = (u32*)(ws + 2 * MB), *xh2 = (u32*)(ws + 4 * MB), *xh3 = (u32*)(ws + 6 * MB);
  u32 *filt = (u32*)(ws + 8 * MB), *x3 = (u32*)(ws + 10 * MB), *wgu = (u32*)(ws + 12 * MB), *wd1 = (u32*)(ws + 15 * MB);
  u32 *wd2 = (u32*)(ws + 17 * MB), *r2 = (u32*)(ws + 19 * MB), *hb = (u32*)(ws + 20 * MB);
  float *rstd = (float*)(ws + 26 * MB), *ssp = (float*)(ws + 27 * MB), *rs2 = (float*)(ws + 28 * MB);
  k_prep<<<256, 256, 0, stream>>>(x, wr, wi, wg, wu, wd, xh0, (u32*)rstd, wgu, wd1, wd2, filt, r2);
  k_fft<1><<<256, 256, 0, stream>>>(xh0, rstd, filt, xh1);
  k_up<0><<<256, 256, 0, stream>>>(xh1, wgu, r2, hb, x3, rs2);
  k_down<0><<<256, 256, 0, stream>>>(hb, wd1, rs2, xh1, xh2, ssp);
  k_fft<8><<<256, 256, 0, stream>>>(xh2, ssp, filt, xh3);
  k_up<1><<<256, 256, 0, stream>>>(xh3, wgu, r2, hb, x3, rs2);
  k_down<1><<<256, 256, 0, stream>>>(hb, wd2, rs2, x3, (u32*)d_out, ssp);
}
```

```cpp
#include <hip/hip_runtime.h>
#include <hip/hip_bf16.h>

typedef unsigned u32;
typedef unsigned short u16;
typedef __attribute__((ext_vector_type(8))) __bf16 bf8;
typedef __attribute__((ext_vector_type(2))) __bf16 bf2;
typedef __attribute__((ext_vector_type(2))) float f2;
typedef __attribute__((ext_vector_type(4))) float f4;
typedef __attribute__((ext_vector_type(16))) float f16v;
typedef __attribute__((ext_vector_type(2))) u32 u2;
typedef __attribute__((ext_vector_type(4))) u32 u4;
typedef __attribute__((ext_vector_type(4))) short s4;
typedef u4 __attribute__((aligned(2))) u4_ua;
#define DI __device__ __forceinline__
#define LDSP __attribute__((address_space(3)))
#define GLBP __attribute__((address_space(1)))

#define STAMP(k, j)
#define STAMPW(k, j)
constexpr int SS = 2048;
constexpr float EPS = 1e-5f;

struct Part { u16 v[16384]; };
#if defined(__HIP_DEVICE_COMPILE__)
constexpr double kPi = 3.14159265358979323846;
constexpr double csin(double x) { double t = x, s = x; for (int i = 1; i < 14; ++i) { t *= -x * x / ((2 * i) * (2 * i + 1)); s += t; } return s; }
constexpr u16 tobf(double x) { float f = (float)x; u32 u = __builtin_bit_cast(u32, f); u += 0x7fffu + ((u >> 16) & 1u); return (u16)(u >> 16); }
struct CosT { u16 v[512]; };
constexpr CosT mkcos() {
  CosT c{};
  for (int i = 0; i < 512; ++i) {
    int q = (i + 128) & 511;
    int y = q <= 128 ? q : (q <= 384 ? 256 - q : q - 512);
    c.v[i] = y == 0 ? (u16)0 : tobf(csin(2 * kPi * y / 512) / 16.0);
  }
  return c;
}
constexpr CosT kCos = mkcos();
constexpr u16 kDC = tobf(0.04419417382415922);
constexpr Part mkR1(int st) {
  Part p{};
  for (int e = 0; e < 16384; ++e) {
    int d = ((e >> 9) << 4) + ((e >> 5) & 8) + (e & 7), s = 32 * st + ((e >> 3) & 31);
    p.v[e] = s >= 2 ? kCos.v[((s >> 1) * d + ((s & 1) << 7)) & 511] : (u16)(kDC | ((s == 0 && (d & 1)) ? 0x8000 : 0));
  }
  return p;
}
template <int I> struct PR { static constexpr Part v = mkR1(I); };
struct Tab { Part p[16]; };
__device__ const __attribute__((aligned(1024))) Tab R1T = {{PR<0>::v, PR<1>::v, PR<2>::v, PR<3>::v, PR<4>::v, PR<5>::v, PR<6>::v, PR<7>::v,
  PR<8>::v, PR<9>::v, PR<10>::v, PR<11>::v, PR<12>::v, PR<13>::v, PR<14>::v, PR<15>::v}};
#else
struct Tab { u16 p[8]; };
__device__ const Tab R1T = {};
#endif

DI f16v mf32(u4 a, u4 b, f16v c) { return __builtin_amdgcn_mfma_f32_32x32x16_bf16(__builtin_bit_cast(bf8, a), __builtin_bit_cast(bf8, b), c, 0, 0, 0); }
DI f4 mf16(u4 a, u4 b, f4 c) { return __builtin_amdgcn_mfma_f32_16x16x32_bf16(__builtin_bit_cast(bf8, a), __builtin_bit_cast(bf8, b), c, 0, 0, 0); }
DI u32 pk(float a, float b) { f2 v = {a, b}; return __builtin_bit_cast(u32, __builtin_convertvector(v, bf2)); }
DI float lo(u32 u) { return __builtin_bit_cast(float, u << 16); }
DI float hi(u32 u) { return __builtin_bit_cast(float, u & 0xffff0000u); }
DI float dot2(u32 a, u32 b, float c) { return __builtin_amdgcn_fdot2_f32_bf16(__builtin_bit_cast(bf2, a), __builtin_bit_cast(bf2, b), c, false); }
DI void glds(const void* g, char* l) { __builtin_amdgcn_global_load_lds((const GLBP void*)g, (LDSP void*)l, 16, 0, 0); }
typedef __amdgpu_buffer_rsrc_t rsrc;
DI rsrc mkr(const void* p) { return __builtin_amdgcn_make_buffer_rsrc((void*)p, 0, 0x7fffffff, 0x00020000); }
template <int O = 0> DI void blds(rsrc r, int vo, int so, char* l) { __builtin_amdgcn_raw_ptr_buffer_load_lds(r, (LDSP void*)l, 16, vo, so, O, 0); }
DI u4 ldtr(const char* a, int d) {
  u2 x = __builtin_bit_cast(u2, __builtin_amdgcn_ds_read_tr16_b64_v4i16((LDSP s4*)a));
  u2 y = __builtin_bit_cast(u2, __builtin_amdgcn_ds_read_tr16_b64_v4i16((LDSP s4*)(a + d)));
  return u4{x[0], x[1], y[0], y[1]};
}
DI u4 lds128(const char* p) { return *(const u4*)p; }
DI u4 pack8(const f16v& a, int s) { u4 o = {pk(a[8 * s], a[8 * s + 1]), pk(a[8 * s + 2], a[8 * s + 3]), pk(a[8 * s + 4], a[8 * s + 5]), pk(a[8 * s + 6], a[8 * s + 7])}; return o; }
template <int N> DI void waitv() { asm volatile("s_waitcnt vmcnt(%0)" ::"n"(N) : "memory"); }
DI void bar() { asm volatile("s_waitcnt lgkmcnt(0)" ::: "memory"); __builtin_amdgcn_s_barrier(); asm volatile("" ::: "memory"); }
DI int dperm(int m) { return (m & 3) + 4 * (m >> 3) + 16 * ((m >> 2) & 1); }
DI int fperm(int m) { int i = (m & 3) + 4 * (m >> 3); return 16 * (i >> 3) + 8 * ((m >> 2) & 1) + (i & 7); }
#define SB() __builtin_amdgcn_sched_barrier(0)
#define SGB(m, n) __builtin_amdgcn_sched_group_barrier(m, n, 0)
template <int NM, int ND, int NV> DI void mix() { if constexpr (NM > 0) { SGB(0x008, 1); SGB(0x100, ND); SGB(0x002, NV); mix<NM - 1, ND, NV>(); } }
template <int I> struct IC { static constexpr int v = I; };
template <int N, class F> DI void unroll(F&& f) { if constexpr (N > 0) { unroll<N - 1>(f); f(IC<N - 1>{}); } }

template <int JOB>
DI void prep_gemm(const u16* __restrict__ q0, const u16* __restrict__ q1, int qt, int nt, char* lds,
                  u32* __restrict__ o0, u32* __restrict__ o1) {
  const int lane = threadIdx.x & 63, w8 = __builtin_amdgcn_readfirstlane(threadIdx.x >> 6), w = w8 & 3;
  const int wp = w >> 1, wq = w & 1, r = lane & 31, h = lane >> 5;
  if (w8 >= 4) {
    const rsrc rp = mkr(&R1T), ra = mkr(q0), rb = mkr(q1);
    int qv[4];
#pragma unroll
    for (int i = 0; i < 4; ++i) {
      if (JOB == 1) {
        int d = 16 * w + 8 * (i >> 1) + (lane >> 3), pos = lane & 7;
        int f = 64 * qt + 16 * ((pos >> 1) ^ (d & 3) ^ (i & 1)) + 8 * (pos & 1);
        qv[i] = (d * 1376 + (f > 1368 ? 1368 : f)) * 2;
      } else {
        int u = 32 * w + 8 * i + (lane >> 3), row;
        if (JOB == 0) { int rho = 128 * qt + (u & 64) + 2 * (u & 31) + ((u >> 5) & 1); row = 32 * (rho & 63) + (rho >> 6); }
        else { row = 128 * qt + (u & 96) + fperm(u & 31); row = row > 1375 ? 1375 : row; }
        qv[i] = row * 1024 + (((lane & 7) ^ ((u >> 1) & 7)) << 4);
      }
    }
    constexpr int QSTEP = JOB == 1 ? 64 * 1376 * 2 : 128;
    auto issue = [&](int c) {
      char* sl = lds + (c & 3) * 32768 + w * 4096;
      unroll<4>([&](auto I) { blds<I.v * 1024>(rp, lane * 16, (4 * nt + w) * 32768 + c * 4096, sl); });
#pragma unroll
      for (int i = 0; i < 4; ++i) blds((JOB == 1 && (i & 1)) ? rb : ra, qv[i], c * QSTEP, sl + 16384 + i * 1024);
    };
    issue(0); issue(1); issue(2); issue(3);
    waitv<24>(); bar();
#pragma unroll 1
    for (int c = 0; c < 8; ++c) {
      if (c < 5) waitv<16>(); else if (c == 5) waitv<8>(); else waitv<0>();
      bar();
      if (c < 4) issue(c + 4);
    }
    return;
  }
  f16v acc[2][2] = {}, acc2[2][2] = {};
  float s1 = 0.f, s2 = 0.f;
  const int pOff = wp * 8192 + lane * 16;
  int qOff;
  if (JOB == 1) {
    int g = lane >> 4, qr = (lane >> 2) & 3, run = 4 * (g & 1) + (lane & 3), q = run >> 1;
    qOff = 16384 + (g >> 1) * 2048 + qr * 128 + (q & 1) * 1024 + (8 * (run & 1) + 4 * (q >> 1)) * 2;
  } else qOff = 16384 + (64 * wq + r) * 128;
  struct Fr { u4 P[2], Q[2]; };
  auto rd = [&](const char* sl, int ks) {
    Fr f;
#pragma unroll
    for (int t = 0; t < 2; ++t) f.P[t] = lds128(sl + pOff + t * 4096 + ks * 1024);
#pragma unroll
    for (int b = 0; b < 2; ++b) {
      if (JOB == 1) {
        f.Q[b] = ldtr(sl + qOff + ks * 4096 + (((2 * wq + b) ^ ((lane >> 2) & 3) ^ ((lane >> 1) & 1)) << 5), 512);
      } else f.Q[b] = lds128(sl + qOff + b * 4096 + (((2 * ks + h) ^ ((r >> 1) & 7)) << 4));
    }
    return f;
  };
  u4 Id[2];
  if (JOB == 2) {
#pragma unroll
    for (int k = 0; k < 2; ++k) {
      int e = dperm(r) - 16 * k - 8 * h;
#pragma unroll
      for (int j = 0; j < 4; ++j) Id[k][j] = (e == 2 * j ? 0x3f80u : 0u) | (e == 2 * j + 1 ? 0x3f800000u : 0u);
    }
  }
  auto mm = [&](const Fr& f, int c, int ks) {
#pragma unroll
    for (int t = 0; t < 2; ++t)
#pragma unroll
      for (int b = 0; b < 2; ++b) acc[t][b] = JOB == 2 ? mf32(f.Q[b], f.P[t], acc[t][b]) : mf32(f.P[t], f.Q[b], acc[t][b]);
    if (JOB == 0) {
      u4 v = wp ? f.Q[1] : f.Q[0];
#pragma unroll
      for (int j = 0; j < 4; ++j) { s1 = dot2(v[j], 0x3f803f80u, s1); s2 = dot2(v[j], v[j], s2); }
    }
    mix<4, 2, 3>();
    if (JOB == 0) asm volatile("" : "+v"(s1), "+v"(s2));
    SB();
    if (JOB == 2) {
      if (c == 2 * nt + wp) {
#pragma unroll
        for (int b = 0; b < 2; ++b) acc2[ks >> 1][b] = mf32(f.Q[b], Id[ks & 1], acc2[ks >> 1][b]);
      }
    }
  };
  bar();
  Fr f0 = rd(lds, 0), f1;
#pragma unroll 1
  for (int c = 0; c < 8; ++c) {
    const char* sl = lds + (c & 3) * 32768;
    SB();
    f1 = rd(sl, 1); mm(f0, c, 0);
    f0 = rd(sl, 2); mm(f1, c, 1);
    f1 = rd(sl, 3); mm(f0, c, 2);
    bar();
    SB();
    f0 = rd(lds + ((c + 1) & 3) * 32768, 0); mm(f1, c, 3);
  }
  STAMP(0, 3);
  if (JOB == 0) {
#pragma unroll
    for (int t = 0; t < 2; ++t)
#pragma unroll
      for (int i = 0; i < 16; i += 2) {
        int col = 64 * nt + 32 * wp + 16 * t + (((i & 3) + 8 * (i >> 2) + 4 * h) >> 1);
        u2 v = {pk(acc[t][0][i], acc[t][0][i + 1]), pk(acc[t][1][i], acc[t][1][i + 1])};
        *(u2*)(o0 + col * SS + 128 * qt + 64 * wq + 2 * r) = v;
      }
    s1 += __shfl_xor(s1, 32); s2 += __shfl_xor(s2, 32);
    float m = s1 * (1.f / 512), var = fmaxf(s2 * (1.f / 512) - m * m, 0.f);
    if (nt == 0 && h == 0) ((float*)o1)[128 * qt + 64 * wq + 2 * r + wp] = __builtin_amdgcn_rsqf(var + EPS) * (1.f / 2048);
  } else if (JOB == 1) {
#pragma unroll
    for (int b = 0; b < 2; ++b) {
      int tile = 4 * qt + 2 * wq + b;
      if (tile < 86)
#pragma unroll
        for (int t = 0; t < 2; ++t)
#pragma unroll
          for (int s = 0; s < 2; ++s) {
            int ks = 8 * nt + 4 * wp + 2 * t + s;
            u4 v = pack8(acc[t][b], s);
            if (ks == 0 && h == 0) v[0] &= 0xffffu;
            *(u4*)(o0 + (tile * 32 + ks) * 256 + lane * 4) = v;
          }
    }
  } else {
#pragma unroll
    for (int b = 0; b < 2; ++b)
#pragma unroll
      for (int s = 0; s < 2; ++s) {
        int ks = 8 * qt + 4 * wq + 2 * b + s;
        if (ks < 86)
#pragma unroll
          for (int t = 0; t < 2; ++t) {
            int o = ((4 * nt + 2 * wp + t) * 86 + ks) * 256 + lane * 4;
            *(u4*)(o0 + o) = pack8(acc[t][b], s);
            *(u4*)(o1 + o) = pack8(acc2[t][b], s);
          }
      }
  }
}

__global__ __launch_bounds__(512) void k_prep(const u16* __restrict__ x, const u16* __restrict__ wr, const u16* __restrict__ wi,
    const u16* __restrict__ wg, const u16* __restrict__ wu, const u16* __restrict__ wd,
    u32* __restrict__ xh, u32* __restrict__ rstd, u32* __restrict__ wgu, u32* __restrict__ wd1, u32* __restrict__ wd2,
    u32* __restrict__ filt, u32* __restrict__ r2) {
  __shared__ __attribute__((aligned(1024))) char lds[131072];
  const int b = blockIdx.x, nt = (b >> 3) & 3, q = 8 * (b >> 5) + (b & 7);
  const int lane = threadIdx.x & 63, w = threadIdx.x >> 6;
  STAMP(0, 0);
  if (q < 16) prep_gemm<0>(x, x, q, nt, lds, xh, rstd);
  else if (q < 38) prep_gemm<1>(wg, wu, q - 16, nt, lds, wgu, wgu);
  else if (q < 49) prep_gemm<2>(wd, wd, q - 38, nt, lds, wd1, wd2);
  else if (q < 57) {
    {
      int unit = ((q - 49) * 4 + nt) * 8 + w, cb = unit & 7, wmg = unit >> 3;
      int c0 = 32 * cb + 8 * (lane >> 4), k0 = 16 * (wmg >> 3) + (lane & 15) + 64 * (16 * ((wmg >> 2) & 1) + 4 * (wmg & 3));
      u4 a[4], bb[4];
#pragma unroll
      for (int i = 0; i < 4; ++i) {
        a[i] = *(const u4_ua*)(wr + (k0 + 64 * i) * 257 + 1 + c0);
        bb[i] = *(const u4_ua*)(wi + (k0 + 64 * i) * 257 + 1 + c0);
      }
#pragma unroll
      for (int e = 0; e < 8; ++e) {
        u4 v;
#pragma unroll
        for (int i = 0; i < 4; ++i) v[i] = (e & 1) ? ((a[i][e >> 1] >> 16) | (bb[i][e >> 1] & 0xffff0000u)) : ((a[i][e >> 1] & 0xffffu) | (bb[i][e >> 1] << 16));
        int col = (c0 + e + 1) & 255;
        *(u4*)(filt + col * SS + (wmg >> 2) * 256 + ((wmg & 3) * 16 + (lane & 15)) * 4) = v;
      }
    }
  } else if (q < 61) {
    int dt = (q - 57) * 4 + nt, d = 32 * dt + dperm(lane & 31), h = lane >> 5;
#pragma unroll
    for (int it = 0; it < 4; ++it) {
      int ks = 4 * w + it;
      u4 v;
#pragma unroll
      for (int jj = 0; jj < 4; ++jj) {
        int c = 8 * ks + 4 * (jj >> 1) + 2 * h + (jj & 1);
        float a = (float)((c * d) & 511) * (1.f / 512);
        float re = __builtin_amdgcn_cosf(a) * 0.0625f, im = __builtin_amdgcn_sinf(a) * -0.0625f;
        if (c == 0) { im = 0.04419417382f; re = (d & 1) ? -im : im; }
        v[jj] = pk(re, im);
      }
      *(u4*)(r2 + (dt * 32 + ks) * 256 + lane * 4) = v;
    }
  }
  STAMPW(0, q < 16 ? 4 : q < 38 ? 5 : q < 49 ? 6 : 7);
}

DI u4 rot(u4 v) { u4 o; for (int j = 0; j < 4; ++j) o[j] = __builtin_amdgcn_alignbit(v[j], v[j], 16) ^ 0x8000u; return o; }
template <int NP>
__global__ __launch_bounds__(256, 2) void k_fft(const u32* __restrict__ xin, const float* __restrict__ st, const u32* __restrict__ filt,
                                                u32* __restrict__ xout) {
  __shared__ __attribute__((aligned(256))) char lds[16384];
  const int col = blockIdx.x, tid = threadIdx.x, lane = tid & 63, w = tid >> 6, l15 = lane & 15, g = lane >> 4;
  constexpr int KI = NP == 1 ? 1 : 4;
  STAMP(KI, 0);
  const u32* xc = xin + col * SS;
  u4 xd[2], xr[2], fw[2];
  f4 sv[NP][2];
#pragma unroll
  for (int i = 0; i < 2; ++i) xd[i] = *(const u4*)(xc + 1024 * i + 4 * tid);
#pragma unroll
  for (int p = 0; p < NP; ++p)
#pragma unroll
    for (int i = 0; i < 2; ++i) sv[p][i] = *(const f4*)(st + p * SS + 1024 * i + 4 * tid);
#pragma unroll
  for (int i = 0; i < 2; ++i) {
    fw[i] = *(const u4*)(filt + col * SS + (2 * w + i) * 256 + lane * 4);
    xr[i] = *(const u4*)(xc + 64 * (16 * i + l15) + 16 * w + 4 * g);
  }
  SB();
  const int k1 = 16 * w + l15;
  const float fk1 = (float)k1, fl = (float)l15, fa = (float)(4 * g);
  u4 F64r[4], F64i[4], F32r[2][2], F32i[2][2];
  float t1c[2][4], t1s[2][4], t2c[2][4], t2s[2][4];
#pragma unroll
  for (int j = 0; j < 4; ++j) {
    float a = (fa + j) * fk1 * (1.f / 64), b = (fa + j) * fl * (1.f / 32);
    F64r[0][j] = pk(__builtin_amdgcn_cosf(a), __builtin_amdgcn_sinf(a));
    F32r[0][0][j] = pk(__builtin_amdgcn_cosf(b), __builtin_amdgcn_sinf(b));
  }
  F64i[0] = rot(F64r[0]); F32i[0][0] = rot(F32r[0][0]);
  const u32 NEG = 0x80008000u, so = (lane & 1) ? NEG : 0u, sr = (lane & 2) ? NEG : 0u, si = ((lane + 1) & 2) ? NEG : 0u;
#pragma unroll
  for (int j = 0; j < 4; ++j) {
    F64r[1][j] = ((lane & 1) ? F64i[0][j] : F64r[0][j]) ^ sr;
    F64i[1][j] = ((lane & 1) ? F64r[0][j] : F64i[0][j]) ^ si;
    F64r[2][j] = F64r[0][j] ^ so; F64i[2][j] = F64i[0][j] ^ so;
    F64r[3][j] = F64r[1][j] ^ so; F64i[3][j] = F64i[1][j] ^ so;
    const u32 sj = (j & 1) ? NEG : 0u;
    F32r[0][1][j] = F32r[0][0][j] ^ so; F32i[0][1][j] = F32i[0][0][j] ^ so;
    F32r[1][0][j] = F32r[0][0][j] ^ sj; F32i[1][0][j] = F32i[0][0][j] ^ sj;
    F32r[1][1][j] = F32r[0][1][j] ^ sj; F32i[1][1][j] = F32i[0][1][j] ^ sj;
  }
#pragma unroll
  for (int t = 0; t < 2; ++t)
#pragma unroll
    for (int j = 0; j < 4; ++j) {
      float a = (fa + (16 * t + j)) * (fk1 * (1.f / 2048)), b = ((fa + (float)(16 * w)) + j) * ((fl + 16 * t) * (1.f / 2048));
      t1c[t][j] = __builtin_amdgcn_cosf(a); t1s[t][j] = __builtin_amdgcn_sinf(a);
      t2c[t][j] = __builtin_amdgcn_cosf(b); t2s[t][j] = __builtin_amdgcn_sinf(b);
    }
  SB();
  STAMP(KI, 1);
  const u32 msk = col == 0 ? 0xffffu : 0xffffffffu;
#pragma unroll
  for (int i = 0; i < 2; ++i) {
    f4 rs = sv[0][i];
    if (NP > 1) {
#pragma unroll
      for (int p = 1; p < NP; ++p) rs += sv[p][i];
#pragma unroll
      for (int j = 0; j < 4; ++j) rs[j] = __builtin_amdgcn_rsqf(rs[j] * (1.f / 512) + EPS) * (1.f / 2048);
    }
    u4 z;
#pragma unroll
    for (int j = 0; j < 4; ++j) z[j] = pk(lo(xd[i][j]) * rs[j], hi(xd[i][j]) * rs[j]) & msk;
    *(u4*)(lds + (16 * i + (tid >> 4)) * 256 + ((l15 ^ (tid >> 4)) << 4)) = z;
  }
  __syncthreads();
  STAMP(KI, 2);
  u4 z[2][4], T[2], Y[2];
  f4 ar[2], ai[2];
  const f4 Z4 = {0, 0, 0, 0};
#pragma unroll
  for (int s = 0; s < 4; ++s)
#pragma unroll
    for (int t = 0; t < 2; ++t) z[t][s] = lds128(lds + (16 * t + l15) * 256 + (((4 * s + g) ^ l15) << 4));
  SB();
#pragma unroll
  for (int t = 0; t < 2; ++t)
#pragma unroll
    for (int s = 0; s < 4; ++s) { ar[t] = mf16(z[t][s], F64r[s], s ? ar[t] : Z4); ai[t] = mf16(z[t][s], F64i[s], s ? ai[t] : Z4); }
#pragma unroll
  for (int t = 0; t < 2; ++t)
#pragma unroll
    for (int j = 0; j < 4; ++j) T[t][j] = pk(ar[t][j] * t1c[t][j] + ai[t][j] * t1s[t][j], ai[t][j] * t1c[t][j] - ar[t][j] * t1s[t][j]);
#pragma unroll
  for (int m = 0; m < 2; ++m)
#pragma unroll
    for (int t = 0; t < 2; ++t) { ar[m] = mf16(F32r[m][t], T[t], t ? ar[m] : Z4); ai[m] = mf16(F32i[m][t], T[t], t ? ai[m] : Z4); }
#pragma unroll
  for (int m = 0; m < 2; ++m)
#pragma unroll
    for (int j = 0; j < 4; ++j) { float a = lo(fw[m][j]), b = hi(fw[m][j]); Y[m][j] = pk(ar[m][j] * a - ai[m][j] * b, -(ar[m][j] * b) - ai[m][j] * a); }
#pragma unroll
  for (int t = 0; t < 2; ++t)
#pragma unroll
    for (int m = 0; m < 2; ++m) { ar[t] = mf16(Y[m], F32r[t][m], m ? ar[t] : Z4); ai[t] = mf16(Y[m], F32i[t][m], m ? ai[t] : Z4); }
#pragma unroll
  for (int t = 0; t < 2; ++t) {
    u4 v;
#pragma unroll
    for (int j = 0; j < 4; ++j) v[j] = pk(ar[t][j] * t2c[t][j] + ai[t][j] * t2s[t][j], ai[t][j] * t2c[t][j] - ar[t][j] * t2s[t][j]);
    *(u4*)(lds + 8192 + (16 * t + l15) * 256 + (((4 * w + g) ^ l15) << 4)) = v;
  }
  __syncthreads();
#pragma unroll
  for (int s = 0; s < 4; ++s)
#pragma unroll
    for (int t = 0; t < 2; ++t) z[t][s] = lds128(lds + 8192 + (16 * t + l15) * 256 + (((4 * s + g) ^ l15) << 4));
  SB();
#pragma unroll
  for (int t = 0; t < 2; ++t)
#pragma unroll
    for (int s = 0; s < 4; ++s) { ar[t] = mf16(F64r[s], z[t][s], s ? ar[t] : Z4); ai[t] = mf16(F64i[s], z[t][s], s ? ai[t] : Z4); }
  const float sg = col == 0 ? 0.f : -1.f;
#pragma unroll
  for (int t = 0; t < 2; ++t) {
    float re[4], im[4];
#pragma unroll
    for (int j = 0; j < 4; ++j) { re[j] = lo(xr[t][j]) + ar[t][j]; im[j] = hi(xr[t][j]) + (col == 0 ? 0.f : sg * ai[t][j]); }
    u32* o = xout + col * SS + 32 * (16 * t + l15) + 8 * w + 2 * g;
    *(u2*)o = u2{pk(re[0], re[1]), pk(re[2], re[3])};
    *(u2*)(o + 1024) = u2{pk(im[0], im[1]), pk(im[2], im[3])};
  }
  STAMP(KI, 3); STAMPW(KI, 4);
}

template <int LAST>
__global__ __launch_bounds__(512) void k_up(const u32* __restrict__ xh, const u32* __restrict__ wgu, const u32* __restrict__ r2,
                                               u32* __restrict__ hb, u32* __restrict__ x3, float* __restrict__ rs2) {
  constexpr int NT = 6 + LAST, CH = NT * 4096 + 16384, NS = LAST ? 3 : 4, NI = NT + 4;
  __shared__ __attribute__((aligned(1024))) char lds[NS * CH];
  const int xc = blockIdx.x & 7, jb = blockIdx.x >> 3, mt = 8 * (xc & 1) + (jb & 7), ng = 4 * (xc >> 1) + (jb >> 3);
  const int t0 = (ng * 43) >> 3, t1 = ((ng + 1) * 43) >> 3;
  const int lane = threadIdx.x & 63, w8 = __builtin_amdgcn_readfirstlane(threadIdx.x >> 6), w = w8 & 3;
  const int wm = w >> 1, wn = w & 1, r = lane & 31, h = lane >> 5;
  constexpr int KI = LAST ? 5 : 2;
  STAMP(KI, 0);
  if (w8 >= 4) {
    const rsrc rq = mkr(xh), rp = mkr(wgu), rr = mkr(r2);
    const int vq = (lane >> 4) * 4096 + (((lane & 15) ^ (4 * (lane >> 4))) << 4);
    auto issue = [&](int c, char* sl) {
#pragma unroll
      for (int i = 0; i < 4; ++i) blds(rq, vq, (64 * c + 16 * w + 4 * i) * 4096 + 256 * mt, sl + NT * 4096 + (4 * w + i) * 1024);
#pragma unroll
      for (int i = 0; i < 3; ++i) {
        int e = 6 * w + 2 * i, so = (t0 + (e >> 2)) * 32768 + (4 * c + (e & 3)) * 1024;
        blds(rp, lane * 16, so, sl + e * 1024); blds<1024>(rp, lane * 16, so, sl + e * 1024);
      }
      if (LAST) blds(rr, lane * 16, (ng * 32 + 4 * c + w) * 1024, sl + (24 + w) * 1024);
    };
#pragma unroll
    for (int c = 0; c < NS; ++c) issue(c, lds + c * CH);
    waitv<(NS - 1) * NI>(); bar();
    int so = 0;
#pragma unroll 1
    for (int c = 0; c < 8; ++c) {
      if (c < 9 - NS) waitv<(NS - 2) * NI>(); else if (NS == 4 && c == 5) waitv<NI>(); else waitv<0>();
      bar();
      if (c < 8 - NS) issue(c + NS, lds + so);
      so = so == (NS - 1) * CH ? 0 : so + CH;
    }
    return;
  }
  f16v acc[3][2] = {}, accx = {};
  float ss = 0.f;
  u32 dcm = h ? 0xffffffffu : 0xffffu;
  const int pOff = wn * 12288 + lane * 16;
  int qOff[2];
#pragma unroll
  for (int b = 0; b < 2; ++b)
    qOff[b] = NT * 4096 + (4 * h + ((lane >> 2) & 3)) * 256 + (((4 * (2 * wm + (b ^ wn)) + ((lane >> 3) & 2) + ((lane >> 1) & 1)) ^ (lane & 12)) << 4) + 8 * (lane & 1);
  struct Fr { u4 P[3], Q[2], X; };
  auto rd = [&](const char* sl, int ks) {
    Fr f;
#pragma unroll
    for (int b = 0; b < 2; ++b) f.Q[b] = ldtr(sl + qOff[b] + ks * 4096, 2048);
#pragma unroll
    for (int t = 0; t < 3; ++t) f.P[t] = lds128(sl + pOff + t * 4096 + ks * 1024);
    if (LAST) f.X = lds128(sl + 24576 + ks * 1024 + lane * 16);
    return f;
  };
  auto mm = [&](const Fr& f, int ks) {
#pragma unroll
    for (int t = 0; t < 3; ++t) { acc[t][0] = mf32(f.P[t], f.Q[0], acc[t][0]); acc[t][1] = mf32(f.P[t], f.Q[1], acc[t][1]); }
    u4 v = f.Q[0];
    if (LAST) accx = mf32(f.X, v, accx);
    if (ks == 0) { v[0] &= dcm; dcm = 0xffffffffu; }
#pragma unroll
    for (int j = 0; j < 4; ++j) ss = dot2(v[j], v[j], ss);
    mix<6 + LAST, 2, 1>();
    asm volatile("" : "+v"(ss));
    SB();
  };
  bar();
  STAMP(KI, 1);
  Fr f0 = rd(lds, 0), f1;
  int so = 0;
#pragma unroll 1
  for (int c = 0; c < 8; ++c) {
    char* sl = lds + so;
    so = so == (NS - 1) * CH ? 0 : so + CH;
    SB();
    f1 = rd(sl, 1); mm(f0, 0);
    f0 = rd(sl, 2); mm(f1, 1);
    f1 = rd(sl, 3); mm(f0, 2);
    bar();
    SB();
    f0 = rd(lds + so, 0); mm(f1, 3);
  }
  STAMP(KI, 3);
#pragma unroll
  for (int t = 0; t < 3; ++t) {
    int tile = t0 + 3 * wn + t;
    if (tile < t1)
#pragma unroll
      for (int b = 0; b < 2; ++b) {
        const f16v& a = acc[t][b];
        u4 o;
#pragma unroll
        for (int j = 0; j < 4; ++j) { int i = 8 * (j >> 1) + 2 * (j & 1); o[j] = pk(fmaxf(a[i], 0.f) * a[i + 4], fmaxf(a[i + 1], 0.f) * a[i + 5]); }
        *(u4*)(hb + (((2 * mt + wm) * 2 + (b ^ wn)) * 86 + tile) * 256 + lane * 4) = o;
      }
  }
  const int rho = 128 * mt + 64 * wm + 32 * wn + r;
  if (LAST) {
    u32* o = x3 + ((4 * mt + w) * 16 + ng) * 512 + lane * 4;
    *(u4*)o = pack8(accx, 0); *(u4*)(o + 256) = pack8(accx, 1);
  }
  if (ng == 0) {
    ss += __shfl_xor(ss, 32);
    if (h == 0) rs2[rho] = 1.f / (ss * (1.f / 512) + EPS);
  }
  STAMP(KI, 4); STAMPW(KI, 5);
}

template <int LAST>
__global__ __launch_bounds__(512) void k_down(const u32* __restrict__ hb, const u32* __restrict__ wd, const float* __restrict__ rs2,
                                              const u32* __restrict__ res, u32* __restrict__ out, float* __restrict__ ssp) {
  __shared__ __attribute__((aligned(1024))) char lds[131072 + 512];
  const int xc = blockIdx.x & 7, jb = blockIdx.x >> 3, rg = 8 * (xc & 3) + (jb & 7), nt = 4 * (xc >> 2) + (jb >> 3);
  const int lane = threadIdx.x & 63, w8 = __builtin_amdgcn_readfirstlane(threadIdx.x >> 6), w = w8 & 3, r = lane & 31, h = lane >> 5;
  constexpr int KI = LAST ? 6 : 3;
  STAMP(KI, 0);
  if (w8 >= 4) {
    const rsrc rs = mkr(w < 2 ? wd + (2 * nt + w) * 86 * 256 : hb + (2 * rg + w - 2) * 86 * 256);
    auto issue = [&](int c) {
      char* sl = lds + (c & 3) * 32768 + w * 8192;
      unroll<4>([&](auto I) { blds<I.v * 1024>(rs, lane * 16, c * 8192, sl); });
      unroll<2>([&](auto I) { blds<I.v * 1024>(rs, lane * 16, c * 8192 + 4096, sl + 4096); });
      if (c < 10) unroll<2>([&](auto I) { blds<I.v * 1024 + 2048>(rs, lane * 16, c * 8192 + 4096, sl + 4096); });
    };
    issue(0); issue(1); issue(2); issue(3);
    waitv<24>(); bar();
#pragma unroll 1
    for (int c = 0; c < 10; ++c) {
      if (c < 7) waitv<16>(); else if (c == 7) waitv<14>(); else if (c == 8) waitv<6>(); else waitv<0>();
      bar();
      if (c < 7) issue(c + 4);
    }
    if (!LAST) bar();
    return;
  }
  const int t = w >> 1, b = w & 1, rho = 64 * rg + 32 * b + r;
  u32 rv[8];
  if (LAST) {
    const u32* p = res + ((2 * rg + b) * 16 + 2 * nt + t) * 512 + lane * 4;
    u4 x = *(const u4*)p, y = *(const u4*)(p + 256);
#pragma unroll
    for (int j = 0; j < 4; ++j) { rv[j] = x[j]; rv[4 + j] = y[j]; }
  } else {
    const u16* p = (const u16*)res + (64 * nt + 32 * t + 4 * h) * SS + rho;
#pragma unroll
    for (int j = 0; j < 8; ++j) { const u16* e = p + (8 * (j >> 1) + 2 * (j & 1)) * SS; rv[j] = (u32)e[0] | ((u32)e[SS] << 16); }
  }
  const float rs = rs2[rho];
  struct Fr { u4 A[4], B[4]; };
  const int aOff = t * 8192 + lane * 16, bOff = 16384 + b * 8192 + lane * 16;
  auto rd = [&](const char* sl, auto N) {
    Fr f;
#pragma unroll
    for (int k = 0; k < N.v; ++k) { f.A[k] = lds128(sl + aOff + k * 1024); f.B[k] = lds128(sl + bOff + k * 1024); }
    return f;
  };
  f16v acc = {};
  auto mm = [&](const Fr& f, auto N) {
#pragma unroll
    for (int k = 0; k < N.v; ++k) acc = mf32(f.A[k], f.B[k], acc);
    mix<N.v, 2, 1>();
    SB();
  };
  bar();
  STAMP(KI, 1);
  Fr f0 = rd(lds, IC<4>{}), f1;
#pragma unroll 1
  for (int c = 0; c < 10; ++c) {
    SB();
    f1 = rd(lds + (c & 3) * 32768 + 4096, IC<4>{}); mm(f0, IC<4>{});
    bar();
    SB();
    f0 = rd(lds + ((c + 1) & 3) * 32768, IC<4>{}); mm(f1, IC<4>{});
  }
  f1 = rd(lds + 2 * 32768 + 4096, IC<2>{}); mm(f0, IC<4>{});
  mm(f1, IC<2>{});
  STAMP(KI, 2);
  u32 v[8];
#pragma unroll
  for (int j = 0; j < 8; ++j) v[j] = pk(lo(rv[j]) + rs * acc[2 * j], hi(rv[j]) + rs * acc[2 * j + 1]);
  if (LAST) {
    u32* o = out + (32 * (32 * b + r) + rg) * 256 + 32 * nt + 16 * t + 8 * h;
    *(u4*)o = u4{v[0], v[1], v[2], v[3]}; *(u4*)(o + 4) = u4{v[4], v[5], v[6], v[7]};
  } else {
    const int c0 = 32 * nt + 16 * t + 2 * h;
    float ss = 0.f;
#pragma unroll
    for (int j = 0; j < 8; ++j) {
      out[(c0 + 4 * (j >> 1) + (j & 1)) * SS + rho] = v[j];
      u32 m = (j == 0 && c0 == 0) ? (v[j] & 0xffffu) : v[j];
      ss = dot2(m, m, ss);
    }
    ss += __shfl_xor(ss, 32);
    float* sl = (float*)(lds + 131072);
    if (h == 0) sl[w * 32 + r] = ss;
    bar();
    if (w == 0) ssp[nt * SS + 64 * rg + lane] = sl[lane] + sl[64 + lane];
  }
  STAMP(KI, 3); STAMPW(KI, 4);
}

extern "C" void kernel_launch(void* const* d_in, const int* in_sizes, int n_in, void* d_out, int out_size, void* d_ws, size_t ws_size,
                              hipStream_t stream) {
  const u16 *x = (const u16*)d_in[0], *wr = (const u16*)d_in[1], *wi = (const u16*)d_in[2];
  const u16 *wg = (const u16*)d_in[4], *wu = (const u16*)d_in[5], *wd = (const u16*)d_in[6];
  char* ws = (char*)d_ws;
  const size_t MB = 1 << 20;
  u32 *xh0 = (u32*)ws, *xh1 = (u32*)(ws + 2 * MB), *xh2 = (u32*)(ws + 4 * MB), *xh3 = (u32*)(ws + 6 * MB);
  u32 *filt = (u32*)(ws + 8 * MB), *x3 = (u32*)(ws + 10 * MB), *wgu = (u32*)(ws + 12 * MB), *wd1 = (u32*)(ws + 15 * MB);
  u32 *wd2 = (u32*)(ws + 17 * MB), *r2 = (u32*)(ws + 19 * MB), *hb = (u32*)(ws + 20 * MB);
  float *rstd = (float*)(ws + 26 * MB), *ssp = (float*)(ws + 27 * MB), *rs2 = (float*)(ws + 28 * MB);
  k_prep<<<256, 512, 0, stream>>>(x, wr, wi, wg, wu, wd, xh0, (u32*)rstd, wgu, wd1, wd2, filt, r2);
  k_fft<1><<<256, 256, 0, stream>>>(xh0, rstd, filt, xh1);
  k_up<0><<<256, 512, 0, stream>>>(xh1, wgu, r2, hb, x3, rs2);
  k_down<0><<<256, 512, 0, stream>>>(hb, wd1, rs2, xh1, xh2, ssp);
  k_fft<8><<<256, 256, 0, stream>>>(xh2, ssp, filt, xh3);
  k_up<1><<<256, 512, 0, stream>>>(xh3, wgu, r2, hb, x3, rs2);
  k_down<1><<<256, 512, 0, stream>>>(hb, wd2, rs2, x3, (u32*)d_out, ssp);
}
```

```cpp
#include <hip/hip_runtime.h>
#include <hip/hip_bf16.h>

typedef unsigned u32;
typedef unsigned short u16;
typedef __attribute__((ext_vector_type(8))) __bf16 bf8;
typedef __attribute__((ext_vector_type(2))) __bf16 bf2;
typedef __attribute__((ext_vector_type(2))) float f2;
typedef __attribute__((ext_vector_type(4))) float f4;
typedef __attribute__((ext_vector_type(16))) float f16v;
typedef __attribute__((ext_vector_type(2))) u32 u2;
typedef __attribute__((ext_vector_type(4))) u32 u4;
typedef __attribute__((ext_vector_type(4))) short s4;
typedef u4 __attribute__((aligned(2))) u4_ua;
#define DI __device__ __forceinline__
#define LDSP __attribute__((address_space(3)))
#define GLBP __attribute__((address_space(1)))

#define STAMP(k, j)
#define STAMPW(k, j)
#define UPPRE 2
constexpr int SS = 2048;
constexpr int XI = 2112, PS = 1088, FS = 2080;
constexpr float EPS = 1e-5f;

struct Part { u16 v[16384]; };
#if defined(__HIP_DEVICE_COMPILE__)
constexpr double kPi = 3.14159265358979323846;
constexpr double csin(double x) { double t = x, s = x; for (int i = 1; i < 14; ++i) { t *= -x * x / ((2 * i) * (2 * i + 1)); s += t; } return s; }
constexpr u16 tobf(double x) { float f = (float)x; u32 u = __builtin_bit_cast(u32, f); u += 0x7fffu + ((u >> 16) & 1u); return (u16)(u >> 16); }
struct CosT { u16 v[512]; };
constexpr CosT mkcos() {
  CosT c{};
  for (int i = 0; i < 512; ++i) {
    int q = (i + 128) & 511;
    int y = q <= 128 ? q : (q <= 384 ? 256 - q : q - 512);
    c.v[i] = y == 0 ? (u16)0 : tobf(csin(2 * kPi * y / 512) / 16.0);
  }
  return c;
}
constexpr CosT kCos = mkcos();
constexpr u16 kDC = tobf(0.04419417382415922);
constexpr Part mkR(int pi) {
  Part p{};
  for (int e = 0; e < 16384; ++e) {
    int r = (e >> 3) & 31, h = (e >> 8) & 1, j = e & 7, d = 0, s = 0;
    if (pi < 4) { d = (((e >> 9) & 7) << 4) + 8 * h + j; s = 32 * (4 * pi + (e >> 12)) + r; }
    else { d = 32 * (pi - 4) + (r & 3) + 4 * (r >> 3) + 16 * ((r >> 2) & 1); s = ((e >> 9) << 4) + 8 * (j >> 2) + 4 * h + (j & 3); }
    p.v[e] = s >= 2 ? kCos.v[((s >> 1) * d + ((s & 1) << 7)) & 511] : (u16)(kDC | ((s == 0 && (d & 1)) ? 0x8000 : 0));
  }
  return p;
}
template <int I> struct PR { static constexpr Part v = mkR(I); };
struct Tab { Part p[20]; };
__device__ const __attribute__((aligned(1024))) Tab R1T = {{PR<0>::v, PR<1>::v, PR<2>::v, PR<3>::v, PR<4>::v, PR<5>::v, PR<6>::v, PR<7>::v,
  PR<8>::v, PR<9>::v, PR<10>::v, PR<11>::v, PR<12>::v, PR<13>::v, PR<14>::v, PR<15>::v, PR<16>::v, PR<17>::v, PR<18>::v, PR<19>::v}};
#else
struct Tab { u16 p[8]; };
__device__ const Tab R1T = {};
#endif

DI f16v mf32(u4 a, u4 b, f16v c) { return __builtin_amdgcn_mfma_f32_32x32x16_bf16(__builtin_bit_cast(bf8, a), __builtin_bit_cast(bf8, b), c, 0, 0, 0); }
DI f4 mf16(u4 a, u4 b, f4 c) { return __builtin_amdgcn_mfma_f32_16x16x32_bf16(__builtin_bit_cast(bf8, a), __builtin_bit_cast(bf8, b), c, 0, 0, 0); }
DI u32 pk(float a, float b) { f2 v = {a, b}; return __builtin_bit_cast(u32, __builtin_convertvector(v, bf2)); }
DI float lo(u32 u) { return __builtin_bit_cast(float, u << 16); }
DI float hi(u32 u) { return __builtin_bit_cast(float, u & 0xffff0000u); }
DI float dot2(u32 a, u32 b, float c) { return __builtin_amdgcn_fdot2_f32_bf16(__builtin_bit_cast(bf2, a), __builtin_bit_cast(bf2, b), c, false); }
DI void glds(const void* g, char* l) { __builtin_amdgcn_global_load_lds((const GLBP void*)g, (LDSP void*)l, 16, 0, 0); }
typedef __amdgpu_buffer_rsrc_t rsrc;
DI rsrc mkr(const void* p) { return __builtin_amdgcn_make_buffer_rsrc((void*)p, 0, 0x7fffffff, 0x00020000); }
template <int O = 0> DI void blds(rsrc r, int vo, int so, char* l) { __builtin_amdgcn_raw_ptr_buffer_load_lds(r, (LDSP void*)l, 16, vo, so, O, 0); }
DI u4 ldtr(const char* a, int d) {
  u2 x = __builtin_bit_cast(u2, __builtin_amdgcn_ds_read_tr16_b64_v4i16((LDSP s4*)a));
  u2 y = __builtin_bit_cast(u2, __builtin_amdgcn_ds_read_tr16_b64_v4i16((LDSP s4*)(a + d)));
  return u4{x[0], x[1], y[0], y[1]};
}
DI u4 lds128(const char* p) { return *(const u4*)p; }
DI u4 pack8(const f16v& a, int s) { u4 o = {pk(a[8 * s], a[8 * s + 1]), pk(a[8 * s + 2], a[8 * s + 3]), pk(a[8 * s + 4], a[8 * s + 5]), pk(a[8 * s + 6], a[8 * s + 7])}; return o; }
template <int N> DI void waitv() { asm volatile("s_waitcnt vmcnt(%0)" ::"n"(N) : "memory"); }
DI void bar() { asm volatile("s_waitcnt lgkmcnt(0)" ::: "memory"); __builtin_amdgcn_s_barrier(); asm volatile("" ::: "memory"); }
DI int dperm(int m) { return (m & 3) + 4 * (m >> 3) + 16 * ((m >> 2) & 1); }
DI int fperm(int m) { int i = (m & 3) + 4 * (m >> 3); return 16 * (i >> 3) + 8 * ((m >> 2) & 1) + (i & 7); }
#define SB() __builtin_amdgcn_sched_barrier(0)
#define SGB(m, n) __builtin_amdgcn_sched_group_barrier(m, n, 0)
template <int NM, int ND, int NV> DI void mix() { if constexpr (NM > 0) { SGB(0x008, 1); SGB(0x100, ND); SGB(0x002, NV); mix<NM - 1, ND, NV>(); } }
template <int I> struct IC { static constexpr int v = I; };
template <int N, class F> DI void unroll(F&& f) { if constexpr (N > 0) { unroll<N - 1>(f); f(IC<N - 1>{}); } }

template <int JOB>
DI void prep_gemm(const u16* __restrict__ q0, const u16* __restrict__ q1, int qt, int nt, char* lds,
                  u32* __restrict__ o0, u32* __restrict__ o1) {
  const int lane = threadIdx.x & 63, w8 = __builtin_amdgcn_readfirstlane(threadIdx.x >> 6), w = w8 & 3;
  const int wp = w >> 1, wq = w & 1, r = lane & 31, h = lane >> 5;
  if (w8 >= 4) {
    const rsrc rp = mkr(&R1T), ra = mkr(q0), rb = mkr(q1);
    int qv[4];
#pragma unroll
    for (int i = 0; i < 4; ++i) {
      if (JOB == 1) {
        int d = 16 * w + 8 * (i >> 1) + (lane >> 3), pos = lane & 7;
        int f = 64 * qt + 16 * ((pos >> 1) ^ (d & 3) ^ (i & 1)) + 8 * (pos & 1);
        qv[i] = (d * 1376 + (f > 1368 ? 1368 : f)) * 2;
      } else {
        int u = 32 * w + 8 * i + (lane >> 3), row;
        if (JOB == 0) { int rho = 128 * qt + (u & 64) + 2 * (u & 31) + ((u >> 5) & 1); row = 32 * (rho & 63) + (rho >> 6); }
        else { row = 128 * qt + (u & 96) + fperm(u & 31); row = row > 1375 ? 1375 : row; }
        qv[i] = row * 1024 + (((lane & 7) ^ ((u >> 1) & 7)) << 4);
      }
    }
    constexpr int QSTEP = JOB == 1 ? 64 * 1376 * 2 : 128;
    auto issP = [&](int c) { unroll<4>([&](auto I) { blds<I.v * 1024>(rp, lane * 16, (4 * nt + w) * 8192 + c * 4096, lds + w * 8192 + c * 4096); }); };
    auto issue = [&](int c) {
#pragma unroll
      for (int i = 0; i < 4; ++i) blds((JOB == 1 && (i & 1)) ? rb : ra, qv[i], c * QSTEP, lds + 32768 + c * 16384 + (4 * w + i) * 1024);
    };
    issP(0); issue(0); issP(1); issue(1); issue(2);
    waitv<12>(); bar();
#pragma unroll 1
    for (int c = 3; c < 8; ++c) issue(c);
    unroll<8>([&](auto I) { waitv<(I.v < 6 ? 4 * (6 - I.v) : 0)>(); bar(); });
    if (JOB == 0) {
      const int u = 32 * nt + 8 * w + (lane >> 3);
      float s1 = 0.f, s2 = 0.f;
#pragma unroll
      for (int c = 0; c < 8; ++c) {
        u4 v = lds128(lds + 32768 + c * 16384 + u * 128 + (lane & 7) * 16);
#pragma unroll
        for (int j = 0; j < 4; ++j) { s1 = dot2(v[j], 0x3f803f80u, s1); s2 = dot2(v[j], v[j], s2); }
      }
#pragma unroll
      for (int o = 1; o < 8; o <<= 1) { s1 += __shfl_xor(s1, o); s2 += __shfl_xor(s2, o); }
      float m = s1 * (1.f / 512), var = fmaxf(s2 * (1.f / 512) - m * m, 0.f);
      if ((lane & 7) == 0) ((float*)o1)[128 * qt + (u & 64) + 2 * (u & 31) + ((u >> 5) & 1)] = __builtin_amdgcn_rsqf(var + EPS) * (1.f / 2048);
    }
    return;
  }
  f16v acc[2][2] = {}, acc2[2][2] = {};
  int qOff;
  if (JOB == 1) {
    int g = lane >> 4, qr = (lane >> 2) & 3, run = 4 * (g & 1) + (lane & 3), q = run >> 1;
    qOff = 32768 + (g >> 1) * 2048 + qr * 128 + (q & 1) * 1024 + (8 * (run & 1) + 4 * (q >> 1)) * 2;
  } else qOff = 32768 + (64 * wq + r) * 128;
  struct Fr { u4 P[2], Q[2]; };
  int pOff; u32 sgn;
  auto turn = [&](int c) {
    int n = ((r >> 1) * (c >> 1)) & 3;
    pOff = wp * 16384 + (c & 1) * 4096 + (lane ^ (n & 1)) * 16;
    sgn = (((r & 1) ? n + 1 : n) & 2) ? 0x80008000u : 0u;
  };
  auto rd = [&](const char* sl, int ks) {
    Fr f;
#pragma unroll
    for (int t = 0; t < 2; ++t) f.P[t] = lds128(lds + pOff + t * 8192 + ks * 1024);
#pragma unroll
    for (int b = 0; b < 2; ++b) {
      if (JOB == 1) {
        f.Q[b] = ldtr(sl + qOff + ks * 4096 + (((2 * wq + b) ^ ((lane >> 2) & 3) ^ ((lane >> 1) & 1)) << 5), 512);
      } else f.Q[b] = lds128(sl + qOff + b * 4096 + (((2 * ks + h) ^ ((r >> 1) & 7)) << 4));
    }
    return f;
  };
  u4 Id[2];
  if (JOB == 2) {
#pragma unroll
    for (int k = 0; k < 2; ++k) {
      int e = dperm(r) - 16 * k - 8 * h;
#pragma unroll
      for (int j = 0; j < 4; ++j) Id[k][j] = (e == 2 * j ? 0x3f80u : 0u) | (e == 2 * j + 1 ? 0x3f800000u : 0u);
    }
  }
  auto mm = [&](const Fr& f, u32 sg, int c, int ks) {
#pragma unroll
    for (int t = 0; t < 2; ++t) {
      const u4 P = f.P[t] ^ sg;
#pragma unroll
      for (int b = 0; b < 2; ++b) acc[t][b] = JOB == 2 ? mf32(f.Q[b], P, acc[t][b]) : mf32(P, f.Q[b], acc[t][b]);
    }
    SGB(0x002, 4);
    mix<4, 2, 2>();
    SB();
    if (JOB == 2) {
      if (c == 2 * nt + wp) {
#pragma unroll
        for (int b = 0; b < 2; ++b) acc2[ks >> 1][b] = mf32(f.Q[b], Id[ks & 1], acc2[ks >> 1][b]);
      }
    }
  };
  turn(0);
  bar();
  Fr f0 = rd(lds, 0), f1;
#pragma unroll 1
  for (int c = 0; c < 8; ++c) {
    const char* sl = lds + c * 16384;
    const u32 sg = sgn;
    SB();
    f1 = rd(sl, 1); mm(f0, sg, c, 0);
    f0 = rd(sl, 2); mm(f1, sg, c, 1);
    f1 = rd(sl, 3); mm(f0, sg, c, 2);
    turn(c + 1);
    bar();
    SB();
    f0 = rd(lds + ((c + 1) & 7) * 16384, 0); mm(f1, sg, c, 3);
  }
  STAMP(0, 3);
  if (JOB == 0) {
#pragma unroll
    for (int t = 0; t < 2; ++t)
#pragma unroll
      for (int i = 0; i < 16; i += 2) {
        int col = 64 * nt + 32 * wp + 16 * t + (((i & 3) + 8 * (i >> 2) + 4 * h) >> 1);
        u2 v = {pk(acc[t][0][i], acc[t][0][i + 1]), pk(acc[t][1][i], acc[t][1][i + 1])};
        *(u2*)(o0 + col * XI + 128 * qt + 64 * wq + 2 * r) = v;
      }
  } else if (JOB == 1) {
#pragma unroll
    for (int b = 0; b < 2; ++b) {
      int tile = 4 * qt + 2 * wq + b;
      if (tile < 86)
#pragma unroll
        for (int t = 0; t < 2; ++t)
#pragma unroll
          for (int s = 0; s < 2; ++s) {
            int ks = 8 * nt + 4 * wp + 2 * t + s;
            u4 v = pack8(acc[t][b], s);
            if (ks == 0 && h == 0) v[0] &= 0xffffu;
            *(u4*)(o0 + (tile * 32 + ks) * 256 + lane * 4) = v;
          }
    }
  } else {
#pragma unroll
    for (int b = 0; b < 2; ++b)
#pragma unroll
      for (int s = 0; s < 2; ++s) {
        int ks = 8 * qt + 4 * wq + 2 * b + s;
        if (ks < 86)
#pragma unroll
          for (int t = 0; t < 2; ++t) {
            int o = ((4 * nt + 2 * wp + t) * 86 + ks) * 256 + lane * 4;
            *(u4*)(o0 + o) = pack8(acc[t][b], s);
            *(u4*)(o1 + o) = pack8(acc2[t][b], s);
          }
      }
  }
}

__global__ __launch_bounds__(512) void k_prep(const u16* __restrict__ x, const u16* __restrict__ wr, const u16* __restrict__ wi,
    const u16* __restrict__ wg, const u16* __restrict__ wu, const u16* __restrict__ wd,
    u32* __restrict__ xh, u32* __restrict__ rstd, u32* __restrict__ wgu, u32* __restrict__ wd1, u32* __restrict__ wd2,
    u32* __restrict__ filt) {
  __shared__ __attribute__((aligned(1024))) char lds[163840];
  const int b = blockIdx.x, nt = (b >> 3) & 3, q = 8 * (b >> 5) + (b & 7);
  const int lane = threadIdx.x & 63, w = __builtin_amdgcn_readfirstlane(threadIdx.x >> 6);
  STAMP(0, 0);
  if (q < 16) prep_gemm<0>(x, x, q, nt, lds, xh, rstd);
  else if (q < 38) prep_gemm<1>(wg, wu, q - 16, nt, lds, wgu, wgu);
  else if (q < 49) prep_gemm<2>(wd, wd, q - 38, nt, lds, wd1, wd2);
  else {
    const int u = b < 256 ? (q - 49) * 4 + nt : b - 196, k0 = 16 * (u >> 4) + 8 * (u & 1) + 256 * ((u >> 1) & 7);
    const rsrc rs = mkr(w < 4 ? wr : wi);
    const int so = (k0 + 64 * (w & 3)) * 514;
    unroll<4>([&](auto I) { blds<I.v * 1024>(rs, lane * 16, so, lds + w * 4160); });
    if (lane == 0) blds(rs, 0, so + 4096, lds + w * 4160 + 4096);
    waitv<0>(); bar();
    STAMP(0, 1);
#pragma unroll
    for (int it = 0; it < 4; ++it) {
      const int cs = 1 + 32 * w + 8 * it + (lane >> 3);
      const u16* p = (const u16*)(lds + (lane & 7) * 514) + cs;
      u4 v;
#pragma unroll
      for (int j = 0; j < 4; ++j) v[j] = (u32)p[j * 2080] | ((u32)p[(4 + j) * 2080] << 16);
      *(u4*)(filt + (cs & 255) * FS + (u >> 3) * 256 + (((u >> 1) & 3) * 16 + 8 * (u & 1) + (lane & 7)) * 4) = v;
    }
  }
  STAMPW(0, q < 16 ? 4 : q < 38 ? 5 : q < 49 ? 6 : 7);
}

DI u4 rot(u4 v) { u4 o; for (int j = 0; j < 4; ++j) o[j] = __builtin_amdgcn_alignbit(v[j], v[j], 16) ^ 0x8000u; return o; }
template <int NP>
__global__ __launch_bounds__(256, 2) void k_fft(const u32* __restrict__ xin, const float* __restrict__ st, const u32* __restrict__ filt,
                                                u32* __restrict__ xout) {
  __shared__ __attribute__((aligned(256))) char lds[16384];
  const int col = blockIdx.x, tid = threadIdx.x, lane = tid & 63, w = tid >> 6, l15 = lane & 15, g = lane >> 4;
  constexpr int KI = NP == 1 ? 1 : 4;
  STAMP(KI, 0);
  const u32* xc = xin + col * XI;
  u4 xd[2], xr[2], fw[2];
  f4 sv[NP][2];
#pragma unroll
  for (int i = 0; i < 2; ++i) xd[i] = *(const u4*)(xc + 1024 * i + 4 * tid);
#pragma unroll
  for (int p = 0; p < NP; ++p)
#pragma unroll
    for (int i = 0; i < 2; ++i) sv[p][i] = *(const f4*)(st + p * SS + 1024 * i + 4 * tid);
#pragma unroll
  for (int i = 0; i < 2; ++i) {
    fw[i] = *(const u4*)(filt + col * FS + (2 * w + i) * 256 + lane * 4);
    xr[i] = *(const u4*)(xc + 64 * (16 * i + l15) + 16 * w + 4 * g);
  }
  SB();
  const int k1 = 16 * w + l15;
  const float fk1 = (float)k1, fl = (float)l15, fa = (float)(4 * g);
  u4 F64r[4], F64i[4], F32r[2][2], F32i[2][2];
  float t1c[2][4], t1s[2][4], t2c[2][4], t2s[2][4];
#pragma unroll
  for (int j = 0; j < 4; ++j) {
    float a = (fa + j) * fk1 * (1.f / 64), b = (fa + j) * fl * (1.f / 32);
    F64r[0][j] = pk(__builtin_amdgcn_cosf(a), __builtin_amdgcn_sinf(a));
    F32r[0][0][j] = pk(__builtin_amdgcn_cosf(b), __builtin_amdgcn_sinf(b));
  }
  F64i[0] = rot(F64r[0]); F32i[0][0] = rot(F32r[0][0]);
  const u32 NEG = 0x80008000u, so = (lane & 1) ? NEG : 0u, sr = (lane & 2) ? NEG : 0u, si = ((lane + 1) & 2) ? NEG : 0u;
#pragma unroll
  for (int j = 0; j < 4; ++j) {
    F64r[1][j] = ((lane & 1) ? F64i[0][j] : F64r[0][j]) ^ sr;
    F64i[1][j] = ((lane & 1) ? F64r[0][j] : F64i[0][j]) ^ si;
    F64r[2][j] = F64r[0][j] ^ so; F64i[2][j] = F64i[0][j] ^ so;
    F64r[3][j] = F64r[1][j] ^ so; F64i[3][j] = F64i[1][j] ^ so;
    const u32 sj = (j & 1) ? NEG : 0u;
    F32r[0][1][j] = F32r[0][0][j] ^ so; F32i[0][1][j] = F32i[0][0][j] ^ so;
    F32r[1][0][j] = F32r[0][0][j] ^ sj; F32i[1][0][j] = F32i[0][0][j] ^ sj;
    F32r[1][1][j] = F32r[0][1][j] ^ sj; F32i[1][1][j] = F32i[0][1][j] ^ sj;
  }
#pragma unroll
  for (int t = 0; t < 2; ++t)
#pragma unroll
    for (int j = 0; j < 4; ++j) {
      float a = (fa + (16 * t + j)) * (fk1 * (1.f / 2048)), b = ((fa + (float)(16 * w)) + j) * ((fl + 16 * t) * (1.f / 2048));
      t1c[t][j] = __builtin_amdgcn_cosf(a); t1s[t][j] = __builtin_amdgcn_sinf(a);
      t2c[t][j] = __builtin_amdgcn_cosf(b); t2s[t][j] = __builtin_amdgcn_sinf(b);
    }
  SB();
  STAMP(KI, 1);
  const u32 msk = col == 0 ? 0xffffu : 0xffffffffu;
#pragma unroll
  for (int i = 0; i < 2; ++i) {
    f4 rs = sv[0][i];
    if (NP > 1) {
#pragma unroll
      for (int p = 1; p < NP; ++p) rs += sv[p][i];
#pragma unroll
      for (int j = 0; j < 4; ++j) rs[j] = __builtin_amdgcn_rsqf(rs[j] * (1.f / 512) + EPS) * (1.f / 2048);
    }
    u4 z;
#pragma unroll
    for (int j = 0; j < 4; ++j) z[j] = pk(lo(xd[i][j]) * rs[j], hi(xd[i][j]) * rs[j]) & msk;
    *(u4*)(lds + (16 * i + (tid >> 4)) * 256 + ((l15 ^ (tid >> 4)) << 4)) = z;
  }
  __syncthreads();
  STAMP(KI, 2);
  u4 z[2][4], T[2], Y[2];
  f4 ar[2], ai[2];
  const f4 Z4 = {0, 0, 0, 0};
#pragma unroll
  for (int s = 0; s < 4; ++s)
#pragma unroll
    for (int t = 0; t < 2; ++t) z[t][s] = lds128(lds + (16 * t + l15) * 256 + (((4 * s + g) ^ l15) << 4));
  SB();
#pragma unroll
  for (int t = 0; t < 2; ++t)
#pragma unroll
    for (int s = 0; s < 4; ++s) { ar[t] = mf16(z[t][s], F64r[s], s ? ar[t] : Z4); ai[t] = mf16(z[t][s], F64i[s], s ? ai[t] : Z4); }
#pragma unroll
  for (int t = 0; t < 2; ++t)
#pragma unroll
    for (int j = 0; j < 4; ++j) T[t][j] = pk(ar[t][j] * t1c[t][j] + ai[t][j] * t1s[t][j], ai[t][j] * t1c[t][j] - ar[t][j] * t1s[t][j]);
#pragma unroll
  for (int m = 0; m < 2; ++m)
#pragma unroll
    for (int t = 0; t < 2; ++t) { ar[m] = mf16(F32r[m][t], T[t], t ? ar[m] : Z4); ai[m] = mf16(F32i[m][t], T[t], t ? ai[m] : Z4); }
#pragma unroll
  for (int m = 0; m < 2; ++m)
#pragma unroll
    for (int j = 0; j < 4; ++j) { float a = lo(fw[m][j]), b = hi(fw[m][j]); Y[m][j] = pk(ar[m][j] * a - ai[m][j] * b, -(ar[m][j] * b) - ai[m][j] * a); }
#pragma unroll
  for (int t = 0; t < 2; ++t)
#pragma unroll
    for (int m = 0; m < 2; ++m) { ar[t] = mf16(Y[m], F32r[t][m], m ? ar[t] : Z4); ai[t] = mf16(Y[m], F32i[t][m], m ? ai[t] : Z4); }
#pragma unroll
  for (int t = 0; t < 2; ++t) {
    u4 v;
#pragma unroll
    for (int j = 0; j < 4; ++j) v[j] = pk(ar[t][j] * t2c[t][j] + ai[t][j] * t2s[t][j], ai[t][j] * t2c[t][j] - ar[t][j] * t2s[t][j]);
    *(u4*)(lds + 8192 + (16 * t + l15) * 256 + (((4 * w + g) ^ l15) << 4)) = v;
  }
  __syncthreads();
#pragma unroll
  for (int s = 0; s < 4; ++s)
#pragma unroll
    for (int t = 0; t < 2; ++t) z[t][s] = lds128(lds + 8192 + (16 * t + l15) * 256 + (((4 * s + g) ^ l15) << 4));
  SB();
#pragma unroll
  for (int t = 0; t < 2; ++t)
#pragma unroll
    for (int s = 0; s < 4; ++s) { ar[t] = mf16(F64r[s], z[t][s], s ? ar[t] : Z4); ai[t] = mf16(F64i[s], z[t][s], s ? ai[t] : Z4); }
  const float sg = col == 0 ? 0.f : -1.f;
#pragma unroll
  for (int t = 0; t < 2; ++t) {
    float re[4], im[4];
#pragma unroll
    for (int j = 0; j < 4; ++j) { re[j] = lo(xr[t][j]) + ar[t][j]; im[j] = hi(xr[t][j]) + (col == 0 ? 0.f : sg * ai[t][j]); }
    u32* o = xout + col * (2 * PS) + 32 * (16 * t + l15) + 8 * w + 2 * g;
    *(u2*)o = u2{pk(re[0], re[1]), pk(re[2], re[3])};
    *(u2*)(o + PS) = u2{pk(im[0], im[1]), pk(im[2], im[3])};
  }
  STAMP(KI, 3); STAMPW(KI, 4);
}

template <int LAST>
__global__ __launch_bounds__(512) void k_up(const u32* __restrict__ xh, const u32* __restrict__ wgu,
                                               u32* __restrict__ hb, u32* __restrict__ x3, float* __restrict__ rs2) {
  constexpr int NT = 6 + LAST, CH = NT * 4096 + 16384, NS = LAST ? 3 : 4, NI = NT + 4, PRE = UPPRE;
  __shared__ __attribute__((aligned(1024))) char lds[NS * CH];
  const int xc = blockIdx.x & 7, jb = blockIdx.x >> 3, mt = 8 * (xc & 1) + (jb & 7), ng = 4 * (xc >> 1) + (jb >> 3);
  const int t0 = (ng * 43) >> 3, t1 = ((ng + 1) * 43) >> 3;
  const int lane = threadIdx.x & 63, w8 = __builtin_amdgcn_readfirstlane(threadIdx.x >> 6), w = w8 & 3;
  const int wm = w >> 1, wn = w & 1, r = lane & 31, h = lane >> 5;
  constexpr int KI = LAST ? 5 : 2;
  STAMP(KI, 0);
  if (w8 >= 4) {
    const rsrc rq = mkr(xh), rp = mkr(wgu), rr = mkr((const char*)&R1T + 131072);
    const int vq = (lane >> 4) * (PS * 4) + (((lane & 15) ^ (4 * (lane >> 4))) << 4);
    auto issue = [&](int c, char* sl) {
#pragma unroll
      for (int i = 0; i < 4; ++i) blds(rq, vq, (64 * c + 16 * w + 4 * i) * (PS * 4) + 256 * mt, sl + NT * 4096 + (4 * w + i) * 1024);
#pragma unroll
      for (int i = 0; i < 3; ++i) {
        int e = 6 * w + 2 * i, so = (t0 + (e >> 2)) * 32768 + (4 * c + (e & 3)) * 1024;
        blds(rp, lane * 16, so, sl + e * 1024); blds<1024>(rp, lane * 16, so, sl + e * 1024);
      }
      if (LAST) blds(rr, lane * 16, (ng * 32 + 4 * c + w) * 1024, sl + (24 + w) * 1024);
    };
#pragma unroll
    for (int c = 0; c < PRE; ++c) issue(c, lds + c * CH);
    waitv<(PRE - 1) * NI>(); bar();
#pragma unroll
    for (int c = PRE; c < NS; ++c) issue(c, lds + c * CH);
    const int sOff = NT * 4096 + lane * 256 + ((ng ^ (4 * (lane & 3))) << 4) + 4 * w;
    u32 v;
    asm volatile("ds_read_b32 %0, %1\n s_waitcnt lgkmcnt(0)" : "=v"(v) : "v"((u32)(size_t)(LDSP char*)(lds + sOff)) : "memory");
    v = lane == 1 ? 0u : v;
    float s0 = lo(v) * lo(v), s1 = hi(v) * hi(v);
    int so = 0;
#pragma unroll 1
    for (int c = 0; c < 8; ++c) {
      if (c < 9 - NS) waitv<(NS - 2) * NI>(); else if (NS == 4 && c == 5) waitv<NI>(); else waitv<0>();
      bar();
      if (c < 8 - NS) issue(c + NS, lds + so);
      so = so == (NS - 1) * CH ? 0 : so + CH;
      if (c < 7) {
        asm volatile("ds_read_b32 %0, %1\n s_waitcnt lgkmcnt(0)" : "=v"(v) : "v"((u32)(size_t)(LDSP char*)(lds + so + sOff)) : "memory");
        s0 += lo(v) * lo(v); s1 += hi(v) * hi(v);
      }
    }
#pragma unroll
    for (int o = 1; o < 64; o <<= 1) { s0 += __shfl_xor(s0, o); s1 += __shfl_xor(s1, o); }
    if (lane < 2) rs2[128 * mt + 8 * ng + 2 * w + lane] = 1.f / ((lane ? s1 : s0) * (1.f / 512) + EPS);
    return;
  }
  f16v acc[3][2] = {}, accx = {};
  const int pOff = wn * 12288 + lane * 16;
  int qOff[2];
#pragma unroll
  for (int b = 0; b < 2; ++b)
    qOff[b] = NT * 4096 + (4 * h + ((lane >> 2) & 3)) * 256 + (((4 * (2 * wm + (b ^ wn)) + ((lane >> 3) & 2) + ((lane >> 1) & 1)) ^ (lane & 12)) << 4) + 8 * (lane & 1);
  struct Fr { u4 P[3], Q[2], X; };
  auto rd = [&](const char* sl, int ks) {
    Fr f;
#pragma unroll
    for (int b = 0; b < 2; ++b) f.Q[b] = ldtr(sl + qOff[b] + ks * 4096, 2048);
#pragma unroll
    for (int t = 0; t < 3; ++t) f.P[t] = lds128(sl + pOff + t * 4096 + ks * 1024);
    if (LAST) f.X = lds128(sl + 24576 + ks * 1024 + lane * 16);
    return f;
  };
  auto mm = [&](const Fr& f, int ks) {
#pragma unroll
    for (int t = 0; t < 3; ++t) { acc[t][0] = mf32(f.P[t], f.Q[0], acc[t][0]); acc[t][1] = mf32(f.P[t], f.Q[1], acc[t][1]); }
    if (LAST) accx = mf32(f.X, f.Q[0], accx);
    mix<6 + LAST, 2, 1>();
    SB();
  };
  bar();
  STAMP(KI, 1);
  Fr f0 = rd(lds, 0), f1;
  int so = 0;
#pragma unroll 1
  for (int c = 0; c < 8; ++c) {
    char* sl = lds + so;
    so = so == (NS - 1) * CH ? 0 : so + CH;
    SB();
    f1 = rd(sl, 1); mm(f0, 0);
    f0 = rd(sl, 2); mm(f1, 1);
    f1 = rd(sl, 3); mm(f0, 2);
    bar();
    SB();
    f0 = rd(lds + so, 0); mm(f1, 3);
  }
  STAMP(KI, 3);
#pragma unroll
  for (int t = 0; t < 3; ++t) {
    int tile = t0 + 3 * wn + t;
    if (tile < t1)
#pragma unroll
      for (int b = 0; b < 2; ++b) {
        const f16v& a = acc[t][b];
        u4 o;
#pragma unroll
        for (int j = 0; j < 4; ++j) { int i = 8 * (j >> 1) + 2 * (j & 1); o[j] = pk(fmaxf(a[i], 0.f) * a[i + 4], fmaxf(a[i + 1], 0.f) * a[i + 5]); }
        *(u4*)(hb + (((2 * mt + wm) * 2 + (b ^ wn)) * 86 + tile) * 256 + lane * 4) = o;
      }
  }
  if (LAST) {
    u32* o = x3 + ((4 * mt + w) * 16 + ng) * 512 + lane * 4;
    *(u4*)o = pack8(accx, 0); *(u4*)(o + 256) = pack8(accx, 1);
  }
  STAMP(KI, 4); STAMPW(KI, 5);
}

template <int LAST>
__global__ __launch_bounds__(512) void k_down(const u32* __restrict__ hb, const u32* __restrict__ wd, const float* __restrict__ rs2,
                                              const u32* __restrict__ res, u32* __restrict__ out, float* __restrict__ ssp) {
  __shared__ __attribute__((aligned(1024))) char lds[131072 + 512];
  const int xc = blockIdx.x & 7, jb = blockIdx.x >> 3, rg = 8 * (xc & 3) + (jb & 7), nt = 4 * (xc >> 2) + (jb >> 3);
  const int lane = threadIdx.x & 63, w8 = __builtin_amdgcn_readfirstlane(threadIdx.x >> 6), w = w8 & 3, r = lane & 31, h = lane >> 5;
  constexpr int KI = LAST ? 6 : 3;
  STAMP(KI, 0);
  if (w8 >= 4) {
    const rsrc rs = mkr(w < 2 ? wd + (2 * nt + w) * 86 * 256 : hb + (2 * rg + w - 2) * 86 * 256);
    auto issue = [&](int c) {
      char* sl = lds + (c & 3) * 32768 + w * 8192;
      unroll<4>([&](auto I) { blds<I.v * 1024>(rs, lane * 16, c * 8192, sl); });
      unroll<2>([&](auto I) { blds<I.v * 1024>(rs, lane * 16, c * 8192 + 4096, sl + 4096); });
      if (c < 10) unroll<2>([&](auto I) { blds<I.v * 1024 + 2048>(rs, lane * 16, c * 8192 + 4096, sl + 4096); });
    };
    issue(0); issue(1); issue(2);
    waitv<16>(); bar();
    issue(3);
#pragma unroll 1
    for (int c = 0; c < 10; ++c) {
      if (c < 7) waitv<16>(); else if (c == 7) waitv<14>(); else if (c == 8) waitv<6>(); else waitv<0>();
      bar();
      if (c < 7) issue(c + 4);
    }
    if (!LAST) bar();
    return;
  }
  const int t = w >> 1, b = w & 1, rho = 64 * rg + 32 * b + r;
  u32 rv[8];
  if (LAST) {
    const u32* p = res + ((2 * rg + b) * 16 + 2 * nt + t) * 512 + lane * 4;
    u4 x = *(const u4*)p, y = *(const u4*)(p + 256);
#pragma unroll
    for (int j = 0; j < 4; ++j) { rv[j] = x[j]; rv[4 + j] = y[j]; }
  } else {
    const u16* p = (const u16*)res + (64 * nt + 32 * t + 4 * h) * (2 * PS) + rho;
#pragma unroll
    for (int j = 0; j < 8; ++j) { const u16* e = p + (8 * (j >> 1) + 2 * (j & 1)) * (2 * PS); rv[j] = (u32)e[0] | ((u32)e[2 * PS] << 16); }
  }
  const float rs = rs2[rho];
  struct Fr { u4 A[4], B[4]; };
  const int aOff = t * 8192 + lane * 16, bOff = 16384 + b * 8192 + lane * 16;
  auto rd = [&](const char* sl, auto N) {
    Fr f;
#pragma unroll
    for (int k = 0; k < N.v; ++k) { f.A[k] = lds128(sl + aOff + k * 1024); f.B[k] = lds128(sl + bOff + k * 1024); }
    return f;
  };
  f16v acc = {};
  auto mm = [&](const Fr& f, auto N) {
#pragma unroll
    for (int k = 0; k < N.v; ++k) acc = mf32(f.A[k], f.B[k], acc);
    mix<N.v, 2, 1>();
    SB();
  };
  bar();
  STAMP(KI, 1);
  Fr f0 = rd(lds, IC<4>{}), f1;
#pragma unroll 1
  for (int c = 0; c < 10; ++c) {
    SB();
    f1 = rd(lds + (c & 3) * 32768 + 4096, IC<4>{}); mm(f0, IC<4>{});
    bar();
    SB();
    f0 = rd(lds + ((c + 1) & 3) * 32768, IC<4>{}); mm(f1, IC<4>{});
  }
  f1 = rd(lds + 2 * 32768 + 4096, IC<2>{}); mm(f0, IC<4>{});
  mm(f1, IC<2>{});
  STAMP(KI, 2);
  u32 v[8];
#pragma unroll
  for (int j = 0; j < 8; ++j) v[j] = pk(lo(rv[j]) + rs * acc[2 * j], hi(rv[j]) + rs * acc[2 * j + 1]);
  if (LAST) {
    u32* o = out + (32 * (32 * b + r) + rg) * 256 + 32 * nt + 16 * t + 8 * h;
    *(u4*)o = u4{v[0], v[1], v[2], v[3]}; *(u4*)(o + 4) = u4{v[4], v[5], v[6], v[7]};
  } else {
    const int c0 = 32 * nt + 16 * t + 2 * h;
    float ss = 0.f;
#pragma unroll
    for (int j = 0; j < 8; ++j) {
      out[(c0 + 4 * (j >> 1) + (j & 1)) * XI + rho] = v[j];
      u32 m = (j == 0 && c0 == 0) ? (v[j] & 0xffffu) : v[j];
      ss = dot2(m, m, ss);
    }
    ss += __shfl_xor(ss, 32);
    float* sl = (float*)(lds + 131072);
    if (h == 0) sl[w * 32 + r] = ss;
    bar();
    if (w == 0) ssp[nt * SS + 64 * rg + lane] = sl[lane] + sl[64 + lane];
  }
  STAMP(KI, 3); STAMPW(KI, 4);
}

extern "C" void kernel_launch(void* const* d_in, const int* in_sizes, int n_in, void* d_out, int out_size, void* d_ws, size_t ws_size,
                              hipStream_t stream) {
  const u16 *x = (const u16*)d_in[0], *wr = (const u16*)d_in[1], *wi = (const u16*)d_in[2];
  const u16 *wg = (const u16*)d_in[4], *wu = (const u16*)d_in[5], *wd = (const u16*)d_in[6];
  char* ws = (char*)d_ws;
  const size_t MB = 1 << 20;
  u32 *xh0 = (u32*)ws, *xh1 = (u32*)(ws + 3 * MB), *xh2 = (u32*)(ws + 6 * MB), *xh3 = (u32*)(ws + 9 * MB);
  u32 *filt = (u32*)(ws + 12 * MB), *x3 = (u32*)(ws + 15 * MB), *wgu = (u32*)(ws + 17 * MB), *wd1 = (u32*)(ws + 20 * MB);
  u32 *wd2 = (u32*)(ws + 22 * MB), *hb = (u32*)(ws + 24 * MB), *hb2 = (u32*)(ws + 30 * MB);
  float *rstd = (float*)(ws + 36 * MB), *ssp = rstd + 4096, *rs2 = rstd + 32768, *rs3 = rstd + 65536;
  k_prep<<<260, 512, 0, stream>>>(x, wr, wi, wg, wu, wd, xh0, (u32*)rstd, wgu, wd1, wd2, filt);
  k_fft<1><<<256, 256, 0, stream>>>(xh0, rstd, filt, xh1);
  k_up<0><<<256, 512, 0, stream>>>(xh1, wgu, hb, x3, rs2);
  k_down<0><<<256, 512, 0, stream>>>(hb, wd1, rs2, xh1, xh2, ssp);
  k_fft<8><<<256, 256, 0, stream>>>(xh2, ssp, filt, xh3);
  k_up<1><<<256, 512, 0, stream>>>(xh3, wgu, hb2, x3, rs3);
  k_down<1><<<256, 512, 0, stream>>>(hb2, wd2, rs3, x3, (u32*)d_out, ssp);
}
```

```cpp
#include <hip/hip_runtime.h>
#include <hip/hip_bf16.h>

typedef unsigned u32;
typedef unsigned short u16;
typedef __attribute__((ext_vector_type(8))) __bf16 bf8;
typedef __attribute__((ext_vector_type(2))) __bf16 bf2;
typedef __attribute__((ext_vector_type(2))) float f2;
typedef __attribute__((ext_vector_type(4))) float f4;
typedef __attribute__((ext_vector_type(16))) float f16v;
typedef __attribute__((ext_vector_type(2))) u32 u2;
typedef __attribute__((ext_vector_type(4))) u32 u4;
typedef __attribute__((ext_vector_type(4))) short s4;
typedef u4 __attribute__((aligned(2))) u4_ua;
#define DI __device__ __forceinline__
#define LDSP __attribute__((address_space(3)))
#define GLBP __attribute__((address_space(1)))

#define STAMP(k, j)
#define STAMPW(k, j)
#define ROT 0
#define UPPRE 2
constexpr int SS = 2048;
constexpr int XI = 2112, PS = 1088, FS = 2080;
constexpr float EPS = 1e-5f;

struct Part { u16 v[16384]; };
#if defined(__HIP_DEVICE_COMPILE__)
constexpr double kPi = 3.14159265358979323846;
constexpr double csin(double x) { double t = x, s = x; for (int i = 1; i < 14; ++i) { t *= -x * x / ((2 * i) * (2 * i + 1)); s += t; } return s; }
constexpr u16 tobf(double x) { float f = (float)x; u32 u = __builtin_bit_cast(u32, f); u += 0x7fffu + ((u >> 16) & 1u); return (u16)(u >> 16); }
struct CosT { u16 v[512]; };
constexpr CosT mkcos() {
  CosT c{};
  for (int i = 0; i < 512; ++i) {
    int q = (i + 128) & 511;
    int y = q <= 128 ? q : (q <= 384 ? 256 - q : q - 512);
    c.v[i] = y == 0 ? (u16)0 : tobf(csin(2 * kPi * y / 512) / 16.0);
  }
  return c;
}
constexpr CosT kCos = mkcos();
constexpr u16 kDC = tobf(0.04419417382415922);
constexpr Part mkR(int pi) {
  Part p{};
  for (int e = 0; e < 16384; ++e) {
    int r = (e >> 3) & 31, h = (e >> 8) & 1, j = e & 7, d = 0, s = 0;
    if (pi < 4) { d = (((e >> 9) & 7) << 4) + 8 * h + j; s = 32 * (4 * pi + (e >> 12)) + r; }
    else { d = 32 * (pi - 4) + (r & 3) + 4 * (r >> 3) + 16 * ((r >> 2) & 1); s = ((e >> 9) << 4) + 8 * (j >> 2) + 4 * h + (j & 3); }
    p.v[e] = s >= 2 ? kCos.v[((s >> 1) * d + ((s & 1) << 7)) & 511] : (u16)(kDC | ((s == 0 && (d & 1)) ? 0x8000 : 0));
  }
  return p;
}
template <int I> struct PR { static constexpr Part v = mkR(I); };
struct Tab { Part p[20]; };
__device__ const __attribute__((aligned(1024))) Tab R1T = {{PR<0>::v, PR<1>::v, PR<2>::v, PR<3>::v, PR<4>::v, PR<5>::v, PR<6>::v, PR<7>::v,
  PR<8>::v, PR<9>::v, PR<10>::v, PR<11>::v, PR<12>::v, PR<13>::v, PR<14>::v, PR<15>::v, PR<16>::v, PR<17>::v, PR<18>::v, PR<19>::v}};
#else
struct Tab { u16 p[8]; };
__device__ const Tab R1T = {};
#endif

DI f16v mf32(u4 a, u4 b, f16v c) { return __builtin_amdgcn_mfma_f32_32x32x16_bf16(__builtin_bit_cast(bf8, a), __builtin_bit_cast(bf8, b), c, 0, 0, 0); }
DI f4 mf16(u4 a, u4 b, f4 c) { return __builtin_amdgcn_mfma_f32_16x16x32_bf16(__builtin_bit_cast(bf8, a), __builtin_bit_cast(bf8, b), c, 0, 0, 0); }
DI u32 pk(float a, float b) { f2 v = {a, b}; return __builtin_bit_cast(u32, __builtin_convertvector(v, bf2)); }
DI float lo(u32 u) { return __builtin_bit_cast(float, u << 16); }
DI float hi(u32 u) { return __builtin_bit_cast(float, u & 0xffff0000u); }
DI float dot2(u32 a, u32 b, float c) { return __builtin_amdgcn_fdot2_f32_bf16(__builtin_bit_cast(bf2, a), __builtin_bit_cast(bf2, b), c, false); }
DI void glds(const void* g, char* l) { __builtin_amdgcn_global_load_lds((const GLBP void*)g, (LDSP void*)l, 16, 0, 0); }
typedef __amdgpu_buffer_rsrc_t rsrc;
DI rsrc mkr(const void* p) { return __builtin_amdgcn_make_buffer_rsrc((void*)p, 0, 0x7fffffff, 0x00020000); }
template <int O = 0> DI void blds(rsrc r, int vo, int so, char* l) { __builtin_amdgcn_raw_ptr_buffer_load_lds(r, (LDSP void*)l, 16, vo, so, O, 0); }
DI u4 ldtr(const char* a, int d) {
  u2 x = __builtin_bit_cast(u2, __builtin_amdgcn_ds_read_tr16_b64_v4i16((LDSP s4*)a));
  u2 y = __builtin_bit_cast(u2, __builtin_amdgcn_ds_read_tr16_b64_v4i16((LDSP s4*)(a + d)));
  return u4{x[0], x[1], y[0], y[1]};
}
#define WT 0
DI void st4(u32* p, u4 v) {
  if (WT == 2) asm volatile("global_store_dwordx4 %0, %1, off sc0 sc1\n s_nop 1" ::"v"(p), "v"(v) : "memory");
  else if (WT == 1) __builtin_nontemporal_store(v, (u4*)p);
  else *(u4*)p = v;
}
DI void st2(u32* p, u2 v) {
  if (WT == 2) asm volatile("global_store_dwordx2 %0, %1, off sc0 sc1" ::"v"(p), "v"(v) : "memory");
  else if (WT == 1) __builtin_nontemporal_store(v, (u2*)p);
  else *(u2*)p = v;
}
DI void st1(u32* p, u32 v) {
  if (WT == 2) asm volatile("global_store_dword %0, %1, off sc0 sc1" ::"v"(p), "v"(v) : "memory");
  else if (WT == 1) __builtin_nontemporal_store(v, p);
  else *p = v;
}
DI u4 lds128(const char* p) { return *(const u4*)p; }
DI u4 pack8(const f16v& a, int s) { u4 o = {pk(a[8 * s], a[8 * s + 1]), pk(a[8 * s + 2], a[8 * s + 3]), pk(a[8 * s + 4], a[8 * s + 5]), pk(a[8 * s + 6], a[8 * s + 7])}; return o; }
template <int N> DI void waitv() { asm volatile("s_waitcnt vmcnt(%0)" ::"n"(N) : "memory"); }
DI void bar() { asm volatile("s_waitcnt lgkmcnt(0)" ::: "memory"); __builtin_amdgcn_s_barrier(); asm volatile("" ::: "memory"); }
DI int dperm(int m) { return (m & 3) + 4 * (m >> 3) + 16 * ((m >> 2) & 1); }
DI int fperm(int m) { int i = (m & 3) + 4 * (m >> 3); return 16 * (i >> 3) + 8 * ((m >> 2) & 1) + (i & 7); }
#define SB() __builtin_amdgcn_sched_barrier(0)
#define SGB(m, n) __builtin_amdgcn_sched_group_barrier(m, n, 0)
template <int NM, int ND, int NV> DI void mix() { if constexpr (NM > 0) { SGB(0x008, 1); SGB(0x100, ND); SGB(0x002, NV); mix<NM - 1, ND, NV>(); } }
template <int I> struct IC { static constexpr int v = I; };
template <int N, class F> DI void unroll(F&& f) { if constexpr (N > 0) { unroll<N - 1>(f); f(IC<N - 1>{}); } }

template <int JOB>
DI void prep_gemm(const u16* __restrict__ q0, const u16* __restrict__ q1, int qt, int nt, int c0, char* lds,
                  u32* __restrict__ o0, u32* __restrict__ o1) {
  const int lane = threadIdx.x & 63, w8 = __builtin_amdgcn_readfirstlane(threadIdx.x >> 6), w = w8 & 3;
  const int wp = w >> 1, wq = w & 1, r = lane & 31, h = lane >> 5;
  if (w8 >= 4) {
    const rsrc rp = mkr(&R1T), ra = mkr(q0), rb = mkr(q1);
    int qv[4];
#pragma unroll
    for (int i = 0; i < 4; ++i) {
      if (JOB == 1) {
        int d = 16 * w + 8 * (i >> 1) + (lane >> 3), pos = lane & 7;
        int f = 64 * qt + 16 * ((pos >> 1) ^ (d & 3) ^ (i & 1)) + 8 * (pos & 1);
        qv[i] = (d * 1376 + (f > 1368 ? 1368 : f)) * 2;
      } else {
        int u = 32 * w + 8 * i + (lane >> 3), row;
        if (JOB == 0) { int rho = 128 * qt + (u & 64) + 2 * (u & 31) + ((u >> 5) & 1); row = 32 * (rho & 63) + (rho >> 6); }
        else { row = 128 * qt + (u & 96) + fperm(u & 31); row = row > 1375 ? 1375 : row; }
        qv[i] = row * 1024 + (((lane & 7) ^ ((u >> 1) & 7)) << 4);
      }
    }
    constexpr int QSTEP = JOB == 1 ? 64 * 1376 * 2 : 128;
    auto issP = [&](int c) { unroll<4>([&](auto I) { blds<I.v * 1024>(rp, lane * 16, (4 * nt + w) * 8192 + c * 4096, lds + w * 8192 + c * 4096); }); };
    auto issue = [&](int cc) {
      const int c = (cc + c0) & 7;
#pragma unroll
      for (int i = 0; i < 4; ++i) blds((JOB == 1 && (i & 1)) ? rb : ra, qv[i], c * QSTEP, lds + 32768 + c * 16384 + (4 * w + i) * 1024);
    };
    issP(0); issP(1); issue(0); issue(1); issue(2);
    waitv<8>(); bar();
#pragma unroll 1
    for (int c = 3; c < 8; ++c) issue(c);
    unroll<8>([&](auto I) { waitv<(I.v < 6 ? 4 * (6 - I.v) : 0)>(); bar(); });
    if (JOB == 0) {
      const int u = 32 * nt + 8 * w + (lane >> 3);
      float s1 = 0.f, s2 = 0.f;
#pragma unroll
      for (int c = 0; c < 8; ++c) {
        u4 v = lds128(lds + 32768 + c * 16384 + u * 128 + (lane & 7) * 16);
#pragma unroll
        for (int j = 0; j < 4; ++j) { s1 = dot2(v[j], 0x3f803f80u, s1); s2 = dot2(v[j], v[j], s2); }
      }
#pragma unroll
      for (int o = 1; o < 8; o <<= 1) { s1 += __shfl_xor(s1, o); s2 += __shfl_xor(s2, o); }
      float m = s1 * (1.f / 512), var = fmaxf(s2 * (1.f / 512) - m * m, 0.f);
      if ((lane & 7) == 0) ((float*)o1)[128 * qt + (u & 64) + 2 * (u & 31) + ((u >> 5) & 1)] = __builtin_amdgcn_rsqf(var + EPS) * (1.f / 2048);
    }
    return;
  }
  f16v acc[2][2] = {}, acc2[2][2] = {};
  int qOff;
  if (JOB == 1) {
    int g = lane >> 4, qr = (lane >> 2) & 3, run = 4 * (g & 1) + (lane & 3), q = run >> 1;
    qOff = 32768 + (g >> 1) * 2048 + qr * 128 + (q & 1) * 1024 + (8 * (run & 1) + 4 * (q >> 1)) * 2;
  } else qOff = 32768 + (64 * wq + r) * 128;
  struct Fr { u4 v[2]; };
  int pOff; u32 sgn;
  auto turn = [&](int c) {
    int n = ((r >> 1) * (c >> 1)) & 3;
    pOff = wp * 16384 + (c & 1) * 4096 + (lane ^ (n & 1)) * 16;
    sgn = (((r & 1) ? n + 1 : n) & 2) ? 0x80008000u : 0u;
  };
  auto rdP = [&](int ks) { return Fr{{lds128(lds + pOff + ks * 1024), lds128(lds + pOff + 8192 + ks * 1024)}}; };
  auto rdQ = [&](const char* sl, int ks) {
    Fr f;
#pragma unroll
    for (int b = 0; b < 2; ++b) {
      if (JOB == 1) {
        f.v[b] = ldtr(sl + qOff + ks * 4096 + (((2 * wq + b) ^ ((lane >> 2) & 3) ^ ((lane >> 1) & 1)) << 5), 512);
      } else f.v[b] = lds128(sl + qOff + b * 4096 + (((2 * ks + h) ^ ((r >> 1) & 7)) << 4));
    }
    return f;
  };
  u4 Id[2];
  if (JOB == 2) {
#pragma unroll
    for (int k = 0; k < 2; ++k) {
      int e = dperm(r) - 16 * k - 8 * h;
#pragma unroll
      for (int j = 0; j < 4; ++j) Id[k][j] = (e == 2 * j ? 0x3f80u : 0u) | (e == 2 * j + 1 ? 0x3f800000u : 0u);
    }
  }
  auto mm = [&](const Fr& P, const Fr& Q, Fr& N, u32 sg, int c, int ks) {
#pragma unroll
    for (int t = 0; t < 2; ++t)
#pragma unroll
      for (int b = 0; b < 2; ++b) acc[t][b] = JOB == 2 ? mf32(Q.v[b], P.v[t], acc[t][b]) : mf32(P.v[t], Q.v[b], acc[t][b]);
    N.v[0] ^= sg; N.v[1] ^= sg;
    SGB(0x008, 1); SGB(0x100, 3); SGB(0x008, 1); SGB(0x100, 3); SGB(0x002, 4); SGB(0x008, 1); SGB(0x002, 4); SGB(0x008, 1);
    asm volatile("" : "+v"(N.v[0]), "+v"(N.v[1]));
    SB();
    if (JOB == 2) {
      if (c == 2 * nt + wp) {
#pragma unroll
        for (int b = 0; b < 2; ++b) acc2[ks >> 1][b] = mf32(Q.v[b], Id[ks & 1], acc2[ks >> 1][b]);
      }
    }
  };
  turn(c0);
  bar();
  STAMP(0, 2);
  Fr qa = rdQ(lds + c0 * 16384, 0), pa = rdP(0), pb = rdP(1), qb, pc, pd;
  pa.v[0] ^= sgn; pa.v[1] ^= sgn;
#pragma unroll 1
  for (int cc = 0; cc < 8; ++cc) {
    const int c = (cc + c0) & 7;
    const char* sl = lds + c * 16384;
    const u32 sg = sgn;
    SB();
    qb = rdQ(sl, 1); pc = rdP(2); mm(pa, qa, pb, sg, c, 0);
    qa = rdQ(sl, 2); pd = rdP(3); mm(pb, qb, pc, sg, c, 1);
    turn((c + 1) & 7);
    SB();
    qb = rdQ(sl, 3); pa = rdP(0); mm(pc, qa, pd, sg, c, 2);
    asm volatile("" ::: "memory"); __builtin_amdgcn_s_barrier(); asm volatile("" ::: "memory");
    SB();
    qa = rdQ(lds + ((c + 1) & 7) * 16384, 0); pb = rdP(1); mm(pd, qb, pa, sgn, c, 3);
  }
  STAMP(0, 3);
  if (JOB == 0) {
#pragma unroll
    for (int t = 0; t < 2; ++t)
#pragma unroll
      for (int i = 0; i < 16; i += 2) {
        int col = 64 * nt + 32 * wp + 16 * t + (((i & 3) + 8 * (i >> 2) + 4 * h) >> 1);
        u2 v = {pk(acc[t][0][i], acc[t][0][i + 1]), pk(acc[t][1][i], acc[t][1][i + 1])};
        st2(o0 + col * XI + 128 * qt + 64 * wq + 2 * r, v);
      }
  } else if (JOB == 1) {
#pragma unroll
    for (int b = 0; b < 2; ++b) {
      int tile = 4 * qt + 2 * wq + b;
      if (tile < 86)
#pragma unroll
        for (int t = 0; t < 2; ++t)
#pragma unroll
          for (int s = 0; s < 2; ++s) {
            int ks = 8 * nt + 4 * wp + 2 * t + s;
            u4 v = pack8(acc[t][b], s);
            if (ks == 0 && h == 0) v[0] &= 0xffffu;
            st4(o0 + (tile * 32 + ks) * 256 + lane * 4, v);
          }
    }
  } else {
#pragma unroll
    for (int b = 0; b < 2; ++b)
#pragma unroll
      for (int s = 0; s < 2; ++s) {
        int ks = 8 * qt + 4 * wq + 2 * b + s;
        if (ks < 86)
#pragma unroll
          for (int t = 0; t < 2; ++t) {
            int o = ((4 * nt + 2 * wp + t) * 86 + ks) * 256 + lane * 4;
            st4(o0 + o, pack8(acc[t][b], s));
            st4(o1 + o, pack8(acc2[t][b], s));
          }
      }
  }
}

__global__ __launch_bounds__(512) void k_prep(const u16* __restrict__ x, const u16* __restrict__ wr, const u16* __restrict__ wi,
    const u16* __restrict__ wg, const u16* __restrict__ wu, const u16* __restrict__ wd,
    u32* __restrict__ xh, u32* __restrict__ rstd, u32* __restrict__ wgu, u32* __restrict__ wd1, u32* __restrict__ wd2,
    u32* __restrict__ filt) {
  __shared__ __attribute__((aligned(1024))) char lds[163840];
  const int b = blockIdx.x, nt = (b >> 3) & 3, q = 8 * (b >> 5) + (b & 7), c0 = ROT;
  const int lane = threadIdx.x & 63, w = __builtin_amdgcn_readfirstlane(threadIdx.x >> 6);
  STAMP(0, 0);
  if (q < 16) prep_gemm<0>(x, x, q, nt, c0, lds, xh, rstd);
  else if (q < 38) prep_gemm<1>(wg, wu, q - 16, nt, 0, lds, wgu, wgu);
  else if (q < 49) prep_gemm<2>(wd, wd, q - 38, nt, c0, lds, wd1, wd2);
  else {
    const int u = b < 256 ? (q - 49) * 4 + nt : b - 196, k0 = 16 * (u >> 4) + 8 * (u & 1) + 256 * ((u >> 1) & 7);
    const rsrc rs = mkr(w < 4 ? wr : wi);
    const int so = (k0 + 64 * (w & 3)) * 514;
    unroll<4>([&](auto I) { blds<I.v * 1024>(rs, lane * 16, so, lds + w * 4160); });
    if (lane == 0) blds(rs, 0, so + 4096, lds + w * 4160 + 4096);
    blds(mkr((const char*)&R1T + 131072), lane * 16, (u * 8 + w) * 1024, lds + 40960 + w * 1024);
    waitv<0>(); bar();
    STAMP(0, 1);
#pragma unroll
    for (int it = 0; it < 4; ++it) {
      const int cs = 1 + 32 * w + 8 * it + (lane >> 3);
      const u16* p = (const u16*)(lds + (lane & 7) * 514) + cs;
      u4 v;
#pragma unroll
      for (int j = 0; j < 4; ++j) v[j] = (u32)p[j * 2080] | ((u32)p[(4 + j) * 2080] << 16);
      st4(filt + (cs & 255) * FS + (u >> 3) * 256 + (((u >> 1) & 3) * 16 + 8 * (u & 1) + (lane & 7)) * 4, v);
    }
  }
  STAMPW(0, q < 16 ? 4 : q < 38 ? 5 : q < 49 ? 6 : 7);
}

DI u4 rot(u4 v) { u4 o; for (int j = 0; j < 4; ++j) o[j] = __builtin_amdgcn_alignbit(v[j], v[j], 16) ^ 0x8000u; return o; }
template <int NP>
__global__ __launch_bounds__(256, 2) void k_fft(const u32* __restrict__ xin, const float* __restrict__ st, const u32* __restrict__ filt,
                                                u32* __restrict__ xout) {
  __shared__ __attribute__((aligned(256))) char lds[16384];
  const int col = blockIdx.x, tid = threadIdx.x, lane = tid & 63, w = tid >> 6, l15 = lane & 15, g = lane >> 4;
  constexpr int KI = NP == 1 ? 1 : 4;
  STAMP(KI, 0);
  const u32* xc = xin + col * XI;
  u4 xd[2], xr[2], fw[2];
  f4 sv[NP][2];
#pragma unroll
  for (int i = 0; i < 2; ++i) xd[i] = *(const u4*)(xc + 1024 * i + 4 * tid);
#pragma unroll
  for (int p = 0; p < NP; ++p)
#pragma unroll
    for (int i = 0; i < 2; ++i) sv[p][i] = *(const f4*)(st + p * SS + 1024 * i + 4 * tid);
#pragma unroll
  for (int i = 0; i < 2; ++i) {
    fw[i] = *(const u4*)(filt + col * FS + (2 * w + i) * 256 + lane * 4);
    xr[i] = *(const u4*)(xc + 64 * (16 * i + l15) + 16 * w + 4 * g);
  }
  SB();
  const int k1 = 16 * w + l15;
  const float fk1 = (float)k1, fl = (float)l15, fa = (float)(4 * g);
  u4 F64r[4], F64i[4], F32r[2][2], F32i[2][2];
  float t1c[2][4], t1s[2][4], t2c[2][4], t2s[2][4];
#pragma unroll
  for (int j = 0; j < 4; ++j) {
    float a = (fa + j) * fk1 * (1.f / 64), b = (fa + j) * fl * (1.f / 32);
    F64r[0][j] = pk(__builtin_amdgcn_cosf(a), __builtin_amdgcn_sinf(a));
    F32r[0][0][j] = pk(__builtin_amdgcn_cosf(b), __builtin_amdgcn_sinf(b));
  }
  F64i[0] = rot(F64r[0]); F32i[0][0] = rot(F32r[0][0]);
  const u32 NEG = 0x80008000u, so = (lane & 1) ? NEG : 0u, sr = (lane & 2) ? NEG : 0u, si = ((lane + 1) & 2) ? NEG : 0u;
#pragma unroll
  for (int j = 0; j < 4; ++j) {
    F64r[1][j] = ((lane & 1) ? F64i[0][j] : F64r[0][j]) ^ sr;
    F64i[1][j] = ((lane & 1) ? F64r[0][j] : F64i[0][j]) ^ si;
    F64r[2][j] = F64r[0][j] ^ so; F64i[2][j] = F64i[0][j] ^ so;
    F64r[3][j] = F64r[1][j] ^ so; F64i[3][j] = F64i[1][j] ^ so;
    const u32 sj = (j & 1) ? NEG : 0u;
    F32r[0][1][j] = F32r[0][0][j] ^ so; F32i[0][1][j] = F32i[0][0][j] ^ so;
    F32r[1][0][j] = F32r[0][0][j] ^ sj; F32i[1][0][j] = F32i[0][0][j] ^ sj;
    F32r[1][1][j] = F32r[0][1][j] ^ sj; F32i[1][1][j] = F32i[0][1][j] ^ sj;
  }
#pragma unroll
  for (int t = 0; t < 2; ++t)
#pragma unroll
    for (int j = 0; j < 4; ++j) {
      float a = (fa + (16 * t + j)) * (fk1 * (1.f / 2048)), b = ((fa + (float)(16 * w)) + j) * ((fl + 16 * t) * (1.f / 2048));
      t1c[t][j] = __builtin_amdgcn_cosf(a); t1s[t][j] = __builtin_amdgcn_sinf(a);
      t2c[t][j] = __builtin_amdgcn_cosf(b); t2s[t][j] = __builtin_amdgcn_sinf(b);
    }
  SB();
  STAMP(KI, 1);
  const u32 msk = col == 0 ? 0xffffu : 0xffffffffu;
#pragma unroll
  for (int i = 0; i < 2; ++i) {
    f4 rs = sv[0][i];
    if (NP > 1) {
#pragma unroll
      for (int p = 1; p < NP; ++p) rs += sv[p][i];
#pragma unroll
      for (int j = 0; j < 4; ++j) rs[j] = __builtin_amdgcn_rsqf(rs[j] * (1.f / 512) + EPS) * (1.f / 2048);
    }
    u4 z;
#pragma unroll
    for (int j = 0; j < 4; ++j) z[j] = pk(lo(xd[i][j]) * rs[j], hi(xd[i][j]) * rs[j]) & msk;
    *(u4*)(lds + (16 * i + (tid >> 4)) * 256 + ((l15 ^ (tid >> 4)) << 4)) = z;
  }
  __syncthreads();
  STAMP(KI, 2);
  u4 z[2][4], T[2], Y[2];
  f4 ar[2], ai[2];
  const f4 Z4 = {0, 0, 0, 0};
#pragma unroll
  for (int s = 0; s < 4; ++s)
#pragma unroll
    for (int t = 0; t < 2; ++t) z[t][s] = lds128(lds + (16 * t + l15) * 256 + (((4 * s + g) ^ l15) << 4));
  SB();
#pragma unroll
  for (int t = 0; t < 2; ++t)
#pragma unroll
    for (int s = 0; s < 4; ++s) { ar[t] = mf16(z[t][s], F64r[s], s ? ar[t] : Z4); ai[t] = mf16(z[t][s], F64i[s], s ? ai[t] : Z4); }
#pragma unroll
  for (int t = 0; t < 2; ++t)
#pragma unroll
    for (int j = 0; j < 4; ++j) T[t][j] = pk(ar[t][j] * t1c[t][j] + ai[t][j] * t1s[t][j], ai[t][j] * t1c[t][j] - ar[t][j] * t1s[t][j]);
#pragma unroll
  for (int m = 0; m < 2; ++m)
#pragma unroll
    for (int t = 0; t < 2; ++t) { ar[m] = mf16(F32r[m][t], T[t], t ? ar[m] : Z4); ai[m] = mf16(F32i[m][t], T[t], t ? ai[m] : Z4); }
#pragma unroll
  for (int m = 0; m < 2; ++m)
#pragma unroll
    for (int j = 0; j < 4; ++j) { float a = lo(fw[m][j]), b = hi(fw[m][j]); Y[m][j] = pk(ar[m][j] * a - ai[m][j] * b, -(ar[m][j] * b) - ai[m][j] * a); }
#pragma unroll
  for (int t = 0; t < 2; ++t)
#pragma unroll
    for (int m = 0; m < 2; ++m) { ar[t] = mf16(Y[m], F32r[t][m], m ? ar[t] : Z4); ai[t] = mf16(Y[m], F32i[t][m], m ? ai[t] : Z4); }
#pragma unroll
  for (int t = 0; t < 2; ++t) {
    u4 v;
#pragma unroll
    for (int j = 0; j < 4; ++j) v[j] = pk(ar[t][j] * t2c[t][j] + ai[t][j] * t2s[t][j], ai[t][j] * t2c[t][j] - ar[t][j] * t2s[t][j]);
    *(u4*)(lds + 8192 + (16 * t + l15) * 256 + (((4 * w + g) ^ l15) << 4)) = v;
  }
  __syncthreads();
#pragma unroll
  for (int s = 0; s < 4; ++s)
#pragma unroll
    for (int t = 0; t < 2; ++t) z[t][s] = lds128(lds + 8192 + (16 * t + l15) * 256 + (((4 * s + g) ^ l15) << 4));
  SB();
#pragma unroll
  for (int t = 0; t < 2; ++t)
#pragma unroll
    for (int s = 0; s < 4; ++s) { ar[t] = mf16(F64r[s], z[t][s], s ? ar[t] : Z4); ai[t] = mf16(F64i[s], z[t][s], s ? ai[t] : Z4); }
  const float sg = col == 0 ? 0.f : -1.f;
#pragma unroll
  for (int t = 0; t < 2; ++t) {
    float re[4], im[4];
#pragma unroll
    for (int j = 0; j < 4; ++j) { re[j] = lo(xr[t][j]) + ar[t][j]; im[j] = hi(xr[t][j]) + (col == 0 ? 0.f : sg * ai[t][j]); }
    u32* o = xout + col * (2 * PS) + 32 * (16 * t + l15) + 8 * w + 2 * g;
    *(u2*)o = u2{pk(re[0], re[1]), pk(re[2], re[3])};
    *(u2*)(o + PS) = u2{pk(im[0], im[1]), pk(im[2], im[3])};
  }
  STAMP(KI, 3); STAMPW(KI, 4);
}

template <int LAST>
__global__ __launch_bounds__(512) void k_up(const u32* __restrict__ xh, const u32* __restrict__ wgu,
                                               u32* __restrict__ hb, u32* __restrict__ x3, float* __restrict__ rs2) {
  constexpr int NT = 6 + LAST, CH = NT * 4096 + 16384, NS = LAST ? 3 : 4, NI = NT + 4, PRE = UPPRE;
  __shared__ __attribute__((aligned(1024))) char lds[NS * CH];
  const int xc = blockIdx.x & 7, jb = blockIdx.x >> 3, mt = 8 * (xc & 1) + (jb & 7), ng = 4 * (xc >> 1) + (jb >> 3);
  const int t0 = (ng * 43) >> 3, t1 = ((ng + 1) * 43) >> 3;
  const int lane = threadIdx.x & 63, w8 = __builtin_amdgcn_readfirstlane(threadIdx.x >> 6), w = w8 & 3;
  const int wm = w >> 1, wn = w & 1, r = lane & 31, h = lane >> 5;
  constexpr int KI = LAST ? 5 : 2;
  STAMP(KI, 0);
  if (w8 >= 4) {
    const rsrc rq = mkr(xh), rp = mkr(wgu), rr = mkr((const char*)&R1T + 131072);
    const int vq = (lane >> 4) * (PS * 4) + (((lane & 15) ^ (4 * (lane >> 4))) << 4);
    auto issue = [&](int c, char* sl) {
#pragma unroll
      for (int i = 0; i < 4; ++i) blds(rq, vq, (64 * c + 16 * w + 4 * i) * (PS * 4) + 256 * mt, sl + NT * 4096 + (4 * w + i) * 1024);
#pragma unroll
      for (int i = 0; i < 3; ++i) {
        int e = 6 * w + 2 * i, so = (t0 + (e >> 2)) * 32768 + (4 * c + (e & 3)) * 1024;
        blds(rp, lane * 16, so, sl + e * 1024); blds<1024>(rp, lane * 16, so, sl + e * 1024);
      }
      if (LAST) blds(rr, lane * 16, (ng * 32 + 4 * c + w) * 1024, sl + (24 + w) * 1024);
    };
#pragma unroll
    for (int c = 0; c < PRE; ++c) issue(c, lds + c * CH);
    waitv<(PRE - 1) * NI>(); bar();
#pragma unroll
    for (int c = PRE; c < NS; ++c) issue(c, lds + c * CH);
    const int sOff = NT * 4096 + lane * 256 + ((ng ^ (4 * (lane & 3))) << 4) + 4 * w;
    u32 v;
    asm volatile("ds_read_b32 %0, %1\n s_waitcnt lgkmcnt(0)" : "=v"(v) : "v"((u32)(size_t)(LDSP char*)(lds + sOff)) : "memory");
    v = lane == 1 ? 0u : v;
    float s0 = lo(v) * lo(v), s1 = hi(v) * hi(v);
    int so = 0;
#pragma unroll 1
    for (int c = 0; c < 8; ++c) {
      if (c < 9 - NS) waitv<(NS - 2) * NI>(); else if (NS == 4 && c == 5) waitv<NI>(); else waitv<0>();
      bar();
      if (c < 8 - NS) issue(c + NS, lds + so);
      so = so == (NS - 1) * CH ? 0 : so + CH;
      if (c < 7) {
        asm volatile("ds_read_b32 %0, %1\n s_waitcnt lgkmcnt(0)" : "=v"(v) : "v"((u32)(size_t)(LDSP char*)(lds + so + sOff)) : "memory");
        s0 += lo(v) * lo(v); s1 += hi(v) * hi(v);
      }
    }
#pragma unroll
    for (int o = 1; o < 64; o <<= 1) { s0 += __shfl_xor(s0, o); s1 += __shfl_xor(s1, o); }
    if (lane < 2) rs2[128 * mt + 8 * ng + 2 * w + lane] = 1.f / ((lane ? s1 : s0) * (1.f / 512) + EPS);
    return;
  }
  f16v acc[3][2] = {}, accx = {};
  const int pOff = wn * 12288 + lane * 16;
  int qOff[2];
#pragma unroll
  for (int b = 0; b < 2; ++b)
    qOff[b] = NT * 4096 + (4 * h + ((lane >> 2) & 3)) * 256 + (((4 * (2 * wm + (b ^ wn)) + ((lane >> 3) & 2) + ((lane >> 1) & 1)) ^ (lane & 12)) << 4) + 8 * (lane & 1);
  struct Fr { u4 P[3], Q[2], X; };
  auto rd = [&](const char* sl, int ks) {
    Fr f;
#pragma unroll
    for (int b = 0; b < 2; ++b) f.Q[b] = ldtr(sl + qOff[b] + ks * 4096, 2048);
#pragma unroll
    for (int t = 0; t < 3; ++t) f.P[t] = lds128(sl + pOff + t * 4096 + ks * 1024);
    if (LAST) f.X = lds128(sl + 24576 + ks * 1024 + lane * 16);
    return f;
  };
  auto mm = [&](const Fr& f, int ks) {
#pragma unroll
    for (int t = 0; t < 3; ++t) { acc[t][0] = mf32(f.P[t], f.Q[0], acc[t][0]); acc[t][1] = mf32(f.P[t], f.Q[1], acc[t][1]); }
    if (LAST) accx = mf32(f.X, f.Q[0], accx);
    mix<6 + LAST, 2, 1>();
    SB();
  };
  bar();
  STAMP(KI, 1);
  Fr f0 = rd(lds, 0), f1;
  int so = 0;
#pragma unroll 1
  for (int c = 0; c < 8; ++c) {
    char* sl = lds + so;
    so = so == (NS - 1) * CH ? 0 : so + CH;
    SB();
    f1 = rd(sl, 1); mm(f0, 0);
    f0 = rd(sl, 2); mm(f1, 1);
    f1 = rd(sl, 3); mm(f0, 2);
    bar();
    SB();
    f0 = rd(lds + so, 0); mm(f1, 3);
  }
  STAMP(KI, 3);
#pragma unroll
  for (int t = 0; t < 3; ++t) {
    int tile = t0 + 3 * wn + t;
    if (tile < t1)
#pragma unroll
      for (int b = 0; b < 2; ++b) {
        const f16v& a = acc[t][b];
        u4 o;
#pragma unroll
        for (int j = 0; j < 4; ++j) { int i = 8 * (j >> 1) + 2 * (j & 1); o[j] = pk(fmaxf(a[i], 0.f) * a[i + 4], fmaxf(a[i + 1], 0.f) * a[i + 5]); }
        st4(hb + (((2 * mt + wm) * 2 + (b ^ wn)) * 86 + tile) * 256 + lane * 4, o);
      }
  }
  if (LAST) {
    u32* o = x3 + ((4 * mt + w) * 16 + ng) * 512 + lane * 4;
    st4(o, pack8(accx, 0)); st4(o + 256, pack8(accx, 1));
  }
  STAMP(KI, 4); STAMPW(KI, 5);
}

template <int LAST>
__global__ __launch_bounds__(512) void k_down(const u32* __restrict__ hb, const u32* __restrict__ wd, const float* __restrict__ rs2,
                                              const u32* __restrict__ res, u32* __restrict__ out, float* __restrict__ ssp) {
  __shared__ __attribute__((aligned(1024))) char lds[131072 + 512];
  const int xc = blockIdx.x & 7, jb = blockIdx.x >> 3, rg = 8 * (xc & 3) + (jb & 7), nt = 4 * (xc >> 2) + (jb >> 3);
  const int lane = threadIdx.x & 63, w8 = __builtin_amdgcn_readfirstlane(threadIdx.x >> 6), w = w8 & 3, r = lane & 31, h = lane >> 5;
  constexpr int KI = LAST ? 6 : 3;
  STAMP(KI, 0);
  if (w8 >= 4) {
    const rsrc rs = mkr(w < 2 ? wd + (2 * nt + w) * 86 * 256 : hb + (2 * rg + w - 2) * 86 * 256);
    auto issue = [&](int c) {
      char* sl = lds + (c & 3) * 32768 + w * 8192;
      unroll<4>([&](auto I) { blds<I.v * 1024>(rs, lane * 16, c * 8192, sl); });
      unroll<2>([&](auto I) { blds<I.v * 1024>(rs, lane * 16, c * 8192 + 4096, sl + 4096); });
      if (c < 10) unroll<2>([&](auto I) { blds<I.v * 1024 + 2048>(rs, lane * 16, c * 8192 + 4096, sl + 4096); });
    };
    issue(0); issue(1); issue(2);
    waitv<16>(); bar();
    issue(3);
#pragma unroll 1
    for (int c = 0; c < 10; ++c) {
      if (c < 7) waitv<16>(); else if (c == 7) waitv<14>(); else if (c == 8) waitv<6>(); else waitv<0>();
      bar();
      if (c < 7) issue(c + 4);
    }
    if (!LAST) bar();
    return;
  }
  const int t = w >> 1, b = w & 1, rho = 64 * rg + 32 * b + r;
  u32 rv[8];
  if (LAST) {
    const u32* p = res + ((2 * rg + b) * 16 + 2 * nt + t) * 512 + lane * 4;
    u4 x = *(const u4*)p, y = *(const u4*)(p + 256);
#pragma unroll
    for (int j = 0; j < 4; ++j) { rv[j] = x[j]; rv[4 + j] = y[j]; }
  } else {
    const u16* p = (const u16*)res + (64 * nt + 32 * t + 4 * h) * (2 * PS) + rho;
#pragma unroll
    for (int j = 0; j < 8; ++j) { const u16* e = p + (8 * (j >> 1) + 2 * (j & 1)) * (2 * PS); rv[j] = (u32)e[0] | ((u32)e[2 * PS] << 16); }
  }
  const float rs = rs2[rho];
  struct Fr { u4 A[4], B[4]; };
  const int aOff = t * 8192 + lane * 16, bOff = 16384 + b * 8192 + lane * 16;
  auto rd = [&](const char* sl, auto N) {
    Fr f;
#pragma unroll
    for (int k = 0; k < N.v; ++k) { f.A[k] = lds128(sl + aOff + k * 1024); f.B[k] = lds128(sl + bOff + k * 1024); }
    return f;
  };
  f16v acc = {};
  auto mm = [&](const Fr& f, auto N) {
#pragma unroll
    for (int k = 0; k < N.v; ++k) acc = mf32(f.A[k], f.B[k], acc);
    mix<N.v, 2, 1>();
    SB();
  };
  bar();
  STAMP(KI, 1);
  Fr f0 = rd(lds, IC<4>{}), f1;
#pragma unroll 1
  for (int c = 0; c < 10; ++c) {
    SB();
    f1 = rd(lds + (c & 3) * 32768 + 4096, IC<4>{}); mm(f0, IC<4>{});
    bar();
    SB();
    f0 = rd(lds + ((c + 1) & 3) * 32768, IC<4>{}); mm(f1, IC<4>{});
  }
  f1 = rd(lds + 2 * 32768 + 4096, IC<2>{}); mm(f0, IC<4>{});
  mm(f1, IC<2>{});
  STAMP(KI, 2);
  u32 v[8];
#pragma unroll
  for (int j = 0; j < 8; ++j) v[j] = pk(lo(rv[j]) + rs * acc[2 * j], hi(rv[j]) + rs * acc[2 * j + 1]);
  if (LAST) {
    u32* o = out + (32 * (32 * b + r) + rg) * 256 + 32 * nt + 16 * t + 8 * h;
    *(u4*)o = u4{v[0], v[1], v[2], v[3]}; *(u4*)(o + 4) = u4{v[4], v[5], v[6], v[7]};
  } else {
    const int c0 = 32 * nt + 16 * t + 2 * h;
    float ss = 0.f;
#pragma unroll
    for (int j = 0; j < 8; ++j) {
      st1(out + (c0 + 4 * (j >> 1) + (j & 1)) * XI + rho, v[j]);
      u32 m = (j == 0 && c0 == 0) ? (v[j] & 0xffffu) : v[j];
      ss = dot2(m, m, ss);
    }
    ss += __shfl_xor(ss, 32);
    float* sl = (float*)(lds + 131072);
    if (h == 0) sl[w * 32 + r] = ss;
    bar();
    if (w == 0) ssp[nt * SS + 64 * rg + lane] = sl[lane] + sl[64 + lane];
  }
  STAMP(KI, 3); STAMPW(KI, 4);
}

extern "C" void kernel_launch(void* const* d_in, const int* in_sizes, int n_in, void* d_out, int out_size, void* d_ws, size_t ws_size,
                              hipStream_t stream) {
  const u16 *x = (const u16*)d_in[0], *wr = (const u16*)d_in[1], *wi = (const u16*)d_in[2];
  const u16 *wg = (const u16*)d_in[4], *wu = (const u16*)d_in[5], *wd = (const u16*)d_in[6];
  char* ws = (char*)d_ws;
  const size_t MB = 1 << 20;
  u32 *xh0 = (u32*)ws, *xh1 = (u32*)(ws + 3 * MB), *xh2 = (u32*)(ws + 6 * MB), *xh3 = (u32*)(ws + 9 * MB);
  u32 *filt = (u32*)(ws + 12 * MB), *x3 = (u32*)(ws + 15 * MB), *wgu = (u32*)(ws + 17 * MB), *wd1 = (u32*)(ws + 20 * MB);
  u32 *wd2 = (u32*)(ws + 22 * MB), *hb = (u32*)(ws + 24 * MB), *hb2 = (u32*)(ws + 30 * MB);
  float *rstd = (float*)(ws + 36 * MB), *ssp = rstd + 4096, *rs2 = rstd + 32768, *rs3 = rstd + 65536;
  k_prep<<<260, 512, 0, stream>>>(x, wr, wi, wg, wu, wd, xh0, (u32*)rstd, wgu, wd1, wd2, filt);
  k_fft<1><<<256, 256, 0, stream>>>(xh0, rstd, filt, xh1);
  k_up<0><<<256, 512, 0, stream>>>(xh1, wgu, hb, x3, rs2);
  k_down<0><<<256, 512, 0, stream>>>(hb, wd1, rs2, xh1, xh2, ssp);
  k_fft<8><<<256, 256, 0, stream>>>(xh2, ssp, filt, xh3);
  k_up<1><<<256, 512, 0, stream>>>(xh3, wgu, hb2, x3, rs3);
  k_down<1><<<256, 512, 0, stream>>>(hb2, wd2, rs3, x3, (u32*)d_out, ssp);
}
```

```cpp
#include <hip/hip_runtime.h>
#include <hip/hip_bf16.h>

typedef unsigned u32;
typedef unsigned short u16;
typedef __attribute__((ext_vector_type(8))) __bf16 bf8;
typedef __attribute__((ext_vector_type(2))) __bf16 bf2;
typedef __attribute__((ext_vector_type(2))) float f2;
typedef __attribute__((ext_vector_type(4))) float f4;
typedef __attribute__((ext_vector_type(16))) float f16v;
typedef __attribute__((ext_vector_type(2))) u32 u2;
typedef __attribute__((ext_vector_type(4))) u32 u4;
typedef __attribute__((ext_vector_type(4))) short s4;
typedef u4 __attribute__((aligned(2))) u4_ua;
#define DI __device__ __forceinline__
#define LDSP __attribute__((address_space(3)))
#define GLBP __attribute__((address_space(1)))

#define STAMP(k, j)
#define STAMPW(k, j)
#define ROT 0
#define UPPRE 2
constexpr int SS = 2048;
constexpr int XI = 2112, PS = 1088, FS = 2080;
constexpr float EPS = 1e-5f;

struct Part { u16 v[16384]; };
#if defined(__HIP_DEVICE_COMPILE__)
constexpr double kPi = 3.14159265358979323846;
constexpr double csin(double x) { double t = x, s = x; for (int i = 1; i < 14; ++i) { t *= -x * x / ((2 * i) * (2 * i + 1)); s += t; } return s; }
constexpr u16 tobf(double x) { float f = (float)x; u32 u = __builtin_bit_cast(u32, f); u += 0x7fffu + ((u >> 16) & 1u); return (u16)(u >> 16); }
struct CosT { u16 v[512]; };
constexpr CosT mkcos() {
  CosT c{};
  for (int i = 0; i < 512; ++i) {
    int q = (i + 128) & 511;
    int y = q <= 128 ? q : (q <= 384 ? 256 - q : q - 512);
    c.v[i] = y == 0 ? (u16)0 : tobf(csin(2 * kPi * y / 512) / 16.0);
  }
  return c;
}
constexpr CosT kCos = mkcos();
constexpr u16 kDC = tobf(0.04419417382415922);
constexpr Part mkR(int pi) {
  Part p{};
  for (int e = 0; e < 16384; ++e) {
    int r = (e >> 3) & 31, h = (e >> 8) & 1, j = e & 7, d = 0, s = 0;
    if (pi < 4) { d = (((e >> 9) & 7) << 4) + 8 * h + j; s = 32 * (4 * pi + (e >> 12)) + r; }
    else { d = 32 * (pi - 4) + (r & 3) + 4 * (r >> 3) + 16 * ((r >> 2) & 1); s = ((e >> 9) << 4) + 8 * (j >> 2) + 4 * h + (j & 3); }
    p.v[e] = s >= 2 ? kCos.v[((s >> 1) * d + ((s & 1) << 7)) & 511] : (u16)(kDC | ((s == 0 && (d & 1)) ? 0x8000 : 0));
  }
  return p;
}
template <int I> struct PR { static constexpr Part v = mkR(I); };
struct Tab { Part p[20]; };
__device__ const __attribute__((aligned(1024))) Tab R1T = {{PR<0>::v, PR<1>::v, PR<2>::v, PR<3>::v, PR<4>::v, PR<5>::v, PR<6>::v, PR<7>::v,
  PR<8>::v, PR<9>::v, PR<10>::v, PR<11>::v, PR<12>::v, PR<13>::v, PR<14>::v, PR<15>::v, PR<16>::v, PR<17>::v, PR<18>::v, PR<19>::v}};
#else
struct Tab { u16 p[8]; };
__device__ const Tab R1T = {};
#endif

DI f16v mf32(u4 a, u4 b, f16v c) { return __builtin_amdgcn_mfma_f32_32x32x16_bf16(__builtin_bit_cast(bf8, a), __builtin_bit_cast(bf8, b), c, 0, 0, 0); }
DI f4 mf16(u4 a, u4 b, f4 c) { return __builtin_amdgcn_mfma_f32_16x16x32_bf16(__builtin_bit_cast(bf8, a), __builtin_bit_cast(bf8, b), c, 0, 0, 0); }
DI u32 pk(float a, float b) { f2 v = {a, b}; return __builtin_bit_cast(u32, __builtin_convertvector(v, bf2)); }
DI float lo(u32 u) { return __builtin_bit_cast(float, u << 16); }
DI float hi(u32 u) { return __builtin_bit_cast(float, u & 0xffff0000u); }
DI float dot2(u32 a, u32 b, float c) { return __builtin_amdgcn_fdot2_f32_bf16(__builtin_bit_cast(bf2, a), __builtin_bit_cast(bf2, b), c, false); }
DI void glds(const void* g, char* l) { __builtin_amdgcn_global_load_lds((const GLBP void*)g, (LDSP void*)l, 16, 0, 0); }
typedef __amdgpu_buffer_rsrc_t rsrc;
DI rsrc mkr(const void* p) { return __builtin_amdgcn_make_buffer_rsrc((void*)p, 0, 0x7fffffff, 0x00020000); }
template <int O = 0> DI void blds(rsrc r, int vo, int so, char* l) { __builtin_amdgcn_raw_ptr_buffer_load_lds(r, (LDSP void*)l, 16, vo, so, O, 0); }
DI u4 ldtr(const char* a, int d) {
  u2 x = __builtin_bit_cast(u2, __builtin_amdgcn_ds_read_tr16_b64_v4i16((LDSP s4*)a));
  u2 y = __builtin_bit_cast(u2, __builtin_amdgcn_ds_read_tr16_b64_v4i16((LDSP s4*)(a + d)));
  return u4{x[0], x[1], y[0], y[1]};
}
#define WT 2
DI void st4(u32* p, u4 v) {
  if (WT == 2) asm volatile("global_store_dwordx4 %0, %1, off sc0 sc1\n s_nop 1" ::"v"(p), "v"(v) : "memory");
  else if (WT == 1) __builtin_nontemporal_store(v, (u4*)p);
  else *(u4*)p = v;
}
DI void st2(u32* p, u2 v) {
  if (WT == 2) asm volatile("global_store_dwordx2 %0, %1, off sc0 sc1" ::"v"(p), "v"(v) : "memory");
  else if (WT == 1) __builtin_nontemporal_store(v, (u2*)p);
  else *(u2*)p = v;
}
DI void st1(u32* p, u32 v) {
  if (WT == 2) asm volatile("global_store_dword %0, %1, off sc0 sc1" ::"v"(p), "v"(v) : "memory");
  else if (WT == 1) __builtin_nontemporal_store(v, p);
  else *p = v;
}
DI u4 lds128(const char* p) { return *(const u4*)p; }
DI u4 pack8(const f16v& a, int s) { u4 o = {pk(a[8 * s], a[8 * s + 1]), pk(a[8 * s + 2], a[8 * s + 3]), pk(a[8 * s + 4], a[8 * s + 5]), pk(a[8 * s + 6], a[8 * s + 7])}; return o; }
template <int N> DI void waitv() { asm volatile("s_waitcnt vmcnt(%0)" ::"n"(N) : "memory"); }
DI void bar() { asm volatile("s_waitcnt lgkmcnt(0)" ::: "memory"); __builtin_amdgcn_s_barrier(); asm volatile("" ::: "memory"); }
DI int dperm(int m) { return (m & 3) + 4 * (m >> 3) + 16 * ((m >> 2) & 1); }
DI int fperm(int m) { int i = (m & 3) + 4 * (m >> 3); return 16 * (i >> 3) + 8 * ((m >> 2) & 1) + (i & 7); }
#define SB() __builtin_amdgcn_sched_barrier(0)
#define SGB(m, n) __builtin_amdgcn_sched_group_barrier(m, n, 0)
template <int NM, int ND, int NV> DI void mix() { if constexpr (NM > 0) { SGB(0x008, 1); SGB(0x100, ND); SGB(0x002, NV); mix<NM - 1, ND, NV>(); } }
template <int I> struct IC { static constexpr int v = I; };
template <int N, class F> DI void unroll(F&& f) { if constexpr (N > 0) { unroll<N - 1>(f); f(IC<N - 1>{}); } }

template <int JOB>
DI void prep_gemm(const u16* __restrict__ q0, const u16* __restrict__ q1, int qt, int nt, int c0, char* lds,
                  u32* __restrict__ o0, u32* __restrict__ o1) {
  const int lane = threadIdx.x & 63, w8 = __builtin_amdgcn_readfirstlane(threadIdx.x >> 6), w = w8 & 3;
  const int wp = w >> 1, wq = w & 1, r = lane & 31, h = lane >> 5;
  if (w8 >= 4) {
    const rsrc rp = mkr(&R1T), ra = mkr(q0), rb = mkr(q1);
    int qv[4];
#pragma unroll
    for (int i = 0; i < 4; ++i) {
      if (JOB == 1) {
        int d = 16 * w + 8 * (i >> 1) + (lane >> 3), pos = lane & 7;
        int f = 64 * qt + 16 * ((pos >> 1) ^ (d & 3) ^ (i & 1)) + 8 * (pos & 1);
        qv[i] = (d * 1376 + (f > 1368 ? 1368 : f)) * 2;
      } else {
        int u = 32 * w + 8 * i + (lane >> 3), row;
        if (JOB == 0) { int rho = 128 * qt + (u & 64) + 2 * (u & 31) + ((u >> 5) & 1); row = 32 * (rho & 63) + (rho >> 6); }
        else { row = 128 * qt + (u & 96) + fperm(u & 31); row = row > 1375 ? 1375 : row; }
        qv[i] = row * 1024 + (((lane & 7) ^ ((u >> 1) & 7)) << 4);
      }
    }
    constexpr int QSTEP = JOB == 1 ? 64 * 1376 * 2 : 128;
    auto issP = [&](int c) { unroll<4>([&](auto I) { blds<I.v * 1024>(rp, lane * 16, (4 * nt + w) * 8192 + c * 4096, lds + w * 8192 + c * 4096); }); };
    auto issue = [&](int cc) {
      const int c = (cc + c0) & 7;
#pragma unroll
      for (int i = 0; i < 4; ++i) blds((JOB == 1 && (i & 1)) ? rb : ra, qv[i], c * QSTEP, lds + 32768 + c * 16384 + (4 * w + i) * 1024);
    };
    issP(0); issP(1); issue(0); issue(1); issue(2);
    waitv<8>(); bar();
#pragma unroll 1
    for (int c = 3; c < 8; ++c) issue(c);
    unroll<8>([&](auto I) { waitv<(I.v < 6 ? 4 * (6 - I.v) : 0)>(); bar(); });
    if (JOB == 0) {
      const int u = 32 * nt + 8 * w + (lane >> 3);
      float s1 = 0.f, s2 = 0.f;
#pragma unroll
      for (int c = 0; c < 8; ++c) {
        u4 v = lds128(lds + 32768 + c * 16384 + u * 128 + (lane & 7) * 16);
#pragma unroll
        for (int j = 0; j < 4; ++j) { s1 = dot2(v[j], 0x3f803f80u, s1); s2 = dot2(v[j], v[j], s2); }
      }
#pragma unroll
      for (int o = 1; o < 8; o <<= 1) { s1 += __shfl_xor(s1, o); s2 += __shfl_xor(s2, o); }
      float m = s1 * (1.f / 512), var = fmaxf(s2 * (1.f / 512) - m * m, 0.f);
      if ((lane & 7) == 0) ((float*)o1)[128 * qt + (u & 64) + 2 * (u & 31) + ((u >> 5) & 1)] = __builtin_amdgcn_rsqf(var + EPS) * (1.f / 2048);
    }
    return;
  }
  f16v acc[2][2] = {}, acc2[2][2] = {};
  int qOff;
  if (JOB == 1) {
    int g = lane >> 4, qr = (lane >> 2) & 3, run = 4 * (g & 1) + (lane & 3), q = run >> 1;
    qOff = 32768 + (g >> 1) * 2048 + qr * 128 + (q & 1) * 1024 + (8 * (run & 1) + 4 * (q >> 1)) * 2;
  } else qOff = 32768 + (64 * wq + r) * 128;
  struct Fr { u4 v[2]; };
  int pOff; u32 sgn;
  auto turn = [&](int c) {
    int n = ((r >> 1) * (c >> 1)) & 3;
    pOff = wp * 16384 + (c & 1) * 4096 + (lane ^ (n & 1)) * 16;
    sgn = (((r & 1) ? n + 1 : n) & 2) ? 0x80008000u : 0u;
  };
  auto rdP = [&](int ks) { return Fr{{lds128(lds + pOff + ks * 1024), lds128(lds + pOff + 8192 + ks * 1024)}}; };
  auto rdQ = [&](const char* sl, int ks) {
    Fr f;
#pragma unroll
    for (int b = 0; b < 2; ++b) {
      if (JOB == 1) {
        f.v[b] = ldtr(sl + qOff + ks * 4096 + (((2 * wq + b) ^ ((lane >> 2) & 3) ^ ((lane >> 1) & 1)) << 5), 512);
      } else f.v[b] = lds128(sl + qOff + b * 4096 + (((2 * ks + h) ^ ((r >> 1) & 7)) << 4));
    }
    return f;
  };
  u4 Id[2];
  if (JOB == 2) {
#pragma unroll
    for (int k = 0; k < 2; ++k) {
      int e = dperm(r) - 16 * k - 8 * h;
#pragma unroll
      for (int j = 0; j < 4; ++j) Id[k][j] = (e == 2 * j ? 0x3f80u : 0u) | (e == 2 * j + 1 ? 0x3f800000u : 0u);
    }
  }
  auto mm = [&](const Fr& P, const Fr& Q, Fr& N, u32 sg, int c, int ks) {
#pragma unroll
    for (int t = 0; t < 2; ++t)
#pragma unroll
      for (int b = 0; b < 2; ++b) acc[t][b] = JOB == 2 ? mf32(Q.v[b], P.v[t], acc[t][b]) : mf32(P.v[t], Q.v[b], acc[t][b]);
    N.v[0] ^= sg; N.v[1] ^= sg;
    SGB(0x008, 1); SGB(0x100, 3); SGB(0x008, 1); SGB(0x100, 3); SGB(0x002, 4); SGB(0x008, 1); SGB(0x002, 4); SGB(0x008, 1);
    asm volatile("" : "+v"(N.v[0]), "+v"(N.v[1]));
    SB();
    if (JOB == 2) {
      if (c == 2 * nt + wp) {
#pragma unroll
        for (int b = 0; b < 2; ++b) acc2[ks >> 1][b] = mf32(Q.v[b], Id[ks & 1], acc2[ks >> 1][b]);
      }
    }
  };
  turn(c0);
  bar();
  STAMP(0, 2);
  Fr qa = rdQ(lds + c0 * 16384, 0), pa = rdP(0), pb = rdP(1), qb, pc, pd;
  pa.v[0] ^= sgn; pa.v[1] ^= sgn;
#pragma unroll 1
  for (int cc = 0; cc < 8; ++cc) {
    const int c = (cc + c0) & 7;
    const char* sl = lds + c * 16384;
    const u32 sg = sgn;
    SB();
    qb = rdQ(sl, 1); pc = rdP(2); mm(pa, qa, pb, sg, c, 0);
    qa = rdQ(sl, 2); pd = rdP(3); mm(pb, qb, pc, sg, c, 1);
    turn((c + 1) & 7);
    SB();
    qb = rdQ(sl, 3); pa = rdP(0); mm(pc, qa, pd, sg, c, 2);
    asm volatile("" ::: "memory"); __builtin_amdgcn_s_barrier(); asm volatile("" ::: "memory");
    SB();
    qa = rdQ(lds + ((c + 1) & 7) * 16384, 0); pb = rdP(1); mm(pd, qb, pa, sgn, c, 3);
  }
  STAMP(0, 3);
  if (JOB == 0) {
#pragma unroll
    for (int t = 0; t < 2; ++t)
#pragma unroll
      for (int i = 0; i < 16; i += 2) {
        int col = 64 * nt + 32 * wp + 16 * t + (((i & 3) + 8 * (i >> 2) + 4 * h) >> 1);
        u2 v = {pk(acc[t][0][i], acc[t][0][i + 1]), pk(acc[t][1][i], acc[t][1][i + 1])};
        st2(o0 + col * XI + 128 * qt + 64 * wq + 2 * r, v);
      }
  } else if (JOB == 1) {
#pragma unroll
    for (int b = 0; b < 2; ++b) {
      int tile = 4 * qt + 2 * wq + b;
      if (tile < 86)
#pragma unroll
        for (int t = 0; t < 2; ++t)
#pragma unroll
          for (int s = 0; s < 2; ++s) {
            int ks = 8 * nt + 4 * wp + 2 * t + s;
            u4 v = pack8(acc[t][b], s);
            if (ks == 0 && h == 0) v[0] &= 0xffffu;
            st4(o0 + (tile * 32 + ks) * 256 + lane * 4, v);
          }
    }
  } else {
#pragma unroll
    for (int b = 0; b < 2; ++b)
#pragma unroll
      for (int s = 0; s < 2; ++s) {
        int ks = 8 * qt + 4 * wq + 2 * b + s;
        if (ks < 86)
#pragma unroll
          for (int t = 0; t < 2; ++t) {
            int o = ((4 * nt + 2 * wp + t) * 86 + ks) * 256 + lane * 4;
            st4(o0 + o, pack8(acc[t][b], s));
            st4(o1 + o, pack8(acc2[t][b], s));
          }
      }
  }
}

__global__ __launch_bounds__(512) void k_prep(const u16* __restrict__ x, const u16* __restrict__ wr, const u16* __restrict__ wi,
    const u16* __restrict__ wg, const u16* __restrict__ wu, const u16* __restrict__ wd,
    u32* __restrict__ xh, u32* __restrict__ rstd, u32* __restrict__ wgu, u32* __restrict__ wd1, u32* __restrict__ wd2,
    u32* __restrict__ filt) {
  __shared__ __attribute__((aligned(1024))) char lds[163840];
  const int b = blockIdx.x, nt = (b >> 3) & 3, q = 8 * (b >> 5) + (b & 7), c0 = ROT;
  const int lane = threadIdx.x & 63, w = __builtin_amdgcn_readfirstlane(threadIdx.x >> 6);
  STAMP(0, 0);
  if (q < 16) prep_gemm<0>(x, x, q, nt, c0, lds, xh, rstd);
  else if (q < 38) prep_gemm<1>(wg, wu, q - 16, nt, 0, lds, wgu, wgu);
  else if (q < 49) prep_gemm<2>(wd, wd, q - 38, nt, c0, lds, wd1, wd2);
  else {
    const int u = b < 256 ? (q - 49) * 4 + nt : b - 196, k0 = 16 * (u >> 4) + 8 * (u & 1) + 256 * ((u >> 1) & 7);
    const rsrc rs = mkr(w < 4 ? wr : wi);
    const int so = (k0 + 64 * (w & 3)) * 514;
    unroll<4>([&](auto I) { blds<I.v * 1024>(rs, lane * 16, so, lds + w * 4160); });
    if (lane == 0) blds(rs, 0, so + 4096, lds + w * 4160 + 4096);
    blds(mkr((const char*)&R1T + 131072), lane * 16, (u * 8 + w) * 1024, lds + 40960 + w * 1024);
    waitv<0>(); bar();
    STAMP(0, 1);
#pragma unroll
    for (int it = 0; it < 4; ++it) {
      const int cs = 1 + 32 * w + 8 * it + (lane >> 3);
      const u16* p = (const u16*)(lds + (lane & 7) * 514) + cs;
      u4 v;
#pragma unroll
      for (int j = 0; j < 4; ++j) v[j] = (u32)p[j * 2080] | ((u32)p[(4 + j) * 2080] << 16);
      st4(filt + (cs & 255) * FS + (u >> 3) * 256 + (((u >> 1) & 3) * 16 + 8 * (u & 1) + (lane & 7)) * 4, v);
    }
  }
  STAMPW(0, q < 16 ? 4 : q < 38 ? 5 : q < 49 ? 6 : 7);
}

DI u4 rot(u4 v) { u4 o; for (int j = 0; j < 4; ++j) o[j] = __builtin_amdgcn_alignbit(v[j], v[j], 16) ^ 0x8000u; return o; }
template <int NP>
__global__ __launch_bounds__(256, 2) void k_fft(const u32* __restrict__ xin, const float* __restrict__ st, const u32* __restrict__ filt,
                                                u32* __restrict__ xout) {
  __shared__ __attribute__((aligned(256))) char lds[16384];
  const int col = blockIdx.x, tid = threadIdx.x, lane = tid & 63, w = tid >> 6, l15 = lane & 15, g = lane >> 4;
  constexpr int KI = NP == 1 ? 1 : 4;
  STAMP(KI, 0);
  const u32* xc = xin + col * XI;
  u4 xd[2], xr[2], fw[2];
  f4 sv[NP][2];
#pragma unroll
  for (int i = 0; i < 2; ++i) xd[i] = *(const u4*)(xc + 1024 * i + 4 * tid);
#pragma unroll
  for (int p = 0; p < NP; ++p)
#pragma unroll
    for (int i = 0; i < 2; ++i) sv[p][i] = *(const f4*)(st + p * SS + 1024 * i + 4 * tid);
#pragma unroll
  for (int i = 0; i < 2; ++i) {
    fw[i] = *(const u4*)(filt + col * FS + (2 * w + i) * 256 + lane * 4);
    xr[i] = *(const u4*)(xc + 64 * (16 * i + l15) + 16 * w + 4 * g);
  }
  SB();
  const int k1 = 16 * w + l15;
  const float fk1 = (float)k1, fl = (float)l15, fa = (float)(4 * g);
  u4 F64r[4], F64i[4], F32r[2][2], F32i[2][2];
  float t1c[2][4], t1s[2][4], t2c[2][4], t2s[2][4];
#pragma unroll
  for (int j = 0; j < 4; ++j) {
    float a = (fa + j) * fk1 * (1.f / 64), b = (fa + j) * fl * (1.f / 32);
    F64r[0][j] = pk(__builtin_amdgcn_cosf(a), __builtin_amdgcn_sinf(a));
    F32r[0][0][j] = pk(__builtin_amdgcn_cosf(b), __builtin_amdgcn_sinf(b));
  }
  F64i[0] = rot(F64r[0]); F32i[0][0] = rot(F32r[0][0]);
  const u32 NEG = 0x80008000u, so = (lane & 1) ? NEG : 0u, sr = (lane & 2) ? NEG : 0u, si = ((lane + 1) & 2) ? NEG : 0u;
#pragma unroll
  for (int j = 0; j < 4; ++j) {
    F64r[1][j] = ((lane & 1) ? F64i[0][j] : F64r[0][j]) ^ sr;
    F64i[1][j] = ((lane & 1) ? F64r[0][j] : F64i[0][j]) ^ si;
    F64r[2][j] = F64r[0][j] ^ so; F64i[2][j] = F64i[0][j] ^ so;
    F64r[3][j] = F64r[1][j] ^ so; F64i[3][j] = F64i[1][j] ^ so;
    const u32 sj = (j & 1) ? NEG : 0u;
    F32r[0][1][j] = F32r[0][0][j] ^ so; F32i[0][1][j] = F32i[0][0][j] ^ so;
    F32r[1][0][j] = F32r[0][0][j] ^ sj; F32i[1][0][j] = F32i[0][0][j] ^ sj;
    F32r[1][1][j] = F32r[0][1][j] ^ sj; F32i[1][1][j] = F32i[0][1][j] ^ sj;
  }
#pragma unroll
  for (int t = 0; t < 2; ++t)
#pragma unroll
    for (int j = 0; j < 4; ++j) {
      float a = (fa + (16 * t + j)) * (fk1 * (1.f / 2048)), b = ((fa + (float)(16 * w)) + j) * ((fl + 16 * t) * (1.f / 2048));
      t1c[t][j] = __builtin_amdgcn_cosf(a); t1s[t][j] = __builtin_amdgcn_sinf(a);
      t2c[t][j] = __builtin_amdgcn_cosf(b); t2s[t][j] = __builtin_amdgcn_sinf(b);
    }
  SB();
  STAMP(KI, 1);
  const u32 msk = col == 0 ? 0xffffu : 0xffffffffu;
#pragma unroll
  for (int i = 0; i < 2; ++i) {
    f4 rs = sv[0][i];
    if (NP > 1) {
#pragma unroll
      for (int p = 1; p < NP; ++p) rs += sv[p][i];
#pragma unroll
      for (int j = 0; j < 4; ++j) rs[j] = __builtin_amdgcn_rsqf(rs[j] * (1.f / 512) + EPS) * (1.f / 2048);
    }
    u4 z;
#pragma unroll
    for (int j = 0; j < 4; ++j) z[j] = pk(lo(xd[i][j]) * rs[j], hi(xd[i][j]) * rs[j]) & msk;
    *(u4*)(lds + (16 * i + (tid >> 4)) * 256 + ((l15 ^ (tid >> 4)) << 4)) = z;
  }
  __syncthreads();
  STAMP(KI, 2);
  u4 z[2][4], T[2], Y[2];
  f4 ar[2], ai[2];
  const f4 Z4 = {0, 0, 0, 0};
#pragma unroll
  for (int s = 0; s < 4; ++s)
#pragma unroll
    for (int t = 0; t < 2; ++t) z[t][s] = lds128(lds + (16 * t + l15) * 256 + (((4 * s + g) ^ l15) << 4));
  SB();
#pragma unroll
  for (int t = 0; t < 2; ++t)
#pragma unroll
    for (int s = 0; s < 4; ++s) { ar[t] = mf16(z[t][s], F64r[s], s ? ar[t] : Z4); ai[t] = mf16(z[t][s], F64i[s], s ? ai[t] : Z4); }
#pragma unroll
  for (int t = 0; t < 2; ++t)
#pragma unroll
    for (int j = 0; j < 4; ++j) T[t][j] = pk(ar[t][j] * t1c[t][j] + ai[t][j] * t1s[t][j], ai[t][j] * t1c[t][j] - ar[t][j] * t1s[t][j]);
#pragma unroll
  for (int m = 0; m < 2; ++m)
#pragma unroll
    for (int t = 0; t < 2; ++t) { ar[m] = mf16(F32r[m][t], T[t], t ? ar[m] : Z4); ai[m] = mf16(F32i[m][t], T[t], t ? ai[m] : Z4); }
#pragma unroll
  for (int m = 0; m < 2; ++m)
#pragma unroll
    for (int j = 0; j < 4; ++j) { float a = lo(fw[m][j]), b = hi(fw[m][j]); Y[m][j] = pk(ar[m][j] * a - ai[m][j] * b, -(ar[m][j] * b) - ai[m][j] * a); }
#pragma unroll
  for (int t = 0; t < 2; ++t)
#pragma unroll
    for (int m = 0; m < 2; ++m) { ar[t] = mf16(Y[m], F32r[t][m], m ? ar[t] : Z4); ai[t] = mf16(Y[m], F32i[t][m], m ? ai[t] : Z4); }
#pragma unroll
  for (int t = 0; t < 2; ++t) {
    u4 v;
#pragma unroll
    for (int j = 0; j < 4; ++j) v[j] = pk(ar[t][j] * t2c[t][j] + ai[t][j] * t2s[t][j], ai[t][j] * t2c[t][j] - ar[t][j] * t2s[t][j]);
    *(u4*)(lds + 8192 + (16 * t + l15) * 256 + (((4 * w + g) ^ l15) << 4)) = v;
  }
  __syncthreads();
#pragma unroll
  for (int s = 0; s < 4; ++s)
#pragma unroll
    for (int t = 0; t < 2; ++t) z[t][s] = lds128(lds + 8192 + (16 * t + l15) * 256 + (((4 * s + g) ^ l15) << 4));
  SB();
#pragma unroll
  for (int t = 0; t < 2; ++t)
#pragma unroll
    for (int s = 0; s < 4; ++s) { ar[t] = mf16(F64r[s], z[t][s], s ? ar[t] : Z4); ai[t] = mf16(F64i[s], z[t][s], s ? ai[t] : Z4); }
  const float sg = col == 0 ? 0.f : -1.f;
#pragma unroll
  for (int t = 0; t < 2; ++t) {
    float re[4], im[4];
#pragma unroll
    for (int j = 0; j < 4; ++j) { re[j] = lo(xr[t][j]) + ar[t][j]; im[j] = hi(xr[t][j]) + (col == 0 ? 0.f : sg * ai[t][j]); }
    u32* o = xout + col * (2 * PS) + 32 * (16 * t + l15) + 8 * w + 2 * g;
    st2(o, u2{pk(re[0], re[1]), pk(re[2], re[3])});
    st2(o + PS, u2{pk(im[0], im[1]), pk(im[2], im[3])});
  }
  STAMP(KI, 3); STAMPW(KI, 4);
}

template <int LAST>
__global__ __launch_bounds__(512) void k_up(const u32* __restrict__ xh, const u32* __restrict__ wgu,
                                               u32* __restrict__ hb, u32* __restrict__ x3, float* __restrict__ rs2) {
  constexpr int NT = 6 + LAST, CH = NT * 4096 + 16384, NS = LAST ? 3 : 4, NI = NT + 4, PRE = UPPRE;
  __shared__ __attribute__((aligned(1024))) char lds[NS * CH];
  const int xc = blockIdx.x & 7, jb = blockIdx.x >> 3, mt = 8 * (xc & 1) + (jb & 7), ng = 4 * (xc >> 1) + (jb >> 3);
  const int t0 = (ng * 43) >> 3, t1 = ((ng + 1) * 43) >> 3;
  const int lane = threadIdx.x & 63, w8 = __builtin_amdgcn_readfirstlane(threadIdx.x >> 6), w = w8 & 3;
  const int wm = w >> 1, wn = w & 1, r = lane & 31, h = lane >> 5;
  constexpr int KI = LAST ? 5 : 2;
  STAMP(KI, 0);
  if (w8 >= 4) {
    const rsrc rq = mkr(xh), rp = mkr(wgu), rr = mkr((const char*)&R1T + 131072);
    const int vq = (lane >> 4) * (PS * 4) + (((lane & 15) ^ (4 * (lane >> 4))) << 4);
    auto issue = [&](int c, char* sl) {
#pragma unroll
      for (int i = 0; i < 4; ++i) blds(rq, vq, (64 * c + 16 * w + 4 * i) * (PS * 4) + 256 * mt, sl + NT * 4096 + (4 * w + i) * 1024);
#pragma unroll
      for (int i = 0; i < 3; ++i) {
        int e = 6 * w + 2 * i, so = (t0 + (e >> 2)) * 32768 + (4 * c + (e & 3)) * 1024;
        blds(rp, lane * 16, so, sl + e * 1024); blds<1024>(rp, lane * 16, so, sl + e * 1024);
      }
      if (LAST) blds(rr, lane * 16, (ng * 32 + 4 * c + w) * 1024, sl + (24 + w) * 1024);
    };
#pragma unroll
    for (int c = 0; c < PRE; ++c) issue(c, lds + c * CH);
    waitv<(PRE - 1) * NI>(); bar();
#pragma unroll
    for (int c = PRE; c < NS; ++c) issue(c, lds + c * CH);
    const int sOff = NT * 4096 + lane * 256 + ((ng ^ (4 * (lane & 3))) << 4) + 4 * w;
    u32 v;
    asm volatile("ds_read_b32 %0, %1\n s_waitcnt lgkmcnt(0)" : "=v"(v) : "v"((u32)(size_t)(LDSP char*)(lds + sOff)) : "memory");
    v = lane == 1 ? 0u : v;
    float s0 = lo(v) * lo(v), s1 = hi(v) * hi(v);
    int so = 0;
#pragma unroll 1
    for (int c = 0; c < 8; ++c) {
      if (c < 9 - NS) waitv<(NS - 2) * NI>(); else if (NS == 4 && c == 5) waitv<NI>(); else waitv<0>();
      bar();
      if (c < 8 - NS) issue(c + NS, lds + so);
      so = so == (NS - 1) * CH ? 0 : so + CH;
      if (c < 7) {
        asm volatile("ds_read_b32 %0, %1\n s_waitcnt lgkmcnt(0)" : "=v"(v) : "v"((u32)(size_t)(LDSP char*)(lds + so + sOff)) : "memory");
        s0 += lo(v) * lo(v); s1 += hi(v) * hi(v);
      }
    }
#pragma unroll
    for (int o = 1; o < 64; o <<= 1) { s0 += __shfl_xor(s0, o); s1 += __shfl_xor(s1, o); }
    if (lane < 2) rs2[128 * mt + 8 * ng + 2 * w + lane] = 1.f / ((lane ? s1 : s0) * (1.f / 512) + EPS);
    return;
  }
  f16v acc[3][2] = {}, accx = {};
  const int pOff = wn * 12288 + lane * 16;
  int qOff[2];
#pragma unroll
  for (int b = 0; b < 2; ++b)
    qOff[b] = NT * 4096 + (4 * h + ((lane >> 2) & 3)) * 256 + (((4 * (2 * wm + (b ^ wn)) + ((lane >> 3) & 2) + ((lane >> 1) & 1)) ^ (lane & 12)) << 4) + 8 * (lane & 1);
  struct Fr { u4 P[3], Q[2], X; };
  auto rd = [&](const char* sl, int ks) {
    Fr f;
#pragma unroll
    for (int b = 0; b < 2; ++b) f.Q[b] = ldtr(sl + qOff[b] + ks * 4096, 2048);
#pragma unroll
    for (int t = 0; t < 3; ++t) f.P[t] = lds128(sl + pOff + t * 4096 + ks * 1024);
    if (LAST) f.X = lds128(sl + 24576 + ks * 1024 + lane * 16);
    return f;
  };
  auto mm = [&](const Fr& f, int ks) {
#pragma unroll
    for (int t = 0; t < 3; ++t) { acc[t][0] = mf32(f.P[t], f.Q[0], acc[t][0]); acc[t][1] = mf32(f.P[t], f.Q[1], acc[t][1]); }
    if (LAST) accx = mf32(f.X, f.Q[0], accx);
    mix<6 + LAST, 2, 1>();
    SB();
  };
  bar();
  STAMP(KI, 1);
  Fr f0 = rd(lds, 0), f1;
  int so = 0;
#pragma unroll 1
  for (int c = 0; c < 8; ++c) {
    char* sl = lds + so;
    so = so == (NS - 1) * CH ? 0 : so + CH;
    SB();
    f1 = rd(sl, 1); mm(f0, 0);
    f0 = rd(sl, 2); mm(f1, 1);
    f1 = rd(sl, 3); mm(f0, 2);
    bar();
    SB();
    f0 = rd(lds + so, 0); mm(f1, 3);
  }
  STAMP(KI, 3);
#pragma unroll
  for (int t = 0; t < 3; ++t) {
    int tile = t0 + 3 * wn + t;
    if (tile < t1)
#pragma unroll
      for (int b = 0; b < 2; ++b) {
        const f16v& a = acc[t][b];
        u4 o;
#pragma unroll
        for (int j = 0; j < 4; ++j) { int i = 8 * (j >> 1) + 2 * (j & 1); o[j] = pk(fmaxf(a[i], 0.f) * a[i + 4], fmaxf(a[i + 1], 0.f) * a[i + 5]); }
        st4(hb + (((2 * mt + wm) * 2 + (b ^ wn)) * 86 + tile) * 256 + lane * 4, o);
      }
  }
  if (LAST) {
    u32* o = x3 + ((4 * mt + w) * 16 + ng) * 512 + lane * 4;
    st4(o, pack8(accx, 0)); st4(o + 256, pack8(accx, 1));
  }
  STAMP(KI, 4); STAMPW(KI, 5);
}

template <int LAST>
__global__ __launch_bounds__(512) void k_down(const u32* __restrict__ hb, const u32* __restrict__ wd, const float* __restrict__ rs2,
                                              const u32* __restrict__ res, u32* __restrict__ out, float* __restrict__ ssp) {
  __shared__ __attribute__((aligned(1024))) char lds[131072 + 512];
  const int xc = blockIdx.x & 7, jb = blockIdx.x >> 3, rg = 8 * (xc & 3) + (jb & 7), nt = 4 * (xc >> 2) + (jb >> 3);
  const int lane = threadIdx.x & 63, w8 = __builtin_amdgcn_readfirstlane(threadIdx.x >> 6), w = w8 & 3, r = lane & 31, h = lane >> 5;
  constexpr int KI = LAST ? 6 : 3;
  STAMP(KI, 0);
  if (w8 >= 4) {
    const rsrc rs = mkr(w < 2 ? wd + (2 * nt + w) * 86 * 256 : hb + (2 * rg + w - 2) * 86 * 256);
    auto issue = [&](int c) {
      char* sl = lds + (c & 3) * 32768 + w * 8192;
      unroll<4>([&](auto I) { blds<I.v * 1024>(rs, lane * 16, c * 8192, sl); });
      unroll<2>([&](auto I) { blds<I.v * 1024>(rs, lane * 16, c * 8192 + 4096, sl + 4096); });
      if (c < 10) unroll<2>([&](auto I) { blds<I.v * 1024 + 2048>(rs, lane * 16, c * 8192 + 4096, sl + 4096); });
    };
    issue(0); issue(1); issue(2);
    waitv<16>(); bar();
    issue(3);
#pragma unroll 1
    for (int c = 0; c < 10; ++c) {
      if (c < 7) waitv<16>(); else if (c == 7) waitv<14>(); else if (c == 8) waitv<6>(); else waitv<0>();
      bar();
      if (c < 7) issue(c + 4);
    }
    if (!LAST) bar();
    return;
  }
  const int t = w >> 1, b = w & 1, rho = 64 * rg + 32 * b + r;
  u32 rv[8];
  if (LAST) {
    const u32* p = res + ((2 * rg + b) * 16 + 2 * nt + t) * 512 + lane * 4;
    u4 x = *(const u4*)p, y = *(const u4*)(p + 256);
#pragma unroll
    for (int j = 0; j < 4; ++j) { rv[j] = x[j]; rv[4 + j] = y[j]; }
  } else {
    const u16* p = (const u16*)res + (64 * nt + 32 * t + 4 * h) * (2 * PS) + rho;
#pragma unroll
    for (int j = 0; j < 8; ++j) { const u16* e = p + (8 * (j >> 1) + 2 * (j & 1)) * (2 * PS); rv[j] = (u32)e[0] | ((u32)e[2 * PS] << 16); }
  }
  const float rs = rs2[rho];
  struct Fr { u4 A[4], B[4]; };
  const int aOff = t * 8192 + lane * 16, bOff = 16384 + b * 8192 + lane * 16;
  auto rd = [&](const char* sl, auto N) {
    Fr f;
#pragma unroll
    for (int k = 0; k < N.v; ++k) { f.A[k] = lds128(sl + aOff + k * 1024); f.B[k] = lds128(sl + bOff + k * 1024); }
    return f;
  };
  f16v acc = {};
  auto mm = [&](const Fr& f, auto N) {
#pragma unroll
    for (int k = 0; k < N.v; ++k) acc = mf32(f.A[k], f.B[k], acc);
    mix<N.v, 2, 1>();
    SB();
  };
  bar();
  STAMP(KI, 1);
  Fr f0 = rd(lds, IC<4>{}), f1;
#pragma unroll 1
  for (int c = 0; c < 10; ++c) {
    SB();
    f1 = rd(lds + (c & 3) * 32768 + 4096, IC<4>{}); mm(f0, IC<4>{});
    bar();
    SB();
    f0 = rd(lds + ((c + 1) & 3) * 32768, IC<4>{}); mm(f1, IC<4>{});
  }
  f1 = rd(lds + 2 * 32768 + 4096, IC<2>{}); mm(f0, IC<4>{});
  mm(f1, IC<2>{});
  STAMP(KI, 2);
  u32 v[8];
#pragma unroll
  for (int j = 0; j < 8; ++j) v[j] = pk(lo(rv[j]) + rs * acc[2 * j], hi(rv[j]) + rs * acc[2 * j + 1]);
  if (LAST) {
    u32* o = out + (32 * (32 * b + r) + rg) * 256 + 32 * nt + 16 * t + 8 * h;
    st4(o, u4{v[0], v[1], v[2], v[3]}); st4(o + 4, u4{v[4], v[5], v[6], v[7]});
  } else {
    const int c0 = 32 * nt + 16 * t + 2 * h;
    float ss = 0.f;
#pragma unroll
    for (int j = 0; j < 8; ++j) {
      st1(out + (c0 + 4 * (j >> 1) + (j & 1)) * XI + rho, v[j]);
      u32 m = (j == 0 && c0 == 0) ? (v[j] & 0xffffu) : v[j];
      ss = dot2(m, m, ss);
    }
    ss += __shfl_xor(ss, 32);
    float* sl = (float*)(lds + 131072);
    if (h == 0) sl[w * 32 + r] = ss;
    bar();
    if (w == 0) ssp[nt * SS + 64 * rg + lane] = sl[lane] + sl[64 + lane];
  }
  STAMP(KI, 3); STAMPW(KI, 4);
}

extern "C" void kernel_launch(void* const* d_in, const int* in_sizes, int n_in, void* d_out, int out_size, void* d_ws, size_t ws_size,
                              hipStream_t stream) {
  const u16 *x = (const u16*)d_in[0], *wr = (const u16*)d_in[1], *wi = (const u16*)d_in[2];
  const u16 *wg = (const u16*)d_in[4], *wu = (const u16*)d_in[5], *wd = (const u16*)d_in[6];
  char* ws = (char*)d_ws;
  const size_t MB = 1 << 20;
  u32 *xh0 = (u32*)ws, *xh1 = (u32*)(ws + 3 * MB), *xh2 = (u32*)(ws + 6 * MB), *xh3 = (u32*)(ws + 9 * MB);
  u32 *filt = (u32*)(ws + 12 * MB), *x3 = (u32*)(ws + 15 * MB), *wgu = (u32*)(ws + 17 * MB), *wd1 = (u32*)(ws + 20 * MB);
  u32 *wd2 = (u32*)(ws + 22 * MB), *hb = (u32*)(ws + 24 * MB), *hb2 = (u32*)(ws + 30 * MB);
  float *rstd = (float*)(ws + 36 * MB), *ssp = rstd + 4096, *rs2 = rstd + 32768, *rs3 = rstd + 65536;
  k_prep<<<260, 512, 0, stream>>>(x, wr, wi, wg, wu, wd, xh0, (u32*)rstd, wgu, wd1, wd2, filt);
  k_fft<1><<<256, 256, 0, stream>>>(xh0, rstd, filt, xh1);
  k_up<0><<<256, 512, 0, stream>>>(xh1, wgu, hb, x3, rs2);
  k_down<0><<<256, 512, 0, stream>>>(hb, wd1, rs2, xh1, xh2, ssp);
  k_fft<8><<<256, 256, 0, stream>>>(xh2, ssp, filt, xh3);
  k_up<1><<<256, 512, 0, stream>>>(xh3, wgu, hb2, x3, rs3);
  k_down<1><<<256, 512, 0, stream>>>(hb2, wd2, rs3, x3, (u32*)d_out, ssp);
}
```

```cpp
#define CODEW
#include <hip/hip_runtime.h>
#include <hip/hip_bf16.h>

typedef unsigned u32;
typedef unsigned short u16;
typedef __attribute__((ext_vector_type(8))) __bf16 bf8;
typedef __attribute__((ext_vector_type(2))) __bf16 bf2;
typedef __attribute__((ext_vector_type(2))) float f2;
typedef __attribute__((ext_vector_type(4))) float f4;
typedef __attribute__((ext_vector_type(16))) float f16v;
typedef __attribute__((ext_vector_type(2))) u32 u2;
typedef __attribute__((ext_vector_type(4))) u32 u4;
typedef __attribute__((ext_vector_type(4))) short s4;
typedef u4 __attribute__((aligned(2))) u4_ua;
#define DI __device__ __forceinline__
#define LDSP __attribute__((address_space(3)))
#define GLBP __attribute__((address_space(1)))

#define STAMP(k, j)
#define STAMPW(k, j)
#define ROT 0
#define UPPRE 2
constexpr int SS = 2048;
constexpr int XI = 2112, PS = 1088, FS = 2080;
constexpr float EPS = 1e-5f;

struct Part { u16 v[16384]; };
#if defined(__HIP_DEVICE_COMPILE__)
constexpr double kPi = 3.14159265358979323846;
constexpr double csin(double x) { double t = x, s = x; for (int i = 1; i < 14; ++i) { t *= -x * x / ((2 * i) * (2 * i + 1)); s += t; } return s; }
constexpr u16 tobf(double x) { float f = (float)x; u32 u = __builtin_bit_cast(u32, f); u += 0x7fffu + ((u >> 16) & 1u); return (u16)(u >> 16); }
struct CosT { u16 v[512]; };
constexpr CosT mkcos() {
  CosT c{};
  for (int i = 0; i < 512; ++i) {
    int q = (i + 128) & 511;
    int y = q <= 128 ? q : (q <= 384 ? 256 - q : q - 512);
    c.v[i] = y == 0 ? (u16)0 : tobf(csin(2 * kPi * y / 512) / 16.0);
  }
  return c;
}
constexpr CosT kCos = mkcos();
constexpr u16 kDC = tobf(0.04419417382415922);
constexpr Part mkR(int pi) {
  Part p{};
  for (int e = 0; e < 16384; ++e) {
    int r = (e >> 3) & 31, h = (e >> 8) & 1, j = e & 7, d = 0, s = 0;
    if (pi < 4) { d = (((e >> 9) & 7) << 4) + 8 * h + j; s = 32 * (4 * pi + (e >> 12)) + r; }
    else { d = 32 * (pi - 4) + (r & 3) + 4 * (r >> 3) + 16 * ((r >> 2) & 1); s = ((e >> 9) << 4) + 8 * (j >> 2) + 4 * h + (j & 3); }
    p.v[e] = s >= 2 ? kCos.v[((s >> 1) * d + ((s & 1) << 7)) & 511] : (u16)(kDC | ((s == 0 && (d & 1)) ? 0x8000 : 0));
  }
  return p;
}
template <int I> struct PR { static constexpr Part v = mkR(I); };
struct Tab { Part p[20]; };
__device__ const __attribute__((aligned(1024))) Tab R1T = {{PR<0>::v, PR<1>::v, PR<2>::v, PR<3>::v, PR<4>::v, PR<5>::v, PR<6>::v, PR<7>::v,
  PR<8>::v, PR<9>::v, PR<10>::v, PR<11>::v, PR<12>::v, PR<13>::v, PR<14>::v, PR<15>::v, PR<16>::v, PR<17>::v, PR<18>::v, PR<19>::v}};
#else
struct Tab { u16 p[8]; };
__device__ const Tab R1T = {};
#endif

DI f16v mf32(u4 a, u4 b, f16v c) { return __builtin_amdgcn_mfma_f32_32x32x16_bf16(__builtin_bit_cast(bf8, a), __builtin_bit_cast(bf8, b), c, 0, 0, 0); }
DI f4 mf16(u4 a, u4 b, f4 c) { return __builtin_amdgcn_mfma_f32_16x16x32_bf16(__builtin_bit_cast(bf8, a), __builtin_bit_cast(bf8, b), c, 0, 0, 0); }
DI u32 pk(float a, float b) { f2 v = {a, b}; return __builtin_bit_cast(u32, __builtin_convertvector(v, bf2)); }
DI float lo(u32 u) { return __builtin_bit_cast(float, u << 16); }
DI float hi(u32 u) { return __builtin_bit_cast(float, u & 0xffff0000u); }
DI float dot2(u32 a, u32 b, float c) { return __builtin_amdgcn_fdot2_f32_bf16(__builtin_bit_cast(bf2, a), __builtin_bit_cast(bf2, b), c, false); }
DI void glds(const void* g, char* l) { __builtin_amdgcn_global_load_lds((const GLBP void*)g, (LDSP void*)l, 16, 0, 0); }
typedef __amdgpu_buffer_rsrc_t rsrc;
DI rsrc mkr(const void* p) { return __builtin_amdgcn_make_buffer_rsrc((void*)p, 0, 0x7fffffff, 0x00020000); }
template <int O = 0> DI void blds(rsrc r, int vo, int so, char* l) { __builtin_amdgcn_raw_ptr_buffer_load_lds(r, (LDSP void*)l, 16, vo, so, O, 0); }
DI u4 ldtr(const char* a, int d) {
  u2 x = __builtin_bit_cast(u2, __builtin_amdgcn_ds_read_tr16_b64_v4i16((LDSP s4*)a));
  u2 y = __builtin_bit_cast(u2, __builtin_amdgcn_ds_read_tr16_b64_v4i16((LDSP s4*)(a + d)));
  return u4{x[0], x[1], y[0], y[1]};
}
#define WT 2
DI void st4(u32* p, u4 v) {
  if (WT == 2) asm volatile("global_store_dwordx4 %0, %1, off sc0 sc1\n s_nop 1" ::"v"(p), "v"(v) : "memory");
  else if (WT == 1) __builtin_nontemporal_store(v, (u4*)p);
  else *(u4*)p = v;
}
DI void st2(u32* p, u2 v) {
  if (WT == 2) asm volatile("global_store_dwordx2 %0, %1, off sc0 sc1" ::"v"(p), "v"(v) : "memory");
  else if (WT == 1) __builtin_nontemporal_store(v, (u2*)p);
  else *(u2*)p = v;
}
DI void st1(u32* p, u32 v) {
  if (WT == 2) asm volatile("global_store_dword %0, %1, off sc0 sc1" ::"v"(p), "v"(v) : "memory");
  else if (WT == 1) __builtin_nontemporal_store(v, p);
  else *p = v;
}
DI u4 lds128(const char* p) { return *(const u4*)p; }
DI u4 pack8(const f16v& a, int s) { u4 o = {pk(a[8 * s], a[8 * s + 1]), pk(a[8 * s + 2], a[8 * s + 3]), pk(a[8 * s + 4], a[8 * s + 5]), pk(a[8 * s + 6], a[8 * s + 7])}; return o; }
template <int N> DI void waitv() { asm volatile("s_waitcnt vmcnt(%0)" ::"n"(N) : "memory"); }
DI void bar() { asm volatile("s_waitcnt lgkmcnt(0)" ::: "memory"); __builtin_amdgcn_s_barrier(); asm volatile("" ::: "memory"); }
DI int dperm(int m) { return (m & 3) + 4 * (m >> 3) + 16 * ((m >> 2) & 1); }
DI int fperm(int m) { int i = (m & 3) + 4 * (m >> 3); return 16 * (i >> 3) + 8 * ((m >> 2) & 1) + (i & 7); }
#define SB() __builtin_amdgcn_sched_barrier(0)
#define SGB(m, n) __builtin_amdgcn_sched_group_barrier(m, n, 0)
template <int NM, int ND, int NV> DI void mix() { if constexpr (NM > 0) { SGB(0x008, 1); SGB(0x100, ND); SGB(0x002, NV); mix<NM - 1, ND, NV>(); } }
template <int I> struct IC { static constexpr int v = I; };
template <int N, class F> DI void unroll(F&& f) { if constexpr (N > 0) { unroll<N - 1>(f); f(IC<N - 1>{}); } }

template <int JOB>
DI void prep_gemm(const u16* __restrict__ q0, const u16* __restrict__ q1, int qt, int nt, int c0, char* lds,
                  u32* __restrict__ o0, u32* __restrict__ o1) {
  const int lane = threadIdx.x & 63, w8 = __builtin_amdgcn_readfirstlane(threadIdx.x >> 6), w = w8 & 3;
  const int wp = w >> 1, wq = w & 1, r = lane & 31, h = lane >> 5;
  if (w8 >= 4) {
    const rsrc rp = mkr(&R1T), ra = mkr(q0), rb = mkr(q1);
    int qv[4];
#pragma unroll
    for (int i = 0; i < 4; ++i) {
      if (JOB == 1) {
        int d = 16 * w + 8 * (i >> 1) + (lane >> 3), pos = lane & 7;
        int f = 64 * qt + 16 * ((pos >> 1) ^ (d & 3) ^ (i & 1)) + 8 * (pos & 1);
        qv[i] = (d * 1376 + (f > 1368 ? 1368 : f)) * 2;
      } else {
        int u = 32 * w + 8 * i + (lane >> 3), row;
        if (JOB == 0) { int rho = 128 * qt + (u & 64) + 2 * (u & 31) + ((u >> 5) & 1); row = 32 * (rho & 63) + (rho >> 6); }
        else { row = 128 * qt + (u & 96) + fperm(u & 31); row = row > 1375 ? 1375 : row; }
        qv[i] = row * 1024 + (((lane & 7) ^ ((u >> 1) & 7)) << 4);
      }
    }
    constexpr int QSTEP = JOB == 1 ? 64 * 1376 * 2 : 128;
    auto issP = [&](int c) { unroll<4>([&](auto I) { blds<I.v * 1024>(rp, lane * 16, (4 * nt + w) * 8192 + c * 4096, lds + w * 8192 + c * 4096); }); };
    auto issue = [&](int cc) {
      const int c = (cc + c0) & 7;
#pragma unroll
      for (int i = 0; i < 4; ++i) blds((JOB == 1 && (i & 1)) ? rb : ra, qv[i], c * QSTEP, lds + 32768 + c * 16384 + (4 * w + i) * 1024);
    };
    issP(0); issP(1); issue(0); issue(1); issue(2);
    waitv<8>(); bar();
#pragma unroll 1
    for (int c = 3; c < 8; ++c) issue(c);
    unroll<8>([&](auto I) { waitv<(I.v < 6 ? 4 * (6 - I.v) : 0)>(); bar(); });
    if (JOB == 0) {
      const int u = 32 * nt + 8 * w + (lane >> 3);
      float s1 = 0.f, s2 = 0.f;
#pragma unroll
      for (int c = 0; c < 8; ++c) {
        u4 v = lds128(lds + 32768 + c * 16384 + u * 128 + (lane & 7) * 16);
#pragma unroll
        for (int j = 0; j < 4; ++j) { s1 = dot2(v[j], 0x3f803f80u, s1); s2 = dot2(v[j], v[j], s2); }
      }
#pragma unroll
      for (int o = 1; o < 8; o <<= 1) { s1 += __shfl_xor(s1, o); s2 += __shfl_xor(s2, o); }
      float m = s1 * (1.f / 512), var = fmaxf(s2 * (1.f / 512) - m * m, 0.f);
      if ((lane & 7) == 0) ((float*)o1)[128 * qt + (u & 64) + 2 * (u & 31) + ((u >> 5) & 1)] = __builtin_amdgcn_rsqf(var + EPS) * (1.f / 2048);
    }
    return;
  }
  f16v acc[2][2] = {}, acc2[2][2] = {};
  int qOff;
  if (JOB == 1) {
    int g = lane >> 4, qr = (lane >> 2) & 3, run = 4 * (g & 1) + (lane & 3), q = run >> 1;
    qOff = 32768 + (g >> 1) * 2048 + qr * 128 + (q & 1) * 1024 + (8 * (run & 1) + 4 * (q >> 1)) * 2;
  } else qOff = 32768 + (64 * wq + r) * 128;
  struct Fr { u4 v[2]; };
  int pOff; u32 sgn;
  auto turn = [&](int c) {
    int n = ((r >> 1) * (c >> 1)) & 3;
    pOff = wp * 16384 + (c & 1) * 4096 + (lane ^ (n & 1)) * 16;
    sgn = (((r & 1) ? n + 1 : n) & 2) ? 0x80008000u : 0u;
  };
  auto rdP = [&](int ks) { return Fr{{lds128(lds + pOff + ks * 1024), lds128(lds + pOff + 8192 + ks * 1024)}}; };
  auto rdQ = [&](const char* sl, int ks) {
    Fr f;
#pragma unroll
    for (int b = 0; b < 2; ++b) {
      if (JOB == 1) {
        f.v[b] = ldtr(sl + qOff + ks * 4096 + (((2 * wq + b) ^ ((lane >> 2) & 3) ^ ((lane >> 1) & 1)) << 5), 512);
      } else f.v[b] = lds128(sl + qOff + b * 4096 + (((2 * ks + h) ^ ((r >> 1) & 7)) << 4));
    }
    return f;
  };
  u4 Id[2];
  if (JOB == 2) {
#pragma unroll
    for (int k = 0; k < 2; ++k) {
      int e = dperm(r) - 16 * k - 8 * h;
#pragma unroll
      for (int j = 0; j < 4; ++j) Id[k][j] = (e == 2 * j ? 0x3f80u : 0u) | (e == 2 * j + 1 ? 0x3f800000u : 0u);
    }
  }
  auto mm = [&](const Fr& P, const Fr& Q, Fr& N, u32 sg, int c, int ks) {
#pragma unroll
    for (int t = 0; t < 2; ++t)
#pragma unroll
      for (int b = 0; b < 2; ++b) acc[t][b] = JOB == 2 ? mf32(Q.v[b], P.v[t], acc[t][b]) : mf32(P.v[t], Q.v[b], acc[t][b]);
    N.v[0] ^= sg; N.v[1] ^= sg;
    SGB(0x008, 1); SGB(0x100, 3); SGB(0x008, 1); SGB(0x100, 3); SGB(0x002, 4); SGB(0x008, 1); SGB(0x002, 4); SGB(0x008, 1);
    asm volatile("" : "+v"(N.v[0]), "+v"(N.v[1]));
    SB();
    if (JOB == 2) {
      if (c == 2 * nt + wp) {
#pragma unroll
        for (int b = 0; b < 2; ++b) acc2[ks >> 1][b] = mf32(Q.v[b], Id[ks & 1], acc2[ks >> 1][b]);
      }
    }
  };
  turn(c0);
  bar();
  STAMP(0, 2);
  Fr qa = rdQ(lds + c0 * 16384, 0), pa = rdP(0), pb = rdP(1), qb, pc, pd;
  pa.v[0] ^= sgn; pa.v[1] ^= sgn;
#pragma unroll 1
  for (int cc = 0; cc < 8; ++cc) {
    const int c = (cc + c0) & 7;
    const char* sl = lds + c * 16384;
    const u32 sg = sgn;
    SB();
    qb = rdQ(sl, 1); pc = rdP(2); mm(pa, qa, pb, sg, c, 0);
    qa = rdQ(sl, 2); pd = rdP(3); mm(pb, qb, pc, sg, c, 1);
    turn((c + 1) & 7);
    SB();
    qb = rdQ(sl, 3); pa = rdP(0); mm(pc, qa, pd, sg, c, 2);
    asm volatile("" ::: "memory"); __builtin_amdgcn_s_barrier(); asm volatile("" ::: "memory");
    SB();
    qa = rdQ(lds + ((c + 1) & 7) * 16384, 0); pb = rdP(1); mm(pd, qb, pa, sgn, c, 3);
  }
  STAMP(0, 3);
  if (JOB == 0) {
#pragma unroll
    for (int t = 0; t < 2; ++t)
#pragma unroll
      for (int i = 0; i < 16; i += 2) {
        int col = 64 * nt + 32 * wp + 16 * t + (((i & 3) + 8 * (i >> 2) + 4 * h) >> 1);
        u2 v = {pk(acc[t][0][i], acc[t][0][i + 1]), pk(acc[t][1][i], acc[t][1][i + 1])};
        st2(o0 + col * XI + 128 * qt + 64 * wq + 2 * r, v);
      }
  } else if (JOB == 1) {
#pragma unroll
    for (int b = 0; b < 2; ++b) {
      int tile = 4 * qt + 2 * wq + b;
      if (tile < 86)
#pragma unroll
        for (int t = 0; t < 2; ++t)
#pragma unroll
          for (int s = 0; s < 2; ++s) {
            int ks = 8 * nt + 4 * wp + 2 * t + s;
            u4 v = pack8(acc[t][b], s);
            if (ks == 0 && h == 0) v[0] &= 0xffffu;
            st4(o0 + (tile * 32 + ks) * 256 + lane * 4, v);
          }
    }
  } else {
#pragma unroll
    for (int b = 0; b < 2; ++b)
#pragma unroll
      for (int s = 0; s < 2; ++s) {
        int ks = 8 * qt + 4 * wq + 2 * b + s;
        if (ks < 86)
#pragma unroll
          for (int t = 0; t < 2; ++t) {
            int o = ((4 * nt + 2 * wp + t) * 86 + ks) * 256 + lane * 4;
            st4(o0 + o, pack8(acc[t][b], s));
            st4(o1 + o, pack8(acc2[t][b], s));
          }
      }
  }
}

__global__ __launch_bounds__(512) void k_prep(const u16* __restrict__ x, const u16* __restrict__ wr, const u16* __restrict__ wi,
    const u16* __restrict__ wg, const u16* __restrict__ wu, const u16* __restrict__ wd,
    u32* __restrict__ xh, u32* __restrict__ rstd, u32* __restrict__ wgu, u32* __restrict__ wd1, u32* __restrict__ wd2,
    u32* __restrict__ filt, const u32* __restrict__ dout) {
  __shared__ __attribute__((aligned(1024))) char lds[163840];
  const int b = blockIdx.x, nt = (b >> 3) & 3, q = 8 * (b >> 5) + (b & 7), c0 = ROT;
  const int lane = threadIdx.x & 63, w = __builtin_amdgcn_readfirstlane(threadIdx.x >> 6);
  STAMP(0, 0);
  if (q < 16) prep_gemm<0>(x, x, q, nt, c0, lds, xh, rstd);
  else if (q < 38) prep_gemm<1>(wg, wu, q - 16, nt, 0, lds, wgu, wgu);
  else if (q < 49) prep_gemm<2>(wd, wd, q - 38, nt, c0, lds, wd1, wd2);
  else {
    const int u = b < 256 ? (q - 49) * 4 + nt : b - 196, k0 = 16 * (u >> 4) + 8 * (u & 1) + 256 * ((u >> 1) & 7);
    const rsrc rs = mkr(w < 4 ? wr : wi);
    const int so = (k0 + 64 * (w & 3)) * 514;
    unroll<4>([&](auto I) { blds<I.v * 1024>(rs, lane * 16, so, lds + w * 4160); });
    if (lane == 0) blds(rs, 0, so + 4096, lds + w * 4160 + 4096);
    blds(mkr((const char*)&R1T + 131072), lane * 16, (u * 8 + w) * 1024, lds + 40960 + w * 1024);
    if (w < 6) {
      u32 lo_, hi_; int nb;
#define PCOF(sym, n) { asm volatile("s_getpc_b64 s[40:41]\n s_add_u32 %0, s40, " sym "@rel32@lo+4\n s_addc_u32 %1, s41, " sym "@rel32@hi+12" : "=s"(lo_), "=s"(hi_) :: "scc", "s40", "s41"); nb = n; }
      if (w == 0) PCOF("_Z5k_fftILi1EEvPKjPKfS1_Pj", 3904)
      else if (w == 1) PCOF("_Z4k_upILi0EEvPKjS1_PjS2_Pf", 4352)
      else if (w == 2) PCOF("_Z6k_downILi0EEvPKjS1_PKfS1_PjPf", 2688)
      else if (w == 3) PCOF("_Z5k_fftILi8EEvPKjPKfS1_Pj", 4352)
      else if (w == 4) PCOF("_Z4k_upILi1EEvPKjS1_PjS2_Pf", 4608)
      else PCOF("_Z6k_downILi1EEvPKjS1_PKfS1_PjPf", 1920)
      const rsrc rc = __builtin_amdgcn_make_buffer_rsrc((void*)(((unsigned long long)hi_ << 32) | lo_), 0, nb, 0x00020000);
      unroll<4>([&](auto I) { blds<I.v * 1024>(rc, lane * 16, 0, lds + 65536 + w * 8192); });
      blds(rc, lane * 16 + 4096, 0, lds + 65536 + w * 8192 + 4096);
    }
    waitv<0>(); bar();
    STAMP(0, 1);
#pragma unroll
    for (int it = 0; it < 4; ++it) {
      const int cs = 1 + 32 * w + 8 * it + (lane >> 3);
      const u16* p = (const u16*)(lds + (lane & 7) * 514) + cs;
      u4 v;
#pragma unroll
      for (int j = 0; j < 4; ++j) v[j] = (u32)p[j * 2080] | ((u32)p[(4 + j) * 2080] << 16);
      st4(filt + (cs & 255) * FS + (u >> 3) * 256 + (((u >> 1) & 3) * 16 + 8 * (u & 1) + (lane & 7)) * 4, v);
    }
  }
  STAMPW(0, q < 16 ? 4 : q < 38 ? 5 : q < 49 ? 6 : 7);
}

DI u4 rot(u4 v) { u4 o; for (int j = 0; j < 4; ++j) o[j] = __builtin_amdgcn_alignbit(v[j], v[j], 16) ^ 0x8000u; return o; }
template <int NP>
__global__ __launch_bounds__(256, 2) void k_fft(const u32* __restrict__ xin, const float* __restrict__ st, const u32* __restrict__ filt,
                                                u32* __restrict__ xout) {
  __shared__ __attribute__((aligned(256))) char lds[16384];
  const int col = blockIdx.x, tid = threadIdx.x, lane = tid & 63, w = tid >> 6, l15 = lane & 15, g = lane >> 4;
  constexpr int KI = NP == 1 ? 1 : 4;
  STAMP(KI, 0);
  const u32* xc = xin + col * XI;
  u4 xd[2], xr[2], fw[2];
  f4 sv[NP][2];
#pragma unroll
  for (int i = 0; i < 2; ++i) xd[i] = *(const u4*)(xc + 1024 * i + 4 * tid);
#pragma unroll
  for (int p = 0; p < NP; ++p)
#pragma unroll
    for (int i = 0; i < 2; ++i) sv[p][i] = *(const f4*)(st + p * SS + 1024 * i + 4 * tid);
#pragma unroll
  for (int i = 0; i < 2; ++i) {
    fw[i] = *(const u4*)(filt + col * FS + (2 * w + i) * 256 + lane * 4);
    xr[i] = *(const u4*)(xc + 64 * (16 * i + l15) + 16 * w + 4 * g);
  }
  SB();
  const int k1 = 16 * w + l15;
  const float fk1 = (float)k1, fl = (float)l15, fa = (float)(4 * g);
  u4 F64r[4], F64i[4], F32r[2][2], F32i[2][2];
  float t1c[2][4], t1s[2][4], t2c[2][4], t2s[2][4];
#pragma unroll
  for (int j = 0; j < 4; ++j) {
    float a = (fa + j) * fk1 * (1.f / 64), b = (fa + j) * fl * (1.f / 32);
    F64r[0][j] = pk(__builtin_amdgcn_cosf(a), __builtin_amdgcn_sinf(a));
    F32r[0][0][j] = pk(__builtin_amdgcn_cosf(b), __builtin_amdgcn_sinf(b));
  }
  F64i[0] = rot(F64r[0]); F32i[0][0] = rot(F32r[0][0]);
  const u32 NEG = 0x80008000u, so = (lane & 1) ? NEG : 0u, sr = (lane & 2) ? NEG : 0u, si = ((lane + 1) & 2) ? NEG : 0u;
#pragma unroll
  for (int j = 0; j < 4; ++j) {
    F64r[1][j] = ((lane & 1) ? F64i[0][j] : F64r[0][j]) ^ sr;
    F64i[1][j] = ((lane & 1) ? F64r[0][j] : F64i[0][j]) ^ si;
    F64r[2][j] = F64r[0][j] ^ so; F64i[2][j] = F64i[0][j] ^ so;
    F64r[3][j] = F64r[1][j] ^ so; F64i[3][j] = F64i[1][j] ^ so;
    const u32 sj = (j & 1) ? NEG : 0u;
    F32r[0][1][j] = F32r[0][0][j] ^ so; F32i[0][1][j] = F32i[0][0][j] ^ so;
    F32r[1][0][j] = F32r[0][0][j] ^ sj; F32i[1][0][j] = F32i[0][0][j] ^ sj;
    F32r[1][1][j] = F32r[0][1][j] ^ sj; F32i[1][1][j] = F32i[0][1][j] ^ sj;
  }
#pragma unroll
  for (int t = 0; t < 2; ++t)
#pragma unroll
    for (int j = 0; j < 4; ++j) {
      float a = (fa + (16 * t + j)) * (fk1 * (1.f / 2048)), b = ((fa + (float)(16 * w)) + j) * ((fl + 16 * t) * (1.f / 2048));
      t1c[t][j] = __builtin_amdgcn_cosf(a); t1s[t][j] = __builtin_amdgcn_sinf(a);
      t2c[t][j] = __builtin_amdgcn_cosf(b); t2s[t][j] = __builtin_amdgcn_sinf(b);
    }
  SB();
  STAMP(KI, 1);
  const u32 msk = col == 0 ? 0xffffu : 0xffffffffu;
#pragma unroll
  for (int i = 0; i < 2; ++i) {
    f4 rs = sv[0][i];
    if (NP > 1) {
#pragma unroll
      for (int p = 1; p < NP; ++p) rs += sv[p][i];
#pragma unroll
      for (int j = 0; j < 4; ++j) rs[j] = __builtin_amdgcn_rsqf(rs[j] * (1.f / 512) + EPS) * (1.f / 2048);
    }
    u4 z;
#pragma unroll
    for (int j = 0; j < 4; ++j) z[j] = pk(lo(xd[i][j]) * rs[j], hi(xd[i][j]) * rs[j]) & msk;
    *(u4*)(lds + (16 * i + (tid >> 4)) * 256 + ((l15 ^ (tid >> 4)) << 4)) = z;
  }
  __syncthreads();
  STAMP(KI, 2);
  u4 z[2][4], T[2], Y[2];
  f4 ar[2], ai[2];
  const f4 Z4 = {0, 0, 0, 0};
#pragma unroll
  for (int s = 0; s < 4; ++s)
#pragma unroll
    for (int t = 0; t < 2; ++t) z[t][s] = lds128(lds + (16 * t + l15) * 256 + (((4 * s + g) ^ l15) << 4));
  SB();
#pragma unroll
  for (int t = 0; t < 2; ++t)
#pragma unroll
    for (int s = 0; s < 4; ++s) { ar[t] = mf16(z[t][s], F64r[s], s ? ar[t] : Z4); ai[t] = mf16(z[t][s], F64i[s], s ? ai[t] : Z4); }
#pragma unroll
  for (int t = 0; t < 2; ++t)
#pragma unroll
    for (int j = 0; j < 4; ++j) T[t][j] = pk(ar[t][j] * t1c[t][j] + ai[t][j] * t1s[t][j], ai[t][j] * t1c[t][j] - ar[t][j] * t1s[t][j]);
#pragma unroll
  for (int m = 0; m < 2; ++m)
#pragma unroll
    for (int t = 0; t < 2; ++t) { ar[m] = mf16(F32r[m][t], T[t], t ? ar[m] : Z4); ai[m] = mf16(F32i[m][t], T[t], t ? ai[m] : Z4); }
#pragma unroll
  for (int m = 0; m < 2; ++m)
#pragma unroll
    for (int j = 0; j < 4; ++j) { float a = lo(fw[m][j]), b = hi(fw[m][j]); Y[m][j] = pk(ar[m][j] * a - ai[m][j] * b, -(ar[m][j] * b) - ai[m][j] * a); }
#pragma unroll
  for (int t = 0; t < 2; ++t)
#pragma unroll
    for (int m = 0; m < 2; ++m) { ar[t] = mf16(Y[m], F32r[t][m], m ? ar[t] : Z4); ai[t] = mf16(Y[m], F32i[t][m], m ? ai[t] : Z4); }
#pragma unroll
  for (int t = 0; t < 2; ++t) {
    u4 v;
#pragma unroll
    for (int j = 0; j < 4; ++j) v[j] = pk(ar[t][j] * t2c[t][j] + ai[t][j] * t2s[t][j], ai[t][j] * t2c[t][j] - ar[t][j] * t2s[t][j]);
    *(u4*)(lds + 8192 + (16 * t + l15) * 256 + (((4 * w + g) ^ l15) << 4)) = v;
  }
  __syncthreads();
#pragma unroll
  for (int s = 0; s < 4; ++s)
#pragma unroll
    for (int t = 0; t < 2; ++t) z[t][s] = lds128(lds + 8192 + (16 * t + l15) * 256 + (((4 * s + g) ^ l15) << 4));
  SB();
#pragma unroll
  for (int t = 0; t < 2; ++t)
#pragma unroll
    for (int s = 0; s < 4; ++s) { ar[t] = mf16(F64r[s], z[t][s], s ? ar[t] : Z4); ai[t] = mf16(F64i[s], z[t][s], s ? ai[t] : Z4); }
  const float sg = col == 0 ? 0.f : -1.f;
#pragma unroll
  for (int t = 0; t < 2; ++t) {
    float re[4], im[4];
#pragma unroll
    for (int j = 0; j < 4; ++j) { re[j] = lo(xr[t][j]) + ar[t][j]; im[j] = hi(xr[t][j]) + (col == 0 ? 0.f : sg * ai[t][j]); }
    u32* o = xout + col * (2 * PS) + 32 * (16 * t + l15) + 8 * w + 2 * g;
    *(u2*)o = u2{pk(re[0], re[1]), pk(re[2], re[3])};
    *(u2*)(o + PS) = u2{pk(im[0], im[1]), pk(im[2], im[3])};
  }
  STAMP(KI, 3); STAMPW(KI, 4);
}

template <int LAST>
__global__ __launch_bounds__(512) void k_up(const u32* __restrict__ xh, const u32* __restrict__ wgu,
                                               u32* __restrict__ hb, u32* __restrict__ x3, float* __restrict__ rs2) {
  constexpr int NT = 6 + LAST, CH = NT * 4096 + 16384, NS = LAST ? 3 : 4, NI = NT + 4, PRE = UPPRE;
  __shared__ __attribute__((aligned(1024))) char lds[NS * CH];
  const int xc = blockIdx.x & 7, jb = blockIdx.x >> 3, mt = 8 * (xc & 1) + (jb & 7), ng = 4 * (xc >> 1) + (jb >> 3);
  const int t0 = (ng * 43) >> 3, t1 = ((ng + 1) * 43) >> 3;
  const int lane = threadIdx.x & 63, w8 = __builtin_amdgcn_readfirstlane(threadIdx.x >> 6), w = w8 & 3;
  const int wm = w >> 1, wn = w & 1, r = lane & 31, h = lane >> 5;
  constexpr int KI = LAST ? 5 : 2;
  STAMP(KI, 0);
  if (w8 >= 4) {
    const rsrc rq = mkr(xh), rp = mkr(wgu), rr = mkr((const char*)&R1T + 131072);
    const int vq = (lane >> 4) * (PS * 4) + (((lane & 15) ^ (4 * (lane >> 4))) << 4);
    auto issue = [&](int c, char* sl) {
#pragma unroll
      for (int i = 0; i < 4; ++i) blds(rq, vq, (64 * c + 16 * w + 4 * i) * (PS * 4) + 256 * mt, sl + NT * 4096 + (4 * w + i) * 1024);
#pragma unroll
      for (int i = 0; i < 3; ++i) {
        int e = 6 * w + 2 * i, so = (t0 + (e >> 2)) * 32768 + (4 * c + (e & 3)) * 1024;
        blds(rp, lane * 16, so, sl + e * 1024); blds<1024>(rp, lane * 16, so, sl + e * 1024);
      }
      if (LAST) blds(rr, lane * 16, (ng * 32 + 4 * c + w) * 1024, sl + (24 + w) * 1024);
    };
#pragma unroll
    for (int c = 0; c < PRE; ++c) issue(c, lds + c * CH);
    waitv<(PRE - 1) * NI>(); bar();
#pragma unroll
    for (int c = PRE; c < NS; ++c) issue(c, lds + c * CH);
    const int sOff = NT * 4096 + lane * 256 + ((ng ^ (4 * (lane & 3))) << 4) + 4 * w;
    u32 v;
    asm volatile("ds_read_b32 %0, %1\n s_waitcnt lgkmcnt(0)" : "=v"(v) : "v"((u32)(size_t)(LDSP char*)(lds + sOff)) : "memory");
    v = lane == 1 ? 0u : v;
    float s0 = lo(v) * lo(v), s1 = hi(v) * hi(v);
    int so = 0;
#pragma unroll 1
    for (int c = 0; c < 8; ++c) {
      if (c < 9 - NS) waitv<(NS - 2) * NI>(); else if (NS == 4 && c == 5) waitv<NI>(); else waitv<0>();
      bar();
      if (c < 8 - NS) issue(c + NS, lds + so);
      so = so == (NS - 1) * CH ? 0 : so + CH;
      if (c < 7) {
        asm volatile("ds_read_b32 %0, %1\n s_waitcnt lgkmcnt(0)" : "=v"(v) : "v"((u32)(size_t)(LDSP char*)(lds + so + sOff)) : "memory");
        s0 += lo(v) * lo(v); s1 += hi(v) * hi(v);
      }
    }
#pragma unroll
    for (int o = 1; o < 64; o <<= 1) { s0 += __shfl_xor(s0, o); s1 += __shfl_xor(s1, o); }
    if (lane < 2) rs2[128 * mt + 8 * ng + 2 * w + lane] = 1.f / ((lane ? s1 : s0) * (1.f / 512) + EPS);
    return;
  }
  f16v acc[3][2] = {}, accx = {};
  const int pOff = wn * 12288 + lane * 16;
  int qOff[2];
#pragma unroll
  for (int b = 0; b < 2; ++b)
    qOff[b] = NT * 4096 + (4 * h + ((lane >> 2) & 3)) * 256 + (((4 * (2 * wm + (b ^ wn)) + ((lane >> 3) & 2) + ((lane >> 1) & 1)) ^ (lane & 12)) << 4) + 8 * (lane & 1);
  struct Fr { u4 P[3], Q[2], X; };
  auto rd = [&](const char* sl, int ks) {
    Fr f;
#pragma unroll
    for (int b = 0; b < 2; ++b) f.Q[b] = ldtr(sl + qOff[b] + ks * 4096, 2048);
#pragma unroll
    for (int t = 0; t < 3; ++t) f.P[t] = lds128(sl + pOff + t * 4096 + ks * 1024);
    if (LAST) f.X = lds128(sl + 24576 + ks * 1024 + lane * 16);
    return f;
  };
  auto mm = [&](const Fr& f, int ks) {
#pragma unroll
    for (int t = 0; t < 3; ++t) { acc[t][0] = mf32(f.P[t], f.Q[0], acc[t][0]); acc[t][1] = mf32(f.P[t], f.Q[1], acc[t][1]); }
    if (LAST) accx = mf32(f.X, f.Q[0], accx);
    mix<6 + LAST, 2, 1>();
    SB();
  };
  bar();
  STAMP(KI, 1);
  Fr f0 = rd(lds, 0), f1;
  int so = 0;
#pragma unroll 1
  for (int c = 0; c < 8; ++c) {
    char* sl = lds + so;
    so = so == (NS - 1) * CH ? 0 : so + CH;
    SB();
    f1 = rd(sl, 1); mm(f0, 0);
    f0 = rd(sl, 2); mm(f1, 1);
    f1 = rd(sl, 3); mm(f0, 2);
    bar();
    SB();
    f0 = rd(lds + so, 0); mm(f1, 3);
  }
  STAMP(KI, 3);
#pragma unroll
  for (int t = 0; t < 3; ++t) {
    int tile = t0 + 3 * wn + t;
    if (tile < t1)
#pragma unroll
      for (int b = 0; b < 2; ++b) {
        const f16v& a = acc[t][b];
        u4 o;
#pragma unroll
        for (int j = 0; j < 4; ++j) { int i = 8 * (j >> 1) + 2 * (j & 1); o[j] = pk(fmaxf(a[i], 0.f) * a[i + 4], fmaxf(a[i + 1], 0.f) * a[i + 5]); }
        st4(hb + (((2 * mt + wm) * 2 + (b ^ wn)) * 86 + tile) * 256 + lane * 4, o);
      }
  }
  if (LAST) {
    u32* o = x3 + ((4 * mt + w) * 16 + ng) * 512 + lane * 4;
    st4(o, pack8(accx, 0)); st4(o + 256, pack8(accx, 1));
  }
  STAMP(KI, 4); STAMPW(KI, 5);
}

template <int LAST>
__global__ __launch_bounds__(512) void k_down(const u32* __restrict__ hb, const u32* __restrict__ wd, const float* __restrict__ rs2,
                                              const u32* __restrict__ res, u32* __restrict__ out, float* __restrict__ ssp) {
  __shared__ __attribute__((aligned(1024))) char lds[131072 + 512];
  const int xc = blockIdx.x & 7, jb = blockIdx.x >> 3, rg = 8 * (xc & 3) + (jb & 7), nt = 4 * (xc >> 2) + (jb >> 3);
  const int lane = threadIdx.x & 63, w8 = __builtin_amdgcn_readfirstlane(threadIdx.x >> 6), w = w8 & 3, r = lane & 31, h = lane >> 5;
  constexpr int KI = LAST ? 6 : 3;
  STAMP(KI, 0);
  if (w8 >= 4) {
    const rsrc rs = mkr(w < 2 ? wd + (2 * nt + w) * 86 * 256 : hb + (2 * rg + w - 2) * 86 * 256);
    auto issue = [&](int c) {
      char* sl = lds + (c & 3) * 32768 + w * 8192;
      unroll<4>([&](auto I) { blds<I.v * 1024>(rs, lane * 16, c * 8192, sl); });
      unroll<2>([&](auto I) { blds<I.v * 1024>(rs, lane * 16, c * 8192 + 4096, sl + 4096); });
      if (c < 10) unroll<2>([&](auto I) { blds<I.v * 1024 + 2048>(rs, lane * 16, c * 8192 + 4096, sl + 4096); });
    };
    issue(0); issue(1); issue(2);
    waitv<16>(); bar();
    issue(3);
#pragma unroll 1
    for (int c = 0; c < 10; ++c) {
      if (c < 7) waitv<16>(); else if (c == 7) waitv<14>(); else if (c == 8) waitv<6>(); else waitv<0>();
      bar();
      if (c < 7) issue(c + 4);
    }
    if (!LAST) bar();
    return;
  }
  const int t = w >> 1, b = w & 1, rho = 64 * rg + 32 * b + r;
  u32 rv[8];
  if (LAST) {
    const u32* p = res + ((2 * rg + b) * 16 + 2 * nt + t) * 512 + lane * 4;
    u4 x = *(const u4*)p, y = *(const u4*)(p + 256);
#pragma unroll
    for (int j = 0; j < 4; ++j) { rv[j] = x[j]; rv[4 + j] = y[j]; }
  } else {
    const u16* p = (const u16*)res + (64 * nt + 32 * t + 4 * h) * (2 * PS) + rho;
#pragma unroll
    for (int j = 0; j < 8; ++j) { const u16* e = p + (8 * (j >> 1) + 2 * (j & 1)) * (2 * PS); rv[j] = (u32)e[0] | ((u32)e[2 * PS] << 16); }
  }
  const float rs = rs2[rho];
  struct Fr { u4 A[4], B[4]; };
  const int aOff = t * 8192 + lane * 16, bOff = 16384 + b * 8192 + lane * 16;
  auto rd = [&](const char* sl, auto N) {
    Fr f;
#pragma unroll
    for (int k = 0; k < N.v; ++k) { f.A[k] = lds128(sl + aOff + k * 1024); f.B[k] = lds128(sl + bOff + k * 1024); }
    return f;
  };
  f16v acc = {};
  auto mm = [&](const Fr& f, auto N) {
#pragma unroll
    for (int k = 0; k < N.v; ++k) acc = mf32(f.A[k], f.B[k], acc);
    mix<N.v, 2, 1>();
    SB();
  };
  bar();
  STAMP(KI, 1);
  Fr f0 = rd(lds, IC<4>{}), f1;
#pragma unroll 1
  for (int c = 0; c < 10; ++c) {
    SB();
    f1 = rd(lds + (c & 3) * 32768 + 4096, IC<4>{}); mm(f0, IC<4>{});
    bar();
    SB();
    f0 = rd(lds + ((c + 1) & 3) * 32768, IC<4>{}); mm(f1, IC<4>{});
  }
  f1 = rd(lds + 2 * 32768 + 4096, IC<2>{}); mm(f0, IC<4>{});
  mm(f1, IC<2>{});
  STAMP(KI, 2);
  u32 v[8];
#pragma unroll
  for (int j = 0; j < 8; ++j) v[j] = pk(lo(rv[j]) + rs * acc[2 * j], hi(rv[j]) + rs * acc[2 * j + 1]);
  if (LAST) {
    u32* o = out + (32 * (32 * b + r) + rg) * 256 + 32 * nt + 16 * t + 8 * h;
    *(u4*)o = u4{v[0], v[1], v[2], v[3]}; *(u4*)(o + 4) = u4{v[4], v[5], v[6], v[7]};
  } else {
    const int c0 = 32 * nt + 16 * t + 2 * h;
    float ss = 0.f;
#pragma unroll
    for (int j = 0; j < 8; ++j) {
      st1(out + (c0 + 4 * (j >> 1) + (j & 1)) * XI + rho, v[j]);
      u32 m = (j == 0 && c0 == 0) ? (v[j] & 0xffffu) : v[j];
      ss = dot2(m, m, ss);
    }
    ss += __shfl_xor(ss, 32);
    float* sl = (float*)(lds + 131072);
    if (h == 0) sl[w * 32 + r] = ss;
    bar();
    if (w == 0) ssp[nt * SS + 64 * rg + lane] = sl[lane] + sl[64 + lane];
  }
  STAMP(KI, 3); STAMPW(KI, 4);
}

extern "C" void kernel_launch(void* const* d_in, const int* in_sizes, int n_in, void* d_out, int out_size, void* d_ws, size_t ws_size,
                              hipStream_t stream) {
  const u16 *x = (const u16*)d_in[0], *wr = (const u16*)d_in[1], *wi = (const u16*)d_in[2];
  const u16 *wg = (const u16*)d_in[4], *wu = (const u16*)d_in[5], *wd = (const u16*)d_in[6];
  char* ws = (char*)d_ws;
  const size_t MB = 1 << 20;
  u32 *xh0 = (u32*)ws, *xh1 = (u32*)(ws + 3 * MB), *xh2 = (u32*)(ws + 6 * MB), *xh3 = (u32*)(ws + 9 * MB);
  u32 *filt = (u32*)(ws + 12 * MB), *x3 = (u32*)(ws + 15 * MB), *wgu = (u32*)(ws + 17 * MB), *wd1 = (u32*)(ws + 20 * MB);
  u32 *wd2 = (u32*)(ws + 22 * MB), *hb = (u32*)(ws + 24 * MB), *hb2 = (u32*)(ws + 30 * MB);
  float *rstd = (float*)(ws + 36 * MB), *ssp = rstd + 4096, *rs2 = rstd + 32768, *rs3 = rstd + 65536;
  k_prep<<<260, 512, 0, stream>>>(x, wr, wi, wg, wu, wd, xh0, (u32*)rstd, wgu, wd1, wd2, filt, (const u32*)d_out);
  k_fft<1><<<256, 256, 0, stream>>>(xh0, rstd, filt, xh1);
  k_up<0><<<256, 512, 0, stream>>>(xh1, wgu, hb, x3, rs2);
  k_down<0><<<256, 512, 0, stream>>>(hb, wd1, rs2, xh1, xh2, ssp);
  k_fft<8><<<256, 256, 0, stream>>>(xh2, ssp, filt, xh3);
  k_up<1><<<256, 512, 0, stream>>>(xh3, wgu, hb2, x3, rs3);
  k_down<1><<<256, 512, 0, stream>>>(hb2, wd2, rs3, x3, (u32*)d_out, ssp);
}
```

```cpp
#include <hip/hip_runtime.h>
#include <hip/hip_bf16.h>

typedef unsigned u32;
typedef unsigned short u16;
typedef __attribute__((ext_vector_type(8))) __bf16 bf8;
typedef __attribute__((ext_vector_type(2))) __bf16 bf2;
typedef __attribute__((ext_vector_type(2))) float f2;
typedef __attribute__((ext_vector_type(4))) float f4;
typedef __attribute__((ext_vector_type(16))) float f16v;
typedef __attribute__((ext_vector_type(2))) u32 u2;
typedef __attribute__((ext_vector_type(4))) u32 u4;
typedef __attribute__((ext_vector_type(4))) short s4;
typedef u4 __attribute__((aligned(2))) u4_ua;
#define DI __device__ __forceinline__
#define LDSP __attribute__((address_space(3)))
#define GLBP __attribute__((address_space(1)))

#define STAMP(k, j)
#define STAMPW(k, j)
#define ROT 0
#define UPPRE 2
constexpr int SS = 2048;
constexpr int XI = 2112, PS = 1088, FS = 2080;
constexpr float EPS = 1e-5f;

struct Part { u16 v[16384]; };
#if defined(__HIP_DEVICE_COMPILE__)
constexpr double kPi = 3.14159265358979323846;
constexpr double csin(double x) { double t = x, s = x; for (int i = 1; i < 14; ++i) { t *= -x * x / ((2 * i) * (2 * i + 1)); s += t; } return s; }
constexpr u16 tobf(double x) { float f = (float)x; u32 u = __builtin_bit_cast(u32, f); u += 0x7fffu + ((u >> 16) & 1u); return (u16)(u >> 16); }
struct CosT { u16 v[512]; };
constexpr CosT mkcos() {
  CosT c{};
  for (int i = 0; i < 512; ++i) {
    int q = (i + 128) & 511;
    int y = q <= 128 ? q : (q <= 384 ? 256 - q : q - 512);
    c.v[i] = y == 0 ? (u16)0 : tobf(csin(2 * kPi * y / 512) / 16.0);
  }
  return c;
}
constexpr CosT kCos = mkcos();
constexpr u16 kDC = tobf(0.04419417382415922);
constexpr Part mkR(int pi) {
  Part p{};
  for (int e = 0; e < 16384; ++e) {
    int r = (e >> 3) & 31, h = (e >> 8) & 1, j = e & 7, d = 0, s = 0;
    if (pi < 4) { d = (((e >> 9) & 7) << 4) + 8 * h + j; s = 32 * (4 * pi + (e >> 12)) + r; }
    else { d = 32 * (pi - 4) + (r & 3) + 4 * (r >> 3) + 16 * ((r >> 2) & 1); s = ((e >> 9) << 4) + 8 * (j >> 2) + 4 * h + (j & 3); }
    p.v[e] = s >= 2 ? kCos.v[((s >> 1) * d + ((s & 1) << 7)) & 511] : (u16)(kDC | ((s == 0 && (d & 1)) ? 0x8000 : 0));
  }
  return p;
}
template <int I> struct PR { static constexpr Part v = mkR(I); };
struct Tab { Part p[20]; };
__device__ const __attribute__((aligned(1024))) Tab R1T = {{PR<0>::v, PR<1>::v, PR<2>::v, PR<3>::v, PR<4>::v, PR<5>::v, PR<6>::v, PR<7>::v,
  PR<8>::v, PR<9>::v, PR<10>::v, PR<11>::v, PR<12>::v, PR<13>::v, PR<14>::v, PR<15>::v, PR<16>::v, PR<17>::v, PR<18>::v, PR<19>::v}};
#else
struct Tab { u16 p[8]; };
__device__ const Tab R1T = {};
#endif

DI f16v mf32(u4 a, u4 b, f16v c) { return __builtin_amdgcn_mfma_f32_32x32x16_bf16(__builtin_bit_cast(bf8, a), __builtin_bit_cast(bf8, b), c, 0, 0, 0); }
DI f4 mf16(u4 a, u4 b, f4 c) { return __builtin_amdgcn_mfma_f32_16x16x32_bf16(__builtin_bit_cast(bf8, a), __builtin_bit_cast(bf8, b), c, 0, 0, 0); }
DI u32 pk(float a, float b) { f2 v = {a, b}; return __builtin_bit_cast(u32, __builtin_convertvector(v, bf2)); }
DI float lo(u32 u) { return __builtin_bit_cast(float, u << 16); }
DI float hi(u32 u) { return __builtin_bit_cast(float, u & 0xffff0000u); }
DI float dot2(u32 a, u32 b, float c) { return __builtin_amdgcn_fdot2_f32_bf16(__builtin_bit_cast(bf2, a), __builtin_bit_cast(bf2, b), c, false); }
DI void glds(const void* g, char* l) { __builtin_amdgcn_global_load_lds((const GLBP void*)g, (LDSP void*)l, 16, 0, 0); }
typedef __amdgpu_buffer_rsrc_t rsrc;
DI rsrc mkr(const void* p) { return __builtin_amdgcn_make_buffer_rsrc((void*)p, 0, 0x7fffffff, 0x00020000); }
template <int O = 0> DI void blds(rsrc r, int vo, int so, char* l) { __builtin_amdgcn_raw_ptr_buffer_load_lds(r, (LDSP void*)l, 16, vo, so, O, 0); }
DI u4 ldtr(const char* a, int d) {
  u2 x = __builtin_bit_cast(u2, __builtin_amdgcn_ds_read_tr16_b64_v4i16((LDSP s4*)a));
  u2 y = __builtin_bit_cast(u2, __builtin_amdgcn_ds_read_tr16_b64_v4i16((LDSP s4*)(a + d)));
  return u4{x[0], x[1], y[0], y[1]};
}
#define WT 2
DI void st4(u32* p, u4 v) {
  if (WT == 2) asm volatile("global_store_dwordx4 %0, %1, off sc0 sc1\n s_nop 1" ::"v"(p), "v"(v) : "memory");
  else if (WT == 1) __builtin_nontemporal_store(v, (u4*)p);
  else *(u4*)p = v;
}
DI void st2(u32* p, u2 v) {
  if (WT == 2) asm volatile("global_store_dwordx2 %0, %1, off sc0 sc1" ::"v"(p), "v"(v) : "memory");
  else if (WT == 1) __builtin_nontemporal_store(v, (u2*)p);
  else *(u2*)p = v;
}
DI void st1(u32* p, u32 v) {
  if (WT == 2) asm volatile("global_store_dword %0, %1, off sc0 sc1" ::"v"(p), "v"(v) : "memory");
  else if (WT == 1) __builtin_nontemporal_store(v, p);
  else *p = v;
}
DI u4 lds128(const char* p) { return *(const u4*)p; }
DI u4 pack8(const f16v& a, int s) { u4 o = {pk(a[8 * s], a[8 * s + 1]), pk(a[8 * s + 2], a[8 * s + 3]), pk(a[8 * s + 4], a[8 * s + 5]), pk(a[8 * s + 6], a[8 * s + 7])}; return o; }
template <int N> DI void waitv() { asm volatile("s_waitcnt vmcnt(%0)" ::"n"(N) : "memory"); }
DI void bar() { asm volatile("s_waitcnt lgkmcnt(0)" ::: "memory"); __builtin_amdgcn_s_barrier(); asm volatile("" ::: "memory"); }
DI int dperm(int m) { return (m & 3) + 4 * (m >> 3) + 16 * ((m >> 2) & 1); }
DI int fperm(int m) { int i = (m & 3) + 4 * (m >> 3); return 16 * (i >> 3) + 8 * ((m >> 2) & 1) + (i & 7); }
#define SB() __builtin_amdgcn_sched_barrier(0)
#define SGB(m, n) __builtin_amdgcn_sched_group_barrier(m, n, 0)
template <int NM, int ND, int NV> DI void mix() { if constexpr (NM > 0) { SGB(0x008, 1); SGB(0x100, ND); SGB(0x002, NV); mix<NM - 1, ND, NV>(); } }
template <int I> struct IC { static constexpr int v = I; };
template <int N, class F> DI void unroll(F&& f) { if constexpr (N > 0) { unroll<N - 1>(f); f(IC<N - 1>{}); } }

template <int JOB>
DI void prep_gemm(const u16* __restrict__ q0, const u16* __restrict__ q1, int qt, int nt, int c0, char* lds,
                  u32* __restrict__ o0, u32* __restrict__ o1) {
  const int lane = threadIdx.x & 63, w8 = __builtin_amdgcn_readfirstlane(threadIdx.x >> 6), w = w8 & 3;
  const int wp = w >> 1, wq = w & 1, r = lane & 31, h = lane >> 5;
  if (w8 >= 4) {
    const rsrc rp = mkr(&R1T), ra = mkr(q0), rb = mkr(q1);
    int qv[4];
#pragma unroll
    for (int i = 0; i < 4; ++i) {
      if (JOB == 1) {
        int d = 16 * w + 8 * (i >> 1) + (lane >> 3), pos = lane & 7;
        int f = 64 * qt + 16 * ((pos >> 1) ^ (d & 3) ^ (i & 1)) + 8 * (pos & 1);
        qv[i] = (d * 1376 + (f > 1368 ? 1368 : f)) * 2;
      } else {
        int u = 32 * w + 8 * i + (lane >> 3), row;
        if (JOB == 0) { int rho = 128 * qt + (u & 64) + 2 * (u & 31) + ((u >> 5) & 1); row = 32 * (rho & 63) + (rho >> 6); }
        else { row = 128 * qt + (u & 96) + fperm(u & 31); row = row > 1375 ? 1375 : row; }
        qv[i] = row * 1024 + (((lane & 7) ^ ((u >> 1) & 7)) << 4);
      }
    }
    constexpr int QSTEP = JOB == 1 ? 64 * 1376 * 2 : 128;
    auto issP = [&](int c) { unroll<4>([&](auto I) { blds<I.v * 1024>(rp, lane * 16, (4 * nt + w) * 8192 + c * 4096, lds + w * 8192 + c * 4096); }); };
    auto issue = [&](int cc) {
      const int c = (cc + c0) & 7;
#pragma unroll
      for (int i = 0; i < 4; ++i) blds((JOB == 1 && (i & 1)) ? rb : ra, qv[i], c * QSTEP, lds + 32768 + c * 16384 + (4 * w + i) * 1024);
    };
    issP(0); issP(1); issue(0); issue(1); issue(2);
    waitv<8>(); bar();
#pragma unroll 1
    for (int c = 3; c < 8; ++c) issue(c);
    unroll<8>([&](auto I) { waitv<(I.v < 6 ? 4 * (6 - I.v) : 0)>(); bar(); });
    if (JOB == 0) {
      const int u = 32 * nt + 8 * w + (lane >> 3);
      float s1 = 0.f, s2 = 0.f;
#pragma unroll
      for (int c = 0; c < 8; ++c) {
        u4 v = lds128(lds + 32768 + c * 16384 + u * 128 + (lane & 7) * 16);
#pragma unroll
        for (int j = 0; j < 4; ++j) { s1 = dot2(v[j], 0x3f803f80u, s1); s2 = dot2(v[j], v[j], s2); }
      }
#pragma unroll
      for (int o = 1; o < 8; o <<= 1) { s1 += __shfl_xor(s1, o); s2 += __shfl_xor(s2, o); }
      float m = s1 * (1.f / 512), var = fmaxf(s2 * (1.f / 512) - m * m, 0.f);
      if ((lane & 7) == 0) ((float*)o1)[128 * qt + (u & 64) + 2 * (u & 31) + ((u >> 5) & 1)] = __builtin_amdgcn_rsqf(var + EPS) * (1.f / 2048);
    }
    return;
  }
  f16v acc[2][2] = {}, acc2[2][2] = {};
  int qOff;
  if (JOB == 1) {
    int g = lane >> 4, qr = (lane >> 2) & 3, run = 4 * (g & 1) + (lane & 3), q = run >> 1;
    qOff = 32768 + (g >> 1) * 2048 + qr * 128 + (q & 1) * 1024 + (8 * (run & 1) + 4 * (q >> 1)) * 2;
  } else qOff = 32768 + (64 * wq + r) * 128;
  struct Fr { u4 v[2]; };
  int pOff; u32 sgn;
  auto turn = [&](int c) {
    int n = ((r >> 1) * (c >> 1)) & 3;
    pOff = wp * 16384 + (c & 1) * 4096 + (lane ^ (n & 1)) * 16;
    sgn = (((r & 1) ? n + 1 : n) & 2) ? 0x80008000u : 0u;
  };
  auto rdP = [&](int ks) { return Fr{{lds128(lds + pOff + ks * 1024), lds128(lds + pOff + 8192 + ks * 1024)}}; };
  auto rdQ = [&](const char* sl, int ks) {
    Fr f;
#pragma unroll
    for (int b = 0; b < 2; ++b) {
      if (JOB == 1) {
        f.v[b] = ldtr(sl + qOff + ks * 4096 + (((2 * wq + b) ^ ((lane >> 2) & 3) ^ ((lane >> 1) & 1)) << 5), 512);
      } else f.v[b] = lds128(sl + qOff + b * 4096 + (((2 * ks + h) ^ ((r >> 1) & 7)) << 4));
    }
    return f;
  };
  u4 Id[2];
  if (JOB == 2) {
#pragma unroll
    for (int k = 0; k < 2; ++k) {
      int e = dperm(r) - 16 * k - 8 * h;
#pragma unroll
      for (int j = 0; j < 4; ++j) Id[k][j] = (e == 2 * j ? 0x3f80u : 0u) | (e == 2 * j + 1 ? 0x3f800000u : 0u);
    }
  }
  auto mm = [&](const Fr& P, const Fr& Q, Fr& N, u32 sg, int c, int ks) {
#pragma unroll
    for (int t = 0; t < 2; ++t)
#pragma unroll
      for (int b = 0; b < 2; ++b) acc[t][b] = JOB == 2 ? mf32(Q.v[b], P.v[t], acc[t][b]) : mf32(P.v[t], Q.v[b], acc[t][b]);
    N.v[0] ^= sg; N.v[1] ^= sg;
    SGB(0x008, 1); SGB(0x100, 3); SGB(0x008, 1); SGB(0x100, 3); SGB(0x002, 4); SGB(0x008, 1); SGB(0x002, 4); SGB(0x008, 1);
    asm volatile("" : "+v"(N.v[0]), "+v"(N.v[1]));
    SB();
    if (JOB == 2) {
      if (c == 2 * nt + wp) {
#pragma unroll
        for (int b = 0; b < 2; ++b) acc2[ks >> 1][b] = mf32(Q.v[b], Id[ks & 1], acc2[ks >> 1][b]);
      }
    }
  };
  turn(c0);
  bar();
  STAMP(0, 2);
  Fr qa = rdQ(lds + c0 * 16384, 0), pa = rdP(0), pb = rdP(1), qb, pc, pd;
  pa.v[0] ^= sgn; pa.v[1] ^= sgn;
#pragma unroll 1
  for (int cc = 0; cc < 8; ++cc) {
    const int c = (cc + c0) & 7;
    const char* sl = lds + c * 16384;
    const u32 sg = sgn;
    SB();
    qb = rdQ(sl, 1); pc = rdP(2); mm(pa, qa, pb, sg, c, 0);
    qa = rdQ(sl, 2); pd = rdP(3); mm(pb, qb, pc, sg, c, 1);
    turn((c + 1) & 7);
    SB();
    qb = rdQ(sl, 3); pa = rdP(0); mm(pc, qa, pd, sg, c, 2);
    asm volatile("" ::: "memory"); __builtin_amdgcn_s_barrier(); asm volatile("" ::: "memory");
    SB();
    qa = rdQ(lds + ((c + 1) & 7) * 16384, 0); pb = rdP(1); mm(pd, qb, pa, sgn, c, 3);
  }
  STAMP(0, 3);
  if (JOB == 0) {
#pragma unroll
    for (int t = 0; t < 2; ++t)
#pragma unroll
      for (int i = 0; i < 16; i += 2) {
        int col = 64 * nt + 32 * wp + 16 * t + (((i & 3) + 8 * (i >> 2) + 4 * h) >> 1);
        u2 v = {pk(acc[t][0][i], acc[t][0][i + 1]), pk(acc[t][1][i], acc[t][1][i + 1])};
        st2(o0 + col * XI + 128 * qt + 64 * wq + 2 * r, v);
      }
  } else if (JOB == 1) {
#pragma unroll
    for (int b = 0; b < 2; ++b) {
      int tile = 4 * qt + 2 * wq + b;
      if (tile < 86)
#pragma unroll
        for (int t = 0; t < 2; ++t)
#pragma unroll
          for (int s = 0; s < 2; ++s) {
            int ks = 8 * nt + 4 * wp + 2 * t + s;
            u4 v = pack8(acc[t][b], s);
            if (ks == 0 && h == 0) v[0] &= 0xffffu;
            st4(o0 + (tile * 32 + ks) * 256 + lane * 4, v);
          }
    }
  } else {
#pragma unroll
    for (int b = 0; b < 2; ++b)
#pragma unroll
      for (int s = 0; s < 2; ++s) {
        int ks = 8 * qt + 4 * wq + 2 * b + s;
        if (ks < 86)
#pragma unroll
          for (int t = 0; t < 2; ++t) {
            int o = ((4 * nt + 2 * wp + t) * 86 + ks) * 256 + lane * 4;
            st4(o0 + o, pack8(acc[t][b], s));
            st4(o1 + o, pack8(acc2[t][b], s));
          }
      }
  }
}

__global__ __launch_bounds__(512) void k_prep(const u16* __restrict__ x, const u16* __restrict__ wr, const u16* __restrict__ wi,
    const u16* __restrict__ wg, const u16* __restrict__ wu, const u16* __restrict__ wd,
    u32* __restrict__ xh, u32* __restrict__ rstd, u32* __restrict__ wgu, u32* __restrict__ wd1, u32* __restrict__ wd2,
    u32* __restrict__ filt, const u32* __restrict__ dout) {
  __shared__ __attribute__((aligned(1024))) char lds[163840];
  const int b = blockIdx.x, nt = (b >> 3) & 3, q = 8 * (b >> 5) + (b & 7), c0 = ROT;
  const int lane = threadIdx.x & 63, w = __builtin_amdgcn_readfirstlane(threadIdx.x >> 6);
  STAMP(0, 0);
  if (q < 16) prep_gemm<0>(x, x, q, nt, c0, lds, xh, rstd);
  else if (q < 38) prep_gemm<1>(wg, wu, q - 16, nt, 0, lds, wgu, wgu);
  else if (q < 49) prep_gemm<2>(wd, wd, q - 38, nt, c0, lds, wd1, wd2);
  else {
    const int u = b < 256 ? (q - 49) * 4 + nt : b - 196, k0 = 16 * (u >> 4) + 8 * (u & 1) + 256 * ((u >> 1) & 7);
    const rsrc rs = mkr(w < 4 ? wr : wi);
    const int so = (k0 + 64 * (w & 3)) * 514;
    unroll<4>([&](auto I) { blds<I.v * 1024>(rs, lane * 16, so, lds + w * 4160); });
    if (lane == 0) blds(rs, 0, so + 4096, lds + w * 4160 + 4096);
    blds(mkr((const char*)&R1T + 131072), lane * 16, (u * 8 + w) * 1024, lds + 40960 + w * 1024);
    waitv<0>(); bar();
    STAMP(0, 1);
#pragma unroll
    for (int it = 0; it < 4; ++it) {
      const int cs = 1 + 32 * w + 8 * it + (lane >> 3);
      const u16* p = (const u16*)(lds + (lane & 7) * 514) + cs;
      u4 v;
#pragma unroll
      for (int j = 0; j < 4; ++j) v[j] = (u32)p[j * 2080] | ((u32)p[(4 + j) * 2080] << 16);
      st4(filt + (cs & 255) * FS + (u >> 3) * 256 + (((u >> 1) & 3) * 16 + 8 * (u & 1) + (lane & 7)) * 4, v);
    }
  }
  STAMPW(0, q < 16 ? 4 : q < 38 ? 5 : q < 49 ? 6 : 7);
}

DI u4 rot(u4 v) { u4 o; for (int j = 0; j < 4; ++j) o[j] = __builtin_amdgcn_alignbit(v[j], v[j], 16) ^ 0x8000u; return o; }
template <int NP>
__global__ __launch_bounds__(256, 2) void k_fft(const u32* __restrict__ xin, const float* __restrict__ st, const u32* __restrict__ filt,
                                                u32* __restrict__ xout) {
  __shared__ __attribute__((aligned(256))) char lds[16384];
  const int col = blockIdx.x, tid = threadIdx.x, lane = tid & 63, w = tid >> 6, l15 = lane & 15, g = lane >> 4;
  constexpr int KI = NP == 1 ? 1 : 4;
  STAMP(KI, 0);
  const u32* xc = xin + col * XI;
  u4 xd[2], xr[2], fw[2];
  f4 sv[NP][2];
#pragma unroll
  for (int i = 0; i < 2; ++i) xd[i] = *(const u4*)(xc + 1024 * i + 4 * tid);
#pragma unroll
  for (int p = 0; p < NP; ++p)
#pragma unroll
    for (int i = 0; i < 2; ++i) sv[p][i] = *(const f4*)(st + p * SS + 1024 * i + 4 * tid);
#pragma unroll
  for (int i = 0; i < 2; ++i) {
    fw[i] = *(const u4*)(filt + col * FS + (2 * w + i) * 256 + lane * 4);
    xr[i] = *(const u4*)(xc + 64 * (16 * i + l15) + 16 * w + 4 * g);
  }
  SB();
  const int k1 = 16 * w + l15;
  const float fk1 = (float)k1, fl = (float)l15, fa = (float)(4 * g);
  u4 F64r[4], F64i[4], F32r[2][2], F32i[2][2];
  float t1c[2][4], t1s[2][4], t2c[2][4], t2s[2][4];
#pragma unroll
  for (int j = 0; j < 4; ++j) {
    float a = (fa + j) * fk1 * (1.f / 64), b = (fa + j) * fl * (1.f / 32);
    F64r[0][j] = pk(__builtin_amdgcn_cosf(a), __builtin_amdgcn_sinf(a));
    F32r[0][0][j] = pk(__builtin_amdgcn_cosf(b), __builtin_amdgcn_sinf(b));
  }
  F64i[0] = rot(F64r[0]); F32i[0][0] = rot(F32r[0][0]);
  const u32 NEG = 0x80008000u, so = (lane & 1) ? NEG : 0u, sr = (lane & 2) ? NEG : 0u, si = ((lane + 1) & 2) ? NEG : 0u;
#pragma unroll
  for (int j = 0; j < 4; ++j) {
    F64r[1][j] = ((lane & 1) ? F64i[0][j] : F64r[0][j]) ^ sr;
    F64i[1][j] = ((lane & 1) ? F64r[0][j] : F64i[0][j]) ^ si;
    F64r[2][j] = F64r[0][j] ^ so; F64i[2][j] = F64i[0][j] ^ so;
    F64r[3][j] = F64r[1][j] ^ so; F64i[3][j] = F64i[1][j] ^ so;
    const u32 sj = (j & 1) ? NEG : 0u;
    F32r[0][1][j] = F32r[0][0][j] ^ so; F32i[0][1][j] = F32i[0][0][j] ^ so;
    F32r[1][0][j] = F32r[0][0][j] ^ sj; F32i[1][0][j] = F32i[0][0][j] ^ sj;
    F32r[1][1][j] = F32r[0][1][j] ^ sj; F32i[1][1][j] = F32i[0][1][j] ^ sj;
  }
#pragma unroll
  for (int t = 0; t < 2; ++t)
#pragma unroll
    for (int j = 0; j < 4; ++j) {
      float a = (fa + (16 * t + j)) * (fk1 * (1.f / 2048)), b = ((fa + (float)(16 * w)) + j) * ((fl + 16 * t) * (1.f / 2048));
      t1c[t][j] = __builtin_amdgcn_cosf(a); t1s[t][j] = __builtin_amdgcn_sinf(a);
      t2c[t][j] = __builtin_amdgcn_cosf(b); t2s[t][j] = __builtin_amdgcn_sinf(b);
    }
  SB();
  STAMP(KI, 1);
  const u32 msk = col == 0 ? 0xffffu : 0xffffffffu;
#pragma unroll
  for (int i = 0; i < 2; ++i) {
    f4 rs = sv[0][i];
    if (NP > 1) {
#pragma unroll
      for (int p = 1; p < NP; ++p) rs += sv[p][i];
#pragma unroll
      for (int j = 0; j < 4; ++j) rs[j] = __builtin_amdgcn_rsqf(rs[j] * (1.f / 512) + EPS) * (1.f / 2048);
    }
    u4 z;
#pragma unroll
    for (int j = 0; j < 4; ++j) z[j] = pk(lo(xd[i][j]) * rs[j], hi(xd[i][j]) * rs[j]) & msk;
    *(u4*)(lds + (16 * i + (tid >> 4)) * 256 + ((l15 ^ (tid >> 4)) << 4)) = z;
  }
  __syncthreads();
  STAMP(KI, 2);
  u4 z[2][4], T[2], Y[2];
  f4 ar[2], ai[2];
  const f4 Z4 = {0, 0, 0, 0};
#pragma unroll
  for (int s = 0; s < 4; ++s)
#pragma unroll
    for (int t = 0; t < 2; ++t) z[t][s] = lds128(lds + (16 * t + l15) * 256 + (((4 * s + g) ^ l15) << 4));
  SB();
#pragma unroll
  for (int t = 0; t < 2; ++t)
#pragma unroll
    for (int s = 0; s < 4; ++s) { ar[t] = mf16(z[t][s], F64r[s], s ? ar[t] : Z4); ai[t] = mf16(z[t][s], F64i[s], s ? ai[t] : Z4); }
#pragma unroll
  for (int t = 0; t < 2; ++t)
#pragma unroll
    for (int j = 0; j < 4; ++j) T[t][j] = pk(ar[t][j] * t1c[t][j] + ai[t][j] * t1s[t][j], ai[t][j] * t1c[t][j] - ar[t][j] * t1s[t][j]);
#pragma unroll
  for (int m = 0; m < 2; ++m)
#pragma unroll
    for (int t = 0; t < 2; ++t) { ar[m] = mf16(F32r[m][t], T[t], t ? ar[m] : Z4); ai[m] = mf16(F32i[m][t], T[t], t ? ai[m] : Z4); }
#pragma unroll
  for (int m = 0; m < 2; ++m)
#pragma unroll
    for (int j = 0; j < 4; ++j) { float a = lo(fw[m][j]), b = hi(fw[m][j]); Y[m][j] = pk(ar[m][j] * a - ai[m][j] * b, -(ar[m][j] * b) - ai[m][j] * a); }
#pragma unroll
  for (int t = 0; t < 2; ++t)
#pragma unroll
    for (int m = 0; m < 2; ++m) { ar[t] = mf16(Y[m], F32r[t][m], m ? ar[t] : Z4); ai[t] = mf16(Y[m], F32i[t][m], m ? ai[t] : Z4); }
#pragma unroll
  for (int t = 0; t < 2; ++t) {
    u4 v;
#pragma unroll
    for (int j = 0; j < 4; ++j) v[j] = pk(ar[t][j] * t2c[t][j] + ai[t][j] * t2s[t][j], ai[t][j] * t2c[t][j] - ar[t][j] * t2s[t][j]);
    *(u4*)(lds + 8192 + (16 * t + l15) * 256 + (((4 * w + g) ^ l15) << 4)) = v;
  }
  __syncthreads();
#pragma unroll
  for (int s = 0; s < 4; ++s)
#pragma unroll
    for (int t = 0; t < 2; ++t) z[t][s] = lds128(lds + 8192 + (16 * t + l15) * 256 + (((4 * s + g) ^ l15) << 4));
  SB();
#pragma unroll
  for (int t = 0; t < 2; ++t)
#pragma unroll
    for (int s = 0; s < 4; ++s) { ar[t] = mf16(F64r[s], z[t][s], s ? ar[t] : Z4); ai[t] = mf16(F64i[s], z[t][s], s ? ai[t] : Z4); }
  const float sg = col == 0 ? 0.f : -1.f;
#pragma unroll
  for (int t = 0; t < 2; ++t) {
    float re[4], im[4];
#pragma unroll
    for (int j = 0; j < 4; ++j) { re[j] = lo(xr[t][j]) + ar[t][j]; im[j] = hi(xr[t][j]) + (col == 0 ? 0.f : sg * ai[t][j]); }
    u32* o = xout + col * (2 * PS) + 32 * (16 * t + l15) + 8 * w + 2 * g;
    *(u2*)o = u2{pk(re[0], re[1]), pk(re[2], re[3])};
    *(u2*)(o + PS) = u2{pk(im[0], im[1]), pk(im[2], im[3])};
  }
  STAMP(KI, 3); STAMPW(KI, 4);
}

template <int LAST>
__global__ __launch_bounds__(512) void k_up(const u32* __restrict__ xh, const u32* __restrict__ wgu,
                                               u32* __restrict__ hb, u32* __restrict__ x3, float* __restrict__ rs2) {
  constexpr int NT = 6 + LAST, CH = NT * 4096 + 16384, NS = LAST ? 3 : 4, NI = NT + 4, PRE = UPPRE;
  __shared__ __attribute__((aligned(1024))) char lds[NS * CH];
  const int xc = blockIdx.x & 7, jb = blockIdx.x >> 3, mt = 8 * (xc & 1) + (jb & 7), ng = 4 * (xc >> 1) + (jb >> 3);
  const int t0 = (ng * 43) >> 3, t1 = ((ng + 1) * 43) >> 3;
  const int lane = threadIdx.x & 63, w8 = __builtin_amdgcn_readfirstlane(threadIdx.x >> 6), w = w8 & 3;
  const int wm = w >> 1, wn = w & 1, r = lane & 31, h = lane >> 5;
  constexpr int KI = LAST ? 5 : 2;
  STAMP(KI, 0);
  if (w8 >= 4) {
    const rsrc rq = mkr(xh), rp = mkr(wgu), rr = mkr((const char*)&R1T + 131072);
    const int vq = (lane >> 4) * (PS * 4) + (((lane & 15) ^ (4 * (lane >> 4))) << 4);
    auto issue = [&](int c, char* sl) {
#pragma unroll
      for (int i = 0; i < 4; ++i) blds(rq, vq, (64 * c + 16 * w + 4 * i) * (PS * 4) + 256 * mt, sl + NT * 4096 + (4 * w + i) * 1024);
#pragma unroll
      for (int i = 0; i < 3; ++i) {
        int e = 6 * w + 2 * i, so = (t0 + (e >> 2)) * 32768 + (4 * c + (e & 3)) * 1024;
        blds(rp, lane * 16, so, sl + e * 1024); blds<1024>(rp, lane * 16, so, sl + e * 1024);
      }
      if (LAST) blds(rr, lane * 16, (ng * 32 + 4 * c + w) * 1024, sl + (24 + w) * 1024);
    };
#pragma unroll
    for (int c = 0; c < PRE; ++c) issue(c, lds + c * CH);
    waitv<(PRE - 1) * NI>(); bar();
#pragma unroll
    for (int c = PRE; c < NS; ++c) issue(c, lds + c * CH);
    const int sOff = NT * 4096 + lane * 256 + ((ng ^ (4 * (lane & 3))) << 4) + 4 * w;
    u32 v;
    asm volatile("ds_read_b32 %0, %1\n s_waitcnt lgkmcnt(0)" : "=v"(v) : "v"((u32)(size_t)(LDSP char*)(lds + sOff)) : "memory");
    v = lane == 1 ? 0u : v;
    float s0 = lo(v) * lo(v), s1 = hi(v) * hi(v);
    int so = 0;
#pragma unroll 1
    for (int c = 0; c < 8; ++c) {
      if (c < 9 - NS) waitv<(NS - 2) * NI>(); else if (NS == 4 && c == 5) waitv<NI>(); else waitv<0>();
      bar();
      if (c < 8 - NS) issue(c + NS, lds + so);
      so = so == (NS - 1) * CH ? 0 : so + CH;
      if (c < 7) {
        asm volatile("ds_read_b32 %0, %1\n s_waitcnt lgkmcnt(0)" : "=v"(v) : "v"((u32)(size_t)(LDSP char*)(lds + so + sOff)) : "memory");
        s0 += lo(v) * lo(v); s1 += hi(v) * hi(v);
      }
    }
#pragma unroll
    for (int o = 1; o < 64; o <<= 1) { s0 += __shfl_xor(s0, o); s1 += __shfl_xor(s1, o); }
    if (lane < 2) rs2[128 * mt + 8 * ng + 2 * w + lane] = 1.f / ((lane ? s1 : s0) * (1.f / 512) + EPS);
    return;
  }
  f16v acc[3][2] = {}, accx = {};
  const int pOff = wn * 12288 + lane * 16;
  int qOff[2];
#pragma unroll
  for (int b = 0; b < 2; ++b)
    qOff[b] = NT * 4096 + (4 * h + ((lane >> 2) & 3)) * 256 + (((4 * (2 * wm + (b ^ wn)) + ((lane >> 3) & 2) + ((lane >> 1) & 1)) ^ (lane & 12)) << 4) + 8 * (lane & 1);
  struct Fr { u4 P[3], Q[2], X; };
  auto rd = [&](const char* sl, int ks) {
    Fr f;
#pragma unroll
    for (int b = 0; b < 2; ++b) f.Q[b] = ldtr(sl + qOff[b] + ks * 4096, 2048);
#pragma unroll
    for (int t = 0; t < 3; ++t) f.P[t] = lds128(sl + pOff + t * 4096 + ks * 1024);
    if (LAST) f.X = lds128(sl + 24576 + ks * 1024 + lane * 16);
    return f;
  };
  auto mm = [&](const Fr& f, int ks) {
#pragma unroll
    for (int t = 0; t < 3; ++t) { acc[t][0] = mf32(f.P[t], f.Q[0], acc[t][0]); acc[t][1] = mf32(f.P[t], f.Q[1], acc[t][1]); }
    if (LAST) accx = mf32(f.X, f.Q[0], accx);
    mix<6 + LAST, 2, 1>();
    SB();
  };
  bar();
  STAMP(KI, 1);
  Fr f0 = rd(lds, 0), f1;
  int so = 0;
#pragma unroll 1
  for (int c = 0; c < 8; ++c) {
    char* sl = lds + so;
    so = so == (NS - 1) * CH ? 0 : so + CH;
    SB();
    f1 = rd(sl, 1); mm(f0, 0);
    f0 = rd(sl, 2); mm(f1, 1);
    f1 = rd(sl, 3); mm(f0, 2);
    bar();
    SB();
    f0 = rd(lds + so, 0); mm(f1, 3);
  }
  STAMP(KI, 3);
#pragma unroll
  for (int t = 0; t < 3; ++t) {
    int tile = t0 + 3 * wn + t;
    if (tile < t1)
#pragma unroll
      for (int b = 0; b < 2; ++b) {
        const f16v& a = acc[t][b];
        u4 o;
#pragma unroll
        for (int j = 0; j < 4; ++j) { int i = 8 * (j >> 1) + 2 * (j & 1); o[j] = pk(fmaxf(a[i], 0.f) * a[i + 4], fmaxf(a[i + 1], 0.f) * a[i + 5]); }
        st4(hb + (((2 * mt + wm) * 2 + (b ^ wn)) * 86 + tile) * 256 + lane * 4, o);
      }
  }
  if (LAST) {
    u32* o = x3 + ((4 * mt + w) * 16 + ng) * 512 + lane * 4;
    st4(o, pack8(accx, 0)); st4(o + 256, pack8(accx, 1));
  }
  STAMP(KI, 4); STAMPW(KI, 5);
}

template <int LAST>
__global__ __launch_bounds__(512) void k_down(const u32* __restrict__ hb, const u32* __restrict__ wd, const float* __restrict__ rs2,
                                              const u32* __restrict__ res, u32* __restrict__ out, float* __restrict__ ssp) {
  __shared__ __attribute__((aligned(1024))) char lds[131072 + 512];
  const int xc = blockIdx.x & 7, jb = blockIdx.x >> 3, rg = 8 * (xc & 3) + (jb & 7), nt = 4 * (xc >> 2) + (jb >> 3);
  const int lane = threadIdx.x & 63, w8 = __builtin_amdgcn_readfirstlane(threadIdx.x >> 6), w = w8 & 3, r = lane & 31, h = lane >> 5;
  constexpr int KI = LAST ? 6 : 3;
  STAMP(KI, 0);
  if (w8 >= 4) {
    const rsrc rs = mkr(w < 2 ? wd + (2 * nt + w) * 86 * 256 : hb + (2 * rg + w - 2) * 86 * 256);
    auto issue = [&](int c) {
      char* sl = lds + (c & 3) * 32768 + w * 8192;
      unroll<4>([&](auto I) { blds<I.v * 1024>(rs, lane * 16, c * 8192, sl); });
      unroll<2>([&](auto I) { blds<I.v * 1024>(rs, lane * 16, c * 8192 + 4096, sl + 4096); });
      if (c < 10) unroll<2>([&](auto I) { blds<I.v * 1024 + 2048>(rs, lane * 16, c * 8192 + 4096, sl + 4096); });
    };
    issue(0); issue(1); issue(2);
    waitv<16>(); bar();
    issue(3);
#pragma unroll 1
    for (int c = 0; c < 10; ++c) {
      if (c < 7) waitv<16>(); else if (c == 7) waitv<14>(); else if (c == 8) waitv<6>(); else waitv<0>();
      bar();
      if (c < 7) issue(c + 4);
    }
    if (!LAST) bar();
    return;
  }
  const int t = w >> 1, b = w & 1, rho = 64 * rg + 32 * b + r;
  u32 rv[8];
  if (LAST) {
    const u32* p = res + ((2 * rg + b) * 16 + 2 * nt + t) * 512 + lane * 4;
    u4 x = *(const u4*)p, y = *(const u4*)(p + 256);
#pragma unroll
    for (int j = 0; j < 4; ++j) { rv[j] = x[j]; rv[4 + j] = y[j]; }
  } else {
    const u16* p = (const u16*)res + (64 * nt + 32 * t + 4 * h) * (2 * PS) + rho;
#pragma unroll
    for (int j = 0; j < 8; ++j) { const u16* e = p + (8 * (j >> 1) + 2 * (j & 1)) * (2 * PS); rv[j] = (u32)e[0] | ((u32)e[2 * PS] << 16); }
  }
  const float rs = rs2[rho];
  struct Fr { u4 A[4], B[4]; };
  const int aOff = t * 8192 + lane * 16, bOff = 16384 + b * 8192 + lane * 16;
  auto rd = [&](const char* sl, auto N) {
    Fr f;
#pragma unroll
    for (int k = 0; k < N.v; ++k) { f.A[k] = lds128(sl + aOff + k * 1024); f.B[k] = lds128(sl + bOff + k * 1024); }
    return f;
  };
  f16v acc = {};
  auto mm = [&](const Fr& f, auto N) {
#pragma unroll
    for (int k = 0; k < N.v; ++k) acc = mf32(f.A[k], f.B[k], acc);
    mix<N.v, 2, 1>();
    SB();
  };
  bar();
  STAMP(KI, 1);
  Fr f0 = rd(lds, IC<4>{}), f1;
#pragma unroll 1
  for (int c = 0; c < 10; ++c) {
    SB();
    f1 = rd(lds + (c & 3) * 32768 + 4096, IC<4>{}); mm(f0, IC<4>{});
    bar();
    SB();
    f0 = rd(lds + ((c + 1) & 3) * 32768, IC<4>{}); mm(f1, IC<4>{});
  }
  f1 = rd(lds + 2 * 32768 + 4096, IC<2>{}); mm(f0, IC<4>{});
  mm(f1, IC<2>{});
  STAMP(KI, 2);
  u32 v[8];
#pragma unroll
  for (int j = 0; j < 8; ++j) v[j] = pk(lo(rv[j]) + rs * acc[2 * j], hi(rv[j]) + rs * acc[2 * j + 1]);
  if (LAST) {
    u32* o = out + (32 * (32 * b + r) + rg) * 256 + 32 * nt + 16 * t + 8 * h;
    *(u4*)o = u4{v[0], v[1], v[2], v[3]}; *(u4*)(o + 4) = u4{v[4], v[5], v[6], v[7]};
  } else {
    const int c0 = 32 * nt + 16 * t + 2 * h;
    float ss = 0.f;
#pragma unroll
    for (int j = 0; j < 8; ++j) {
      st1(out + (c0 + 4 * (j >> 1) + (j & 1)) * XI + rho, v[j]);
      u32 m = (j == 0 && c0 == 0) ? (v[j] & 0xffffu) : v[j];
      ss = dot2(m, m, ss);
    }
    ss += __shfl_xor(ss, 32);
    float* sl = (float*)(lds + 131072);
    if (h == 0) sl[w * 32 + r] = ss;
    bar();
    if (w == 0) ssp[nt * SS + 64 * rg + lane] = sl[lane] + sl[64 + lane];
  }
  STAMP(KI, 3); STAMPW(KI, 4);
}

extern "C" void kernel_launch(void* const* d_in, const int* in_sizes, int n_in, void* d_out, int out_size, void* d_ws, size_t ws_size,
                              hipStream_t stream) {
  const u16 *x = (const u16*)d_in[0], *wr = (const u16*)d_in[1], *wi = (const u16*)d_in[2];
  const u16 *wg = (const u16*)d_in[4], *wu = (const u16*)d_in[5], *wd = (const u16*)d_in[6];
  char* ws = (char*)d_ws;
  const size_t MB = 1 << 20;
  u32 *xh0 = (u32*)ws, *xh1 = (u32*)(ws + 3 * MB), *xh2 = (u32*)(ws + 6 * MB), *xh3 = (u32*)(ws + 9 * MB);
  u32 *filt = (u32*)(ws + 12 * MB), *x3 = (u32*)(ws + 15 * MB), *wgu = (u32*)(ws + 17 * MB), *wd1 = (u32*)(ws + 20 * MB);
  u32 *wd2 = (u32*)(ws + 22 * MB), *hb = (u32*)(ws + 24 * MB), *hb2 = (u32*)(ws + 30 * MB);
  float *rstd = (float*)(ws + 36 * MB), *ssp = rstd + 4096, *rs2 = rstd + 32768, *rs3 = rstd + 65536;
  k_prep<<<260, 512, 0, stream>>>(x, wr, wi, wg, wu, wd, xh0, (u32*)rstd, wgu, wd1, wd2, filt, (const u32*)d_out);
  k_fft<1><<<256, 256, 0, stream>>>(xh0, rstd, filt, xh1);
  k_up<0><<<256, 512, 0, stream>>>(xh1, wgu, hb, x3, rs2);
  k_down<0><<<256, 512, 0, stream>>>(hb, wd1, rs2, xh1, xh2, ssp);
  k_fft<8><<<256, 256, 0, stream>>>(xh2, ssp, filt, xh3);
  k_up<1><<<256, 512, 0, stream>>>(xh3, wgu, hb2, x3, rs3);
  k_down<1><<<256, 512, 0, stream>>>(hb2, wd2, rs3, x3, (u32*)d_out, ssp);
}
```

```cpp
#include <hip/hip_runtime.h>
#include <hip/hip_bf16.h>

typedef unsigned u32;
typedef unsigned short u16;
typedef __attribute__((ext_vector_type(8))) __bf16 bf8;
typedef __attribute__((ext_vector_type(2))) __bf16 bf2;
typedef __attribute__((ext_vector_type(2))) float f2;
typedef __attribute__((ext_vector_type(4))) float f4;
typedef __attribute__((ext_vector_type(16))) float f16v;
typedef __attribute__((ext_vector_type(2))) u32 u2;
typedef __attribute__((ext_vector_type(4))) u32 u4;
typedef __attribute__((ext_vector_type(4))) short s4;
typedef u4 __attribute__((aligned(2))) u4_ua;
#define DI __device__ __forceinline__
#define LDSP __attribute__((address_space(3)))
#define GLBP __attribute__((address_space(1)))

#define STAMP(k, j)
#define STAMPW(k, j)
#define ROT 0
#define UPPRE 2
constexpr int SS = 2048;
constexpr int XI = 2112, PS = 1088, FS = 2080;
constexpr float EPS = 1e-5f;

struct Part { u16 v[16384]; };
#if defined(__HIP_DEVICE_COMPILE__)
constexpr double kPi = 3.14159265358979323846;
constexpr double csin(double x) { double t = x, s = x; for (int i = 1; i < 14; ++i) { t *= -x * x / ((2 * i) * (2 * i + 1)); s += t; } return s; }
constexpr u16 tobf(double x) { float f = (float)x; u32 u = __builtin_bit_cast(u32, f); u += 0x7fffu + ((u >> 16) & 1u); return (u16)(u >> 16); }
struct CosT { u16 v[512]; };
constexpr CosT mkcos() {
  CosT c{};
  for (int i = 0; i < 512; ++i) {
    int q = (i + 128) & 511;
    int y = q <= 128 ? q : (q <= 384 ? 256 - q : q - 512);
    c.v[i] = y == 0 ? (u16)0 : tobf(csin(2 * kPi * y / 512) / 16.0);
  }
  return c;
}
constexpr CosT kCos = mkcos();
constexpr u16 kDC = tobf(0.04419417382415922);
constexpr Part mkR(int pi) {
  Part p{};
  for (int e = 0; e < 16384; ++e) {
    int r = (e >> 3) & 31, h = (e >> 8) & 1, j = e & 7, d = 0, s = 0;
    if (pi < 4) { d = (((e >> 9) & 7) << 4) + 8 * h + j; s = 32 * (4 * pi + (e >> 12)) + r; }
    else { d = 32 * (pi - 4) + (r & 3) + 4 * (r >> 3) + 16 * ((r >> 2) & 1); s = ((e >> 9) << 4) + 8 * (j >> 2) + 4 * h + (j & 3); }
    p.v[e] = s >= 2 ? kCos.v[((s >> 1) * d + ((s & 1) << 7)) & 511] : (u16)(kDC | ((s == 0 && (d & 1)) ? 0x8000 : 0));
  }
  return p;
}
template <int I> struct PR { static constexpr Part v = mkR(I); };
struct Tab { Part p[20]; };
__device__ const __attribute__((aligned(1024))) Tab R1T = {{PR<0>::v, PR<1>::v, PR<2>::v, PR<3>::v, PR<4>::v, PR<5>::v, PR<6>::v, PR<7>::v,
  PR<8>::v, PR<9>::v, PR<10>::v, PR<11>::v, PR<12>::v, PR<13>::v, PR<14>::v, PR<15>::v, PR<16>::v, PR<17>::v, PR<18>::v, PR<19>::v}};
#else
struct Tab { u16 p[8]; };
__device__ const Tab R1T = {};
#endif

DI f16v mf32(u4 a, u4 b, f16v c) { return __builtin_amdgcn_mfma_f32_32x32x16_bf16(__builtin_bit_cast(bf8, a), __builtin_bit_cast(bf8, b), c, 0, 0, 0); }
DI f4 mf16(u4 a, u4 b, f4 c) { return __builtin_amdgcn_mfma_f32_16x16x32_bf16(__builtin_bit_cast(bf8, a), __builtin_bit_cast(bf8, b), c, 0, 0, 0); }
DI u32 pk(float a, float b) { f2 v = {a, b}; return __builtin_bit_cast(u32, __builtin_convertvector(v, bf2)); }
DI float lo(u32 u) { return __builtin_bit_cast(float, u << 16); }
DI float hi(u32 u) { return __builtin_bit_cast(float, u & 0xffff0000u); }
DI float dot2(u32 a, u32 b, float c) { return __builtin_amdgcn_fdot2_f32_bf16(__builtin_bit_cast(bf2, a), __builtin_bit_cast(bf2, b), c, false); }
DI void glds(const void* g, char* l) { __builtin_amdgcn_global_load_lds((const GLBP void*)g, (LDSP void*)l, 16, 0, 0); }
typedef __amdgpu_buffer_rsrc_t rsrc;
DI rsrc mkr(const void* p) { return __builtin_amdgcn_make_buffer_rsrc((void*)p, 0, 0x7fffffff, 0x00020000); }
template <int O = 0> DI void blds(rsrc r, int vo, int so, char* l) { __builtin_amdgcn_raw_ptr_buffer_load_lds(r, (LDSP void*)l, 16, vo, so, O, 0); }
DI u32 selfpf(int nb) {
  const rsrc r = __builtin_amdgcn_make_buffer_rsrc((void*)(__builtin_amdgcn_s_getpc() & ~127ll), 0, nb, 0x00020000);
  return __builtin_amdgcn_raw_buffer_load_b32(r, (threadIdx.x & 255) * 128, 0, 0);
}
DI void keep(u32 v) { asm volatile("" ::"v"(v)); }
DI u4 ldtr(const char* a, int d) {
  u2 x = __builtin_bit_cast(u2, __builtin_amdgcn_ds_read_tr16_b64_v4i16((LDSP s4*)a));
  u2 y = __builtin_bit_cast(u2, __builtin_amdgcn_ds_read_tr16_b64_v4i16((LDSP s4*)(a + d)));
  return u4{x[0], x[1], y[0], y[1]};
}
#define WT 2
DI void st4(u32* p, u4 v) {
  if (WT == 2) asm volatile("global_store_dwordx4 %0, %1, off sc0 sc1\n s_nop 1" ::"v"(p), "v"(v) : "memory");
  else if (WT == 1) __builtin_nontemporal_store(v, (u4*)p);
  else *(u4*)p = v;
}
DI void st2(u32* p, u2 v) {
  if (WT == 2) asm volatile("global_store_dwordx2 %0, %1, off sc0 sc1" ::"v"(p), "v"(v) : "memory");
  else if (WT == 1) __builtin_nontemporal_store(v, (u2*)p);
  else *(u2*)p = v;
}
DI void st1(u32* p, u32 v) {
  if (WT == 2) asm volatile("global_store_dword %0, %1, off sc0 sc1" ::"v"(p), "v"(v) : "memory");
  else if (WT == 1) __builtin_nontemporal_store(v, p);
  else *p = v;
}
DI u4 lds128(const char* p) { return *(const u4*)p; }
DI u4 pack8(const f16v& a, int s) { u4 o = {pk(a[8 * s], a[8 * s + 1]), pk(a[8 * s + 2], a[8 * s + 3]), pk(a[8 * s + 4], a[8 * s + 5]), pk(a[8 * s + 6], a[8 * s + 7])}; return o; }
template <int N> DI void waitv() { asm volatile("s_waitcnt vmcnt(%0)" ::"n"(N) : "memory"); }
DI void bar() { asm volatile("s_waitcnt lgkmcnt(0)" ::: "memory"); __builtin_amdgcn_s_barrier(); asm volatile("" ::: "memory"); }
DI int dperm(int m) { return (m & 3) + 4 * (m >> 3) + 16 * ((m >> 2) & 1); }
DI int fperm(int m) { int i = (m & 3) + 4 * (m >> 3); return 16 * (i >> 3) + 8 * ((m >> 2) & 1) + (i & 7); }
#define SB() __builtin_amdgcn_sched_barrier(0)
#define SGB(m, n) __builtin_amdgcn_sched_group_barrier(m, n, 0)
template <int NM, int ND, int NV> DI void mix() { if constexpr (NM > 0) { SGB(0x008, 1); SGB(0x100, ND); SGB(0x002, NV); mix<NM - 1, ND, NV>(); } }
template <int I> struct IC { static constexpr int v = I; };
template <int N, class F> DI void unroll(F&& f) { if constexpr (N > 0) { unroll<N - 1>(f); f(IC<N - 1>{}); } }

template <int JOB>
DI void prep_gemm(const u16* __restrict__ q0, const u16* __restrict__ q1, int qt, int nt, int c0, u32 pf, char* lds,
                  u32* __restrict__ o0, u32* __restrict__ o1) {
  const int lane = threadIdx.x & 63, w8 = __builtin_amdgcn_readfirstlane(threadIdx.x >> 6), w = w8 & 3;
  const int wp = w >> 1, wq = w & 1, r = lane & 31, h = lane >> 5;
  if (w8 >= 4) {
    const rsrc rp = mkr(&R1T), ra = mkr(q0), rb = mkr(q1);
    int qv[4];
#pragma unroll
    for (int i = 0; i < 4; ++i) {
      if (JOB == 1) {
        int d = 16 * w + 8 * (i >> 1) + (lane >> 3), pos = lane & 7;
        int f = 64 * qt + 16 * ((pos >> 1) ^ (d & 3) ^ (i & 1)) + 8 * (pos & 1);
        qv[i] = (d * 1376 + (f > 1368 ? 1368 : f)) * 2;
      } else {
        int u = 32 * w + 8 * i + (lane >> 3), row;
        if (JOB == 0) { int rho = 128 * qt + (u & 64) + 2 * (u & 31) + ((u >> 5) & 1); row = 32 * (rho & 63) + (rho >> 6); }
        else { row = 128 * qt + (u & 96) + fperm(u & 31); row = row > 1375 ? 1375 : row; }
        qv[i] = row * 1024 + (((lane & 7) ^ ((u >> 1) & 7)) << 4);
      }
    }
    constexpr int QSTEP = JOB == 1 ? 64 * 1376 * 2 : 128;
    auto issP = [&](int c) { unroll<4>([&](auto I) { blds<I.v * 1024>(rp, lane * 16, (4 * nt + w) * 8192 + c * 4096, lds + w * 8192 + c * 4096); }); };
    auto issue = [&](int cc) {
      const int c = (cc + c0) & 7;
#pragma unroll
      for (int i = 0; i < 4; ++i) blds((JOB == 1 && (i & 1)) ? rb : ra, qv[i], c * QSTEP, lds + 32768 + c * 16384 + (4 * w + i) * 1024);
    };
    issP(0); issP(1); issue(0); issue(1); issue(2);
    waitv<8>(); bar();
    keep(pf);
#pragma unroll 1
    for (int c = 3; c < 8; ++c) issue(c);
    unroll<8>([&](auto I) { waitv<(I.v < 6 ? 4 * (6 - I.v) : 0)>(); bar(); });
    if (JOB == 0) {
      const int u = 32 * nt + 8 * w + (lane >> 3);
      float s1 = 0.f, s2 = 0.f;
#pragma unroll
      for (int c = 0; c < 8; ++c) {
        u4 v = lds128(lds + 32768 + c * 16384 + u * 128 + (lane & 7) * 16);
#pragma unroll
        for (int j = 0; j < 4; ++j) { s1 = dot2(v[j], 0x3f803f80u, s1); s2 = dot2(v[j], v[j], s2); }
      }
#pragma unroll
      for (int o = 1; o < 8; o <<= 1) { s1 += __shfl_xor(s1, o); s2 += __shfl_xor(s2, o); }
      float m = s1 * (1.f / 512), var = fmaxf(s2 * (1.f / 512) - m * m, 0.f);
      if ((lane & 7) == 0) ((float*)o1)[128 * qt + (u & 64) + 2 * (u & 31) + ((u >> 5) & 1)] = __builtin_amdgcn_rsqf(var + EPS) * (1.f / 2048);
    }
    return;
  }
  f16v acc[2][2] = {};
  int qOff;
  if (JOB == 1) {
    int g = lane >> 4, qr = (lane >> 2) & 3, run = 4 * (g & 1) + (lane & 3), q = run >> 1;
    qOff = 32768 + (g >> 1) * 2048 + qr * 128 + (q & 1) * 1024 + (8 * (run & 1) + 4 * (q >> 1)) * 2;
  } else qOff = 32768 + (64 * wq + r) * 128;
  struct Fr { u4 v[2]; };
  int pOff; u32 sgn;
  auto turn = [&](int c) {
    int n = ((r >> 1) * (c >> 1)) & 3;
    pOff = wp * 16384 + (c & 1) * 4096 + (lane ^ (n & 1)) * 16;
    sgn = (((r & 1) ? n + 1 : n) & 2) ? 0x80008000u : 0u;
  };
  auto rdP = [&](int ks) { return Fr{{lds128(lds + pOff + ks * 1024), lds128(lds + pOff + 8192 + ks * 1024)}}; };
  auto rdQ = [&](const char* sl, int ks) {
    Fr f;
#pragma unroll
    for (int b = 0; b < 2; ++b) {
      if (JOB == 1) {
        f.v[b] = ldtr(sl + qOff + ks * 4096 + (((2 * wq + b) ^ ((lane >> 2) & 3) ^ ((lane >> 1) & 1)) << 5), 512);
      } else f.v[b] = lds128(sl + qOff + b * 4096 + (((2 * ks + h) ^ ((r >> 1) & 7)) << 4));
    }
    return f;
  };
  auto mm = [&](const Fr& P, const Fr& Q, Fr& N, u32 sg, int c, int ks) {
#pragma unroll
    for (int t = 0; t < 2; ++t)
#pragma unroll
      for (int b = 0; b < 2; ++b) acc[t][b] = JOB == 2 ? mf32(Q.v[b], P.v[t], acc[t][b]) : mf32(P.v[t], Q.v[b], acc[t][b]);
    N.v[0] ^= sg; N.v[1] ^= sg;
    SGB(0x008, 1); SGB(0x100, 3); SGB(0x008, 1); SGB(0x100, 3); SGB(0x002, 4); SGB(0x008, 1); SGB(0x002, 4); SGB(0x008, 1);
    asm volatile("" : "+v"(N.v[0]), "+v"(N.v[1]));
    SB();
  };
  turn(c0);
  bar();
  STAMP(0, 2);
  Fr qa = rdQ(lds + c0 * 16384, 0), pa = rdP(0), pb = rdP(1), qb, pc, pd;
  pa.v[0] ^= sgn; pa.v[1] ^= sgn;
#pragma unroll 1
  for (int cc = 0; cc < 8; ++cc) {
    const int c = (cc + c0) & 7;
    const char* sl = lds + c * 16384;
    const u32 sg = sgn;
    SB();
    qb = rdQ(sl, 1); pc = rdP(2); mm(pa, qa, pb, sg, c, 0);
    qa = rdQ(sl, 2); pd = rdP(3); mm(pb, qb, pc, sg, c, 1);
    turn((c + 1) & 7);
    SB();
    qb = rdQ(sl, 3); pa = rdP(0); mm(pc, qa, pd, sg, c, 2);
    asm volatile("" ::: "memory"); __builtin_amdgcn_s_barrier(); asm volatile("" ::: "memory");
    SB();
    qa = rdQ(lds + ((c + 1) & 7) * 16384, 0); pb = rdP(1); mm(pd, qb, pa, sgn, c, 3);
  }
  STAMP(0, 3);
  if (JOB == 0) {
#pragma unroll
    for (int t = 0; t < 2; ++t)
#pragma unroll
      for (int i = 0; i < 16; i += 2) {
        int col = 64 * nt + 32 * wp + 16 * t + (((i & 3) + 8 * (i >> 2) + 4 * h) >> 1);
        u2 v = {pk(acc[t][0][i], acc[t][0][i + 1]), pk(acc[t][1][i], acc[t][1][i + 1])};
        st2(o0 + col * XI + 128 * qt + 64 * wq + 2 * r, v);
      }
  } else if (JOB == 1) {
#pragma unroll
    for (int b = 0; b < 2; ++b) {
      int tile = 4 * qt + 2 * wq + b;
      if (tile < 86)
#pragma unroll
        for (int t = 0; t < 2; ++t)
#pragma unroll
          for (int s = 0; s < 2; ++s) {
            int ks = 8 * nt + 4 * wp + 2 * t + s;
            u4 v = pack8(acc[t][b], s);
            if (ks == 0 && h == 0) v[0] &= 0xffffu;
            st4(o0 + (tile * 32 + ks) * 256 + lane * 4, v);
          }
    }
  } else {
#pragma unroll
    for (int b = 0; b < 2; ++b)
#pragma unroll
      for (int s = 0; s < 2; ++s) {
        int ks = 8 * qt + 4 * wq + 2 * b + s;
        if (ks < 86)
#pragma unroll
          for (int t = 0; t < 2; ++t) {
            int o = ((4 * nt + 2 * wp + t) * 86 + ks) * 256 + lane * 4;
            st4(o0 + o, pack8(acc[t][b], s));
          }
      }
  }
}

__global__ __launch_bounds__(512) void k_prep(const u16* __restrict__ x, const u16* __restrict__ wr, const u16* __restrict__ wi,
    const u16* __restrict__ wg, const u16* __restrict__ wu, const u16* __restrict__ wd,
    u32* __restrict__ xh, u32* __restrict__ rstd, u32* __restrict__ wgu, u32* __restrict__ wd1, u32* __restrict__ wd2,
    u32* __restrict__ filt) {
  __shared__ __attribute__((aligned(1024))) char lds[163840];
  const int b = blockIdx.x, nt = (b >> 3) & 3, q = 8 * (b >> 5) + (b & 7), c0 = ROT;
  const int lane = threadIdx.x & 63, w = __builtin_amdgcn_readfirstlane(threadIdx.x >> 6);
  STAMP(0, 0);
  u32 pf = 0;
  if (w >= 4) pf = selfpf(12288);
  if (b >= 260) {
    const int v = b - 260;
#pragma unroll
    for (int i = 0; i < 4; ++i) blds(mkr(wd), lane * 16, (32 * v + 4 * w + i) * 1024, lds + (4 * w + i) * 1088);
    waitv<0>(); bar();
    keep(pf);
    const int e = lane & 3, a0 = (8 * (lane >> 5) + ((lane >> 2) & 3)) * 1088 + (8 * ((lane >> 4) & 1) + 4 * (e >> 1) + 16 * (e & 1)) * 2;
#pragma unroll
    for (int i = 0; i < 4; ++i) {
      const int dt = 2 * w + (i >> 1), ks = i & 1;
      st4(wd2 + (dt * 86 + 2 * v + ks) * 256 + lane * 4, ldtr(lds + a0 + ks * 16 * 1088 + dt * 64, 4 * 1088));
    }
  } else if (q < 16) prep_gemm<0>(x, x, q, nt, c0, pf, lds, xh, rstd);
  else if (q < 38) prep_gemm<1>(wg, wu, q - 16, nt, 0, pf, lds, wgu, wgu);
  else if (q < 49) prep_gemm<2>(wd, wd, q - 38, nt, c0, pf, lds, wd1, wd1);
  else {
    const int u = b < 256 ? (q - 49) * 4 + nt : b - 196, k0 = 16 * (u >> 4) + 8 * (u & 1) + 256 * ((u >> 1) & 7);
    const rsrc rs = mkr(w < 4 ? wr : wi);
    const int so = (k0 + 64 * (w & 3)) * 514;
    unroll<4>([&](auto I) { blds<I.v * 1024>(rs, lane * 16, so, lds + w * 4160); });
    if (lane == 0) blds(rs, 0, so + 4096, lds + w * 4160 + 4096);
    blds(mkr((const char*)&R1T + 131072), lane * 16, (u * 8 + w) * 1024, lds + 40960 + w * 1024);
    if (w < 6) {
      u32 lo_, hi_; int nb;
#define PCOF(sym, n) { asm volatile("s_getpc_b64 s[40:41]\n s_add_u32 %0, s40, " sym "@rel32@lo+4\n s_addc_u32 %1, s41, " sym "@rel32@hi+12" : "=s"(lo_), "=s"(hi_) :: "scc", "s40", "s41"); nb = n; }
      if (w == 0) PCOF("_Z5k_fftILi1EEvPKjPKfS1_Pj", 3584)
      else if (w == 1) PCOF("_Z4k_upILi0EEvPKjS1_PjS2_Pf", 4352)
      else if (w == 2) PCOF("_Z6k_downILi0EEvPKjS1_PKfS1_PjPf", 2688)
      else if (w == 3) PCOF("_Z5k_fftILi8EEvPKjPKfS1_Pj", 4224)
      else if (w == 4) PCOF("_Z4k_upILi1EEvPKjS1_PjS2_Pf", 4608)
      else PCOF("_Z6k_downILi1EEvPKjS1_PKfS1_PjPf", 1920)
      const rsrc rc = __builtin_amdgcn_make_buffer_rsrc((void*)(((unsigned long long)hi_ << 32) | lo_), 0, nb, 0x00020000);
      unroll<4>([&](auto I) { blds<I.v * 1024>(rc, lane * 16, 0, lds + 65536 + w * 8192); });
      blds(rc, lane * 16 + 4096, 0, lds + 65536 + w * 8192 + 4096);
    }
    waitv<0>(); bar();
    keep(pf);
    STAMP(0, 1);
#pragma unroll
    for (int it = 0; it < 4; ++it) {
      const int cs = 1 + 32 * w + 8 * it + (lane >> 3);
      const u16* p = (const u16*)(lds + (lane & 7) * 514) + cs;
      u4 v;
#pragma unroll
      for (int j = 0; j < 4; ++j) v[j] = (u32)p[j * 2080] | ((u32)p[(4 + j) * 2080] << 16);
      st4(filt + (cs & 255) * FS + (u >> 3) * 256 + (((u >> 1) & 3) * 16 + 8 * (u & 1) + (lane & 7)) * 4, v);
    }
  }
  STAMPW(0, q < 16 ? 4 : q < 38 ? 5 : q < 49 ? 6 : 7);
}

DI u4 rot(u4 v) { u4 o; for (int j = 0; j < 4; ++j) o[j] = __builtin_amdgcn_alignbit(v[j], v[j], 16) ^ 0x8000u; return o; }
DI u4 pk4(f4 a, f4 b) { return u4{pk(a[0], b[0]), pk(a[1], b[1]), pk(a[2], b[2]), pk(a[3], b[3])}; }
DI f4 lo4(u4 v) { return __builtin_bit_cast(f4, v << 16); }
DI f4 hi4(u4 v) { return __builtin_bit_cast(f4, v & 0xffff0000u); }
template <int CJ> DI u4 cmulc(f4 re, f4 im, f4 c, f4 s) {
  u4 o;
#pragma unroll
  for (int q = 0; q < 4; q += 2) {
    const f2 r = {re[q], re[q + 1]}, i = {im[q], im[q + 1]}, cc = {c[q], c[q + 1]}, ss = {s[q], s[q + 1]};
    const f2 rc = r * cc, rs = r * ss;
    const f2 x = __builtin_elementwise_fma(CJ ? -i : i, ss, rc), y = __builtin_elementwise_fma(CJ ? -i : i, cc, -rs);
    o[q] = pk(x[0], y[0]); o[q + 1] = pk(x[1], y[1]);
  }
  return o;
}
template <int NP>
__global__ __launch_bounds__(256, 2) void k_fft(const u32* __restrict__ xin, const float* __restrict__ st, const u32* __restrict__ filt,
                                                u32* __restrict__ xout) {
  __shared__ __attribute__((aligned(256))) char lds[16384];
  const int col = blockIdx.x, tid = threadIdx.x, lane = tid & 63, w = tid >> 6, l15 = lane & 15, g = lane >> 4;
  constexpr int KI = NP == 1 ? 1 : 4;
  STAMP(KI, 0);
  const u32* xc = xin + col * XI;
  u4 xd[2], xr[2], fw[2];
  f4 sv[NP][2];
#pragma unroll
  for (int i = 0; i < 2; ++i) xd[i] = *(const u4*)(xc + 1024 * i + 4 * tid);
#pragma unroll
  for (int p = 0; p < NP; ++p)
#pragma unroll
    for (int i = 0; i < 2; ++i) sv[p][i] = *(const f4*)(st + p * SS + 1024 * i + 4 * tid);
#pragma unroll
  for (int i = 0; i < 2; ++i) {
    fw[i] = *(const u4*)(filt + col * FS + (2 * w + i) * 256 + lane * 4);
    xr[i] = *(const u4*)(xc + 64 * (16 * i + l15) + 16 * w + 4 * g);
  }
  if (NP == 1) SB();
  const int k1 = 16 * w + l15;
  const float fk1 = (float)k1, fl = (float)l15, fa = (float)(4 * g);
  u4 F64r[4], F64i[4], F32r[2][2], F32i[2][2];
  f4 t1c[2], t1s[2], t2c[2], t2s[2];
#pragma unroll
  for (int j = 0; j < 4; ++j) {
    float a = (fa + j) * fk1 * (1.f / 64), b = (fa + j) * fl * (1.f / 32);
    F64r[0][j] = pk(__builtin_amdgcn_cosf(a), __builtin_amdgcn_sinf(a));
    F32r[0][0][j] = pk(__builtin_amdgcn_cosf(b), __builtin_amdgcn_sinf(b));
  }
  F64i[0] = rot(F64r[0]); F32i[0][0] = rot(F32r[0][0]);
  const u32 NEG = 0x80008000u, so = (lane & 1) ? NEG : 0u, sr = (lane & 2) ? NEG : 0u, si = ((lane + 1) & 2) ? NEG : 0u;
#pragma unroll
  for (int j = 0; j < 4; ++j) {
    F64r[1][j] = ((lane & 1) ? F64i[0][j] : F64r[0][j]) ^ sr;
    F64i[1][j] = ((lane & 1) ? F64r[0][j] : F64i[0][j]) ^ si;
    F64r[2][j] = F64r[0][j] ^ so; F64i[2][j] = F64i[0][j] ^ so;
    F64r[3][j] = F64r[1][j] ^ so; F64i[3][j] = F64i[1][j] ^ so;
    const u32 sj = (j & 1) ? NEG : 0u;
    F32r[0][1][j] = F32r[0][0][j] ^ so; F32i[0][1][j] = F32i[0][0][j] ^ so;
    F32r[1][0][j] = F32r[0][0][j] ^ sj; F32i[1][0][j] = F32i[0][0][j] ^ sj;
    F32r[1][1][j] = F32r[0][1][j] ^ sj; F32i[1][1][j] = F32i[0][1][j] ^ sj;
  }
#pragma unroll
  for (int t = 0; t < 2; ++t)
#pragma unroll
    for (int j = 0; j < 4; ++j) {
      float a = (fa + (16 * t + j)) * (fk1 * (1.f / 2048)), b = ((fa + (float)(16 * w)) + j) * ((fl + 16 * t) * (1.f / 2048));
      t1c[t][j] = __builtin_amdgcn_cosf(a); t1s[t][j] = __builtin_amdgcn_sinf(a);
      t2c[t][j] = __builtin_amdgcn_cosf(b); t2s[t][j] = __builtin_amdgcn_sinf(b);
    }
  if (NP > 1) unroll<22>([&](auto) { SGB(0x020, 1); SGB(0x402, 11); });
  SB();
  STAMP(KI, 1);
  const u32 msk = col == 0 ? 0xffffu : 0xffffffffu;
#pragma unroll
  for (int i = 0; i < 2; ++i) {
    f4 rs = sv[0][i];
    if (NP > 1) {
#pragma unroll
      for (int p = 1; p < NP; ++p) rs += sv[p][i];
#pragma unroll
      for (int j = 0; j < 4; ++j) rs[j] = __builtin_amdgcn_rsqf(rs[j] * (1.f / 512) + EPS) * (1.f / 2048);
    }
    const u4 z = pk4(lo4(xd[i]) * rs, hi4(xd[i]) * rs) & msk;
    *(u4*)(lds + (16 * i + (tid >> 4)) * 256 + ((l15 ^ (tid >> 4)) << 4)) = z;
  }
  __syncthreads();
  STAMP(KI, 2);
  u4 z[2][4], T[2], Y[2];
  f4 ar[2], ai[2];
  const f4 Z4 = {0, 0, 0, 0};
#pragma unroll
  for (int s = 0; s < 4; ++s)
#pragma unroll
    for (int t = 0; t < 2; ++t) z[t][s] = lds128(lds + (16 * t + l15) * 256 + (((4 * s + g) ^ l15) << 4));
  SB();
#pragma unroll
  for (int t = 0; t < 2; ++t)
#pragma unroll
    for (int s = 0; s < 4; ++s) { ar[t] = mf16(z[t][s], F64r[s], s ? ar[t] : Z4); ai[t] = mf16(z[t][s], F64i[s], s ? ai[t] : Z4); }
#pragma unroll
  for (int t = 0; t < 2; ++t) T[t] = cmulc<0>(ar[t], ai[t], t1c[t], t1s[t]);
#pragma unroll
  for (int m = 0; m < 2; ++m)
#pragma unroll
    for (int t = 0; t < 2; ++t) { ar[m] = mf16(F32r[m][t], T[t], t ? ar[m] : Z4); ai[m] = mf16(F32i[m][t], T[t], t ? ai[m] : Z4); }
#pragma unroll
  for (int m = 0; m < 2; ++m) Y[m] = cmulc<1>(ar[m], ai[m], lo4(fw[m]), hi4(fw[m]));
#pragma unroll
  for (int t = 0; t < 2; ++t)
#pragma unroll
    for (int m = 0; m < 2; ++m) { ar[t] = mf16(Y[m], F32r[t][m], m ? ar[t] : Z4); ai[t] = mf16(Y[m], F32i[t][m], m ? ai[t] : Z4); }
#pragma unroll
  for (int t = 0; t < 2; ++t) {
    const u4 v = cmulc<0>(ar[t], ai[t], t2c[t], t2s[t]);
    *(u4*)(lds + 8192 + (16 * t + l15) * 256 + (((4 * w + g) ^ l15) << 4)) = v;
  }
  __syncthreads();
#pragma unroll
  for (int s = 0; s < 4; ++s)
#pragma unroll
    for (int t = 0; t < 2; ++t) z[t][s] = lds128(lds + 8192 + (16 * t + l15) * 256 + (((4 * s + g) ^ l15) << 4));
  SB();
#pragma unroll
  for (int t = 0; t < 2; ++t)
#pragma unroll
    for (int s = 0; s < 4; ++s) { ar[t] = mf16(F64r[s], z[t][s], s ? ar[t] : Z4); ai[t] = mf16(F64i[s], z[t][s], s ? ai[t] : Z4); }
  const u32 im0 = col == 0 ? 0u : 0xffffffffu;
#pragma unroll
  for (int t = 0; t < 2; ++t) {
    const f4 re = lo4(xr[t]) + ar[t], im = hi4(xr[t]) - __builtin_bit_cast(f4, __builtin_bit_cast(u4, ai[t]) & im0);
    u32* o = xout + col * (2 * PS) + 32 * (16 * t + l15) + 8 * w + 2 * g;
    *(u2*)o = u2{pk(re[0], re[1]), pk(re[2], re[3])};
    *(u2*)(o + PS) = u2{pk(im[0], im[1]), pk(im[2], im[3])};
  }
  STAMP(KI, 3); STAMPW(KI, 4);
}

template <int LAST>
__global__ __launch_bounds__(512) void k_up(const u32* __restrict__ xh, const u32* __restrict__ wgu,
                                               u32* __restrict__ hb, u32* __restrict__ x3, float* __restrict__ rs2) {
  constexpr int NT = 6 + LAST, CH = NT * 4096 + 16384, NS = LAST ? 3 : 4, NI = NT + 4, PRE = UPPRE;
  __shared__ __attribute__((aligned(1024))) char lds[NS * CH];
  const int xc = blockIdx.x & 7, jb = blockIdx.x >> 3, mt = 8 * (xc & 1) + (jb & 7), ng = 4 * (xc >> 1) + (jb >> 3);
  const int t0 = (ng * 43) >> 3, t1 = ((ng + 1) * 43) >> 3;
  const int lane = threadIdx.x & 63, w8 = __builtin_amdgcn_readfirstlane(threadIdx.x >> 6), w = w8 & 3;
  const int wm = w >> 1, wn = w & 1, r = lane & 31, h = lane >> 5;
  constexpr int KI = LAST ? 5 : 2;
  STAMP(KI, 0);
  if (w8 >= 4) {
    const rsrc rq = mkr(xh), rp = mkr(wgu), rr = mkr((const char*)&R1T + 131072);
    const int vq = (lane >> 4) * (PS * 4) + (((lane & 15) ^ (4 * (lane >> 4))) << 4);
    auto issue = [&](int c, char* sl) {
#pragma unroll
      for (int i = 0; i < 4; ++i) blds(rq, vq, (64 * c + 16 * w + 4 * i) * (PS * 4) + 256 * mt, sl + NT * 4096 + (4 * w + i) * 1024);
#pragma unroll
      for (int i = 0; i < 3; ++i) {
        int e = 6 * w + 2 * i, so = (t0 + (e >> 2)) * 32768 + (4 * c + (e & 3)) * 1024;
        blds(rp, lane * 16, so, sl + e * 1024); blds<1024>(rp, lane * 16, so, sl + e * 1024);
      }
      if (LAST) blds(rr, lane * 16, (ng * 32 + 4 * c + w) * 1024, sl + (24 + w) * 1024);
    };
#pragma unroll
    for (int c = 0; c < PRE; ++c) issue(c, lds + c * CH);
    waitv<(PRE - 1) * NI>(); bar();
#pragma unroll
    for (int c = PRE; c < NS; ++c) issue(c, lds + c * CH);
    const int sOff = NT * 4096 + lane * 256 + ((ng ^ (4 * (lane & 3))) << 4) + 4 * w;
    u32 v;
    asm volatile("ds_read_b32 %0, %1\n s_waitcnt lgkmcnt(0)" : "=v"(v) : "v"((u32)(size_t)(LDSP char*)(lds + sOff)) : "memory");
    v = lane == 1 ? 0u : v;
    float s0 = lo(v) * lo(v), s1 = hi(v) * hi(v);
    int so = 0;
#pragma unroll 1
    for (int c = 0; c < 8; ++c) {
      if (c < 9 - NS) waitv<(NS - 2) * NI>(); else if (NS == 4 && c == 5) waitv<NI>(); else waitv<0>();
      bar();
      if (c < 8 - NS) issue(c + NS, lds + so);
      so = so == (NS - 1) * CH ? 0 : so + CH;
      if (c < 7) {
        asm volatile("ds_read_b32 %0, %1\n s_waitcnt lgkmcnt(0)" : "=v"(v) : "v"((u32)(size_t)(LDSP char*)(lds + so + sOff)) : "memory");
        s0 += lo(v) * lo(v); s1 += hi(v) * hi(v);
      }
    }
#pragma unroll
    for (int o = 1; o < 64; o <<= 1) { s0 += __shfl_xor(s0, o); s1 += __shfl_xor(s1, o); }
    if (lane < 2) rs2[128 * mt + 8 * ng + 2 * w + lane] = 1.f / ((lane ? s1 : s0) * (1.f / 512) + EPS);
    return;
  }
  f16v acc[3][2] = {}, accx = {};
  const int pOff = wn * 12288 + lane * 16;
  int qOff[2];
#pragma unroll
  for (int b = 0; b < 2; ++b)
    qOff[b] = NT * 4096 + (4 * h + ((lane >> 2) & 3)) * 256 + (((4 * (2 * wm + (b ^ wn)) + ((lane >> 3) & 2) + ((lane >> 1) & 1)) ^ (lane & 12)) << 4) + 8 * (lane & 1);
  struct Fr { u4 P[3], Q[2], X; };
  auto rd = [&](const char* sl, int ks) {
    Fr f;
#pragma unroll
    for (int b = 0; b < 2; ++b) f.Q[b] = ldtr(sl + qOff[b] + ks * 4096, 2048);
#pragma unroll
    for (int t = 0; t < 3; ++t) f.P[t] = lds128(sl + pOff + t * 4096 + ks * 1024);
    if (LAST) f.X = lds128(sl + 24576 + ks * 1024 + lane * 16);
    return f;
  };
  auto mm = [&](const Fr& f, int ks) {
#pragma unroll
    for (int t = 0; t < 3; ++t) { acc[t][0] = mf32(f.P[t], f.Q[0], acc[t][0]); acc[t][1] = mf32(f.P[t], f.Q[1], acc[t][1]); }
    if (LAST) accx = mf32(f.X, f.Q[0], accx);
    mix<6 + LAST, 2, 1>();
    SB();
  };
  bar();
  STAMP(KI, 1);
  Fr f0 = rd(lds, 0), f1;
  int so = 0;
#pragma unroll 1
  for (int c = 0; c < 8; ++c) {
    char* sl = lds + so;
    so = so == (NS - 1) * CH ? 0 : so + CH;
    SB();
    f1 = rd(sl, 1); mm(f0, 0);
    f0 = rd(sl, 2); mm(f1, 1);
    f1 = rd(sl, 3); mm(f0, 2);
    bar();
    SB();
    f0 = rd(lds + so, 0); mm(f1, 3);
  }
  STAMP(KI, 3);
#pragma unroll
  for (int t = 0; t < 3; ++t) {
    int tile = t0 + 3 * wn + t;
    if (tile < t1)
#pragma unroll
      for (int b = 0; b < 2; ++b) {
        const f16v& a = acc[t][b];
        u4 o;
#pragma unroll
        for (int j = 0; j < 4; ++j) { int i = 8 * (j >> 1) + 2 * (j & 1); o[j] = pk(fmaxf(a[i], 0.f) * a[i + 4], fmaxf(a[i + 1], 0.f) * a[i + 5]); }
        st4(hb + (((2 * mt + wm) * 2 + (b ^ wn)) * 86 + tile) * 256 + lane * 4, o);
      }
  }
  if (LAST) {
    u32* o = x3 + ((4 * mt + w) * 16 + ng) * 512 + lane * 4;
    st4(o, pack8(accx, 0)); st4(o + 256, pack8(accx, 1));
  }
  STAMP(KI, 4); STAMPW(KI, 5);
}

template <int LAST>
__global__ __launch_bounds__(512) void k_down(const u32* __restrict__ hb, const u32* __restrict__ wd, const float* __restrict__ rs2,
                                              const u32* __restrict__ res, u32* __restrict__ out, float* __restrict__ ssp) {
  __shared__ __attribute__((aligned(1024))) char lds[131072 + 512];
  const int xc = blockIdx.x & 7, jb = blockIdx.x >> 3, rg = 8 * (xc & 3) + (jb & 7), nt = 4 * (xc >> 2) + (jb >> 3);
  const int lane = threadIdx.x & 63, w8 = __builtin_amdgcn_readfirstlane(threadIdx.x >> 6), w = w8 & 3, r = lane & 31, h = lane >> 5;
  constexpr int KI = LAST ? 6 : 3;
  STAMP(KI, 0);
  if (w8 >= 4) {
    const rsrc rs = mkr(w < 2 ? wd + (2 * nt + w) * 86 * 256 : hb + (2 * rg + w - 2) * 86 * 256);
    auto issue = [&](int c) {
      char* sl = lds + (c & 3) * 32768 + w * 8192;
      unroll<4>([&](auto I) { blds<I.v * 1024>(rs, lane * 16, c * 8192, sl); });
      unroll<2>([&](auto I) { blds<I.v * 1024>(rs, lane * 16, c * 8192 + 4096, sl + 4096); });
      if (c < 10) unroll<2>([&](auto I) { blds<I.v * 1024 + 2048>(rs, lane * 16, c * 8192 + 4096, sl + 4096); });
    };
    issue(0); issue(1); issue(2);
    waitv<16>(); bar();
    issue(3);
#pragma unroll 1
    for (int c = 0; c < 10; ++c) {
      if (c < 7) waitv<16>(); else if (c == 7) waitv<14>(); else if (c == 8) waitv<6>(); else waitv<0>();
      bar();
      if (c < 7) issue(c + 4);
    }
    if (!LAST) bar();
    return;
  }
  const int t = w >> 1, b = w & 1, rho = 64 * rg + 32 * b + r;
  u32 rv[8];
  if (LAST) {
    const u32* p = res + ((2 * rg + b) * 16 + 2 * nt + t) * 512 + lane * 4;
    u4 x = *(const u4*)p, y = *(const u4*)(p + 256);
#pragma unroll
    for (int j = 0; j < 4; ++j) { rv[j] = x[j]; rv[4 + j] = y[j]; }
  } else {
    const u16* p = (const u16*)res + (64 * nt + 32 * t + 4 * h) * (2 * PS) + rho;
#pragma unroll
    for (int j = 0; j < 8; ++j) { const u16* e = p + (8 * (j >> 1) + 2 * (j & 1)) * (2 * PS); rv[j] = (u32)e[0] | ((u32)e[2 * PS] << 16); }
  }
  const float rs = rs2[rho];
  struct Fr { u4 A[4], B[4]; };
  const int aOff = t * 8192 + lane * 16, bOff = 16384 + b * 8192 + lane * 16;
  auto rd = [&](const char* sl, auto N) {
    Fr f;
#pragma unroll
    for (int k = 0; k < N.v; ++k) { f.A[k] = lds128(sl + aOff + k * 1024); f.B[k] = lds128(sl + bOff + k * 1024); }
    return f;
  };
  f16v acc = {};
  auto mm = [&](const Fr& f, auto N) {
#pragma unroll
    for (int k = 0; k < N.v; ++k) acc = mf32(f.A[k], f.B[k], acc);
    mix<N.v, 2, 1>();
    SB();
  };
  bar();
  STAMP(KI, 1);
  Fr f0 = rd(lds, IC<4>{}), f1;
#pragma unroll 1
  for (int c = 0; c < 10; ++c) {
    SB();
    f1 = rd(lds + (c & 3) * 32768 + 4096, IC<4>{}); mm(f0, IC<4>{});
    bar();
    SB();
    f0 = rd(lds + ((c + 1) & 3) * 32768, IC<4>{}); mm(f1, IC<4>{});
  }
  f1 = rd(lds + 2 * 32768 + 4096, IC<2>{}); mm(f0, IC<4>{});
  mm(f1, IC<2>{});
  STAMP(KI, 2);
  u32 v[8];
#pragma unroll
  for (int j = 0; j < 8; ++j) v[j] = pk(lo(rv[j]) + rs * acc[2 * j], hi(rv[j]) + rs * acc[2 * j + 1]);
  if (LAST) {
    u32* o = out + (32 * (32 * b + r) + rg) * 256 + 32 * nt + 16 * t + 8 * h;
    *(u4*)o = u4{v[0], v[1], v[2], v[3]}; *(u4*)(o + 4) = u4{v[4], v[5], v[6], v[7]};
  } else {
    const int c0 = 32 * nt + 16 * t + 2 * h;
    float ss = 0.f;
#pragma unroll
    for (int j = 0; j < 8; ++j) {
      st1(out + (c0 + 4 * (j >> 1) + (j & 1)) * XI + rho, v[j]);
      u32 m = (j == 0 && c0 == 0) ? (v[j] & 0xffffu) : v[j];
      ss = dot2(m, m, ss);
    }
    ss += __shfl_xor(ss, 32);
    float* sl = (float*)(lds + 131072);
    if (h == 0) sl[w * 32 + r] = ss;
    bar();
    if (w == 0) ssp[nt * SS + 64 * rg + lane] = sl[lane] + sl[64 + lane];
  }
  STAMP(KI, 3); STAMPW(KI, 4);
}

extern "C" void kernel_launch(void* const* d_in, const int* in_sizes, int n_in, void* d_out, int out_size, void* d_ws, size_t ws_size,
                              hipStream_t stream) {
  const u16 *x = (const u16*)d_in[0], *wr = (const u16*)d_in[1], *wi = (const u16*)d_in[2];
  const u16 *wg = (const u16*)d_in[4], *wu = (const u16*)d_in[5], *wd = (const u16*)d_in[6];
  char* ws = (char*)d_ws;
  const size_t MB = 1 << 20;
  u32 *xh0 = (u32*)ws, *xh1 = (u32*)(ws + 3 * MB), *xh2 = (u32*)(ws + 6 * MB), *xh3 = (u32*)(ws + 9 * MB);
  u32 *filt = (u32*)(ws + 12 * MB), *x3 = (u32*)(ws + 15 * MB), *wgu = (u32*)(ws + 17 * MB), *wd1 = (u32*)(ws + 20 * MB);
  u32 *wd2 = (u32*)(ws + 22 * MB), *hb = (u32*)(ws + 24 * MB), *hb2 = (u32*)(ws + 30 * MB);
  float *rstd = (float*)(ws + 36 * MB), *ssp = rstd + 4096, *rs2 = rstd + 32768, *rs3 = rstd + 65536;
  k_prep<<<303, 512, 0, stream>>>(x, wr, wi, wg, wu, wd, xh0, (u32*)rstd, wgu, wd1, wd2, filt);
  k_fft<1><<<256, 256, 0, stream>>>(xh0, rstd, filt, xh1);
  k_up<0><<<256, 512, 0, stream>>>(xh1, wgu, hb, x3, rs2);
  k_down<0><<<256, 512, 0, stream>>>(hb, wd1, rs2, xh1, xh2, ssp);
  k_fft<8><<<256, 256, 0, stream>>>(xh2, ssp, filt, xh3);
  k_up<1><<<256, 512, 0, stream>>>(xh3, wgu, hb2, x3, rs3);
  k_down<1><<<256, 512, 0, stream>>>(hb2, wd2, rs3, x3, (u32*)d_out, ssp);
}
```

```cpp
#include <hip/hip_runtime.h>
#include <hip/hip_bf16.h>

typedef unsigned u32;
typedef unsigned short u16;
typedef __attribute__((ext_vector_type(8))) __bf16 bf8;
typedef __attribute__((ext_vector_type(2))) __bf16 bf2;
typedef __attribute__((ext_vector_type(2))) float f2;
typedef __attribute__((ext_vector_type(4))) float f4;
typedef __attribute__((ext_vector_type(16))) float f16v;
typedef __attribute__((ext_vector_type(2))) u32 u2;
typedef __attribute__((ext_vector_type(4))) u32 u4;
typedef __attribute__((ext_vector_type(4))) short s4;
typedef u4 __attribute__((aligned(2))) u4_ua;
#define DI __device__ __forceinline__
#define LDSP __attribute__((address_space(3)))
#define GLBP __attribute__((address_space(1)))

#define STAMP(k, j)
#define STAMPW(k, j)
#define ROT 0
#define UPPRE 2
constexpr int SS = 2048;
constexpr int XI = 2112, PS = 1088, FS = 2080;
constexpr float EPS = 1e-5f;

struct Part { u16 v[16384]; };
#if defined(__HIP_DEVICE_COMPILE__)
constexpr double kPi = 3.14159265358979323846;
constexpr double csin(double x) { double t = x, s = x; for (int i = 1; i < 14; ++i) { t *= -x * x / ((2 * i) * (2 * i + 1)); s += t; } return s; }
constexpr u16 tobf(double x) { float f = (float)x; u32 u = __builtin_bit_cast(u32, f); u += 0x7fffu + ((u >> 16) & 1u); return (u16)(u >> 16); }
struct CosT { u16 v[512]; };
constexpr CosT mkcos() {
  CosT c{};
  for (int i = 0; i < 512; ++i) {
    int q = (i + 128) & 511;
    int y = q <= 128 ? q : (q <= 384 ? 256 - q : q - 512);
    c.v[i] = y == 0 ? (u16)0 : tobf(csin(2 * kPi * y / 512) / 16.0);
  }
  return c;
}
constexpr CosT kCos = mkcos();
constexpr u16 kDC = tobf(0.04419417382415922);
constexpr Part mkR(int pi) {
  Part p{};
  for (int e = 0; e < 16384; ++e) {
    int r = (e >> 3) & 31, h = (e >> 8) & 1, j = e & 7, d = 0, s = 0;
    if (pi < 4) { d = (((e >> 9) & 7) << 4) + 8 * h + j; s = 32 * (4 * pi + (e >> 12)) + r; }
    else { d = 32 * (pi - 4) + (r & 3) + 4 * (r >> 3) + 16 * ((r >> 2) & 1); s = ((e >> 9) << 4) + 8 * (j >> 2) + 4 * h + (j & 3); }
    p.v[e] = s >= 2 ? kCos.v[((s >> 1) * d + ((s & 1) << 7)) & 511] : (u16)(kDC | ((s == 0 && (d & 1)) ? 0x8000 : 0));
  }
  return p;
}
template <int I> struct PR { static constexpr Part v = mkR(I); };
struct Tab { Part p[20]; };
__device__ const __attribute__((aligned(1024))) Tab R1T = {{PR<0>::v, PR<1>::v, PR<2>::v, PR<3>::v, PR<4>::v, PR<5>::v, PR<6>::v, PR<7>::v,
  PR<8>::v, PR<9>::v, PR<10>::v, PR<11>::v, PR<12>::v, PR<13>::v, PR<14>::v, PR<15>::v, PR<16>::v, PR<17>::v, PR<18>::v, PR<19>::v}};
#else
struct Tab { u16 p[8]; };
__device__ const Tab R1T = {};
#endif

DI f16v mf32(u4 a, u4 b, f16v c) { return __builtin_amdgcn_mfma_f32_32x32x16_bf16(__builtin_bit_cast(bf8, a), __builtin_bit_cast(bf8, b), c, 0, 0, 0); }
DI f4 mf16(u4 a, u4 b, f4 c) { return __builtin_amdgcn_mfma_f32_16x16x32_bf16(__builtin_bit_cast(bf8, a), __builtin_bit_cast(bf8, b), c, 0, 0, 0); }
DI u32 pk(float a, float b) { f2 v = {a, b}; return __builtin_bit_cast(u32, __builtin_convertvector(v, bf2)); }
DI float lo(u32 u) { return __builtin_bit_cast(float, u << 16); }
DI float hi(u32 u) { return __builtin_bit_cast(float, u & 0xffff0000u); }
DI float dot2(u32 a, u32 b, float c) { return __builtin_amdgcn_fdot2_f32_bf16(__builtin_bit_cast(bf2, a), __builtin_bit_cast(bf2, b), c, false); }
DI void glds(const void* g, char* l) { __builtin_amdgcn_global_load_lds((const GLBP void*)g, (LDSP void*)l, 16, 0, 0); }
typedef __amdgpu_buffer_rsrc_t rsrc;
DI rsrc mkr(const void* p) { return __builtin_amdgcn_make_buffer_rsrc((void*)p, 0, 0x7fffffff, 0x00020000); }
template <int O = 0> DI void blds(rsrc r, int vo, int so, char* l) { __builtin_amdgcn_raw_ptr_buffer_load_lds(r, (LDSP void*)l, 16, vo, so, O, 0); }
DI u32 selfpf(int nb) {
  const rsrc r = __builtin_amdgcn_make_buffer_rsrc((void*)(__builtin_amdgcn_s_getpc() & ~127ll), 0, nb, 0x00020000);
  return __builtin_amdgcn_raw_buffer_load_b32(r, (threadIdx.x & 255) * 128, 0, 0);
}
DI void keep(u32 v) { asm volatile("" ::"v"(v)); }
DI u4 ldtr(const char* a, int d) {
  u2 x = __builtin_bit_cast(u2, __builtin_amdgcn_ds_read_tr16_b64_v4i16((LDSP s4*)a));
  u2 y = __builtin_bit_cast(u2, __builtin_amdgcn_ds_read_tr16_b64_v4i16((LDSP s4*)(a + d)));
  return u4{x[0], x[1], y[0], y[1]};
}
#define WT 2
DI void st4(u32* p, u4 v) {
  if (WT == 2) asm volatile("global_store_dwordx4 %0, %1, off sc0 sc1\n s_nop 1" ::"v"(p), "v"(v) : "memory");
  else if (WT == 1) __builtin_nontemporal_store(v, (u4*)p);
  else *(u4*)p = v;
}
DI void st2(u32* p, u2 v) {
  if (WT == 2) asm volatile("global_store_dwordx2 %0, %1, off sc0 sc1" ::"v"(p), "v"(v) : "memory");
  else if (WT == 1) __builtin_nontemporal_store(v, (u2*)p);
  else *(u2*)p = v;
}
DI void st1(u32* p, u32 v) {
  if (WT == 2) asm volatile("global_store_dword %0, %1, off sc0 sc1" ::"v"(p), "v"(v) : "memory");
  else if (WT == 1) __builtin_nontemporal_store(v, p);
  else *p = v;
}
DI u4 lds128(const char* p) { return *(const u4*)p; }
DI u4 pack8(const f16v& a, int s) { u4 o = {pk(a[8 * s], a[8 * s + 1]), pk(a[8 * s + 2], a[8 * s + 3]), pk(a[8 * s + 4], a[8 * s + 5]), pk(a[8 * s + 6], a[8 * s + 7])}; return o; }
template <int N> DI void waitv() { asm volatile("s_waitcnt vmcnt(%0)" ::"n"(N) : "memory"); }
DI void bar() { asm volatile("s_waitcnt lgkmcnt(0)" ::: "memory"); __builtin_amdgcn_s_barrier(); asm volatile("" ::: "memory"); }
DI int dperm(int m) { return (m & 3) + 4 * (m >> 3) + 16 * ((m >> 2) & 1); }
DI int fperm(int m) { int i = (m & 3) + 4 * (m >> 3); return 16 * (i >> 3) + 8 * ((m >> 2) & 1) + (i & 7); }
#define SB() __builtin_amdgcn_sched_barrier(0)
#define SGB(m, n) __builtin_amdgcn_sched_group_barrier(m, n, 0)
template <int NM, int ND, int NV> DI void mix() { if constexpr (NM > 0) { SGB(0x008, 1); SGB(0x100, ND); SGB(0x002, NV); mix<NM - 1, ND, NV>(); } }
template <int I> struct IC { static constexpr int v = I; };
template <int N, class F> DI void unroll(F&& f) { if constexpr (N > 0) { unroll<N - 1>(f); f(IC<N - 1>{}); } }

template <int JOB>
DI void prep_gemm(const u16* __restrict__ q0, const u16* __restrict__ q1, int qt, int nt, int c0, u32 pf, char* lds,
                  u32* __restrict__ o0, u32* __restrict__ o1) {
  const int lane = threadIdx.x & 63, w8 = __builtin_amdgcn_readfirstlane(threadIdx.x >> 6), w = w8 & 3;
  const int wp = w >> 1, wq = w & 1, r = lane & 31, h = lane >> 5;
  if (w8 >= 4) {
    const rsrc rp = mkr(&R1T), ra = mkr(q0), rb = mkr(q1);
    int qv[4];
#pragma unroll
    for (int i = 0; i < 4; ++i) {
      if (JOB == 1) {
        int d = 16 * w + 8 * (i >> 1) + (lane >> 3), pos = lane & 7;
        int f = 64 * qt + 16 * ((pos >> 1) ^ (d & 3) ^ (i & 1)) + 8 * (pos & 1);
        qv[i] = (d * 1376 + (f > 1368 ? 1368 : f)) * 2;
      } else {
        int u = 32 * w + 8 * i + (lane >> 3), row;
        if (JOB == 0) { int rho = 128 * qt + (u & 64) + 2 * (u & 31) + ((u >> 5) & 1); row = 32 * (rho & 63) + (rho >> 6); }
        else { row = 128 * qt + (u & 96) + fperm(u & 31); row = row > 1375 ? 1375 : row; }
        qv[i] = row * 1024 + (((lane & 7) ^ ((u >> 1) & 7)) << 4);
      }
    }
    constexpr int QSTEP = JOB == 1 ? 64 * 1376 * 2 : 128;
    auto issP = [&](int c) { unroll<4>([&](auto I) { blds<I.v * 1024>(rp, lane * 16, (4 * nt + w) * 8192 + c * 4096, lds + w * 8192 + c * 4096); }); };
    auto issue = [&](int cc) {
      const int c = (cc + c0) & 7;
#pragma unroll
      for (int i = 0; i < 4; ++i) blds((JOB == 1 && (i & 1)) ? rb : ra, qv[i], c * QSTEP, lds + 32768 + c * 16384 + (4 * w + i) * 1024);
    };
    issP(0); issP(1); issue(0); issue(1); issue(2);
    waitv<8>(); bar();
    keep(pf);
#pragma unroll 1
    for (int c = 3; c < 8; ++c) issue(c);
    unroll<8>([&](auto I) { waitv<(I.v < 6 ? 4 * (6 - I.v) : 0)>(); bar(); });
    if (JOB == 0) {
      const int u = 32 * nt + 8 * w + (lane >> 3);
      float s1 = 0.f, s2 = 0.f;
#pragma unroll
      for (int c = 0; c < 8; ++c) {
        u4 v = lds128(lds + 32768 + c * 16384 + u * 128 + (lane & 7) * 16);
#pragma unroll
        for (int j = 0; j < 4; ++j) { s1 = dot2(v[j], 0x3f803f80u, s1); s2 = dot2(v[j], v[j], s2); }
      }
#pragma unroll
      for (int o = 1; o < 8; o <<= 1) { s1 += __shfl_xor(s1, o); s2 += __shfl_xor(s2, o); }
      float m = s1 * (1.f / 512), var = fmaxf(s2 * (1.f / 512) - m * m, 0.f);
      if ((lane & 7) == 0) ((float*)o1)[128 * qt + (u & 64) + 2 * (u & 31) + ((u >> 5) & 1)] = __builtin_amdgcn_rsqf(var + EPS) * (1.f / 2048);
    }
    return;
  }
  f16v acc[2][2] = {}, acc2[2][2] = {};
  int qOff;
  if (JOB == 1) {
    int g = lane >> 4, qr = (lane >> 2) & 3, run = 4 * (g & 1) + (lane & 3), q = run >> 1;
    qOff = 32768 + (g >> 1) * 2048 + qr * 128 + (q & 1) * 1024 + (8 * (run & 1) + 4 * (q >> 1)) * 2;
  } else qOff = 32768 + (64 * wq + r) * 128;
  struct Fr { u4 v[2]; };
  int pOff; u32 sgn;
  auto turn = [&](int c) {
    int n = ((r >> 1) * (c >> 1)) & 3;
    pOff = wp * 16384 + (c & 1) * 4096 + (lane ^ (n & 1)) * 16;
    sgn = (((r & 1) ? n + 1 : n) & 2) ? 0x80008000u : 0u;
  };
  auto rdP = [&](int ks) { return Fr{{lds128(lds + pOff + ks * 1024), lds128(lds + pOff + 8192 + ks * 1024)}}; };
  auto rdQ = [&](const char* sl, int ks) {
    Fr f;
#pragma unroll
    for (int b = 0; b < 2; ++b) {
      if (JOB == 1) {
        f.v[b] = ldtr(sl + qOff + ks * 4096 + (((2 * wq + b) ^ ((lane >> 2) & 3) ^ ((lane >> 1) & 1)) << 5), 512);
      } else f.v[b] = lds128(sl + qOff + b * 4096 + (((2 * ks + h) ^ ((r >> 1) & 7)) << 4));
    }
    return f;
  };
  u4 Id[2];
  if (JOB == 2) {
#pragma unroll
    for (int k = 0; k < 2; ++k) {
      int e = dperm(r) - 16 * k - 8 * h;
#pragma unroll
      for (int j = 0; j < 4; ++j) Id[k][j] = (e == 2 * j ? 0x3f80u : 0u) | (e == 2 * j + 1 ? 0x3f800000u : 0u);
    }
  }
  auto mm = [&](const Fr& P, const Fr& Q, Fr& N, u32 sg, int c, int ks) {
#pragma unroll
    for (int t = 0; t < 2; ++t)
#pragma unroll
      for (int b = 0; b < 2; ++b) acc[t][b] = JOB == 2 ? mf32(Q.v[b], P.v[t], acc[t][b]) : mf32(P.v[t], Q.v[b], acc[t][b]);
    N.v[0] ^= sg; N.v[1] ^= sg;
    SGB(0x008, 1); SGB(0x100, 3); SGB(0x008, 1); SGB(0x100, 3); SGB(0x002, 4); SGB(0x008, 1); SGB(0x002, 4); SGB(0x008, 1);
    asm volatile("" : "+v"(N.v[0]), "+v"(N.v[1]));
    SB();
    if (JOB == 2) {
      if (c == 2 * nt + wp) {
#pragma unroll
        for (int b = 0; b < 2; ++b) acc2[ks >> 1][b] = mf32(Q.v[b], Id[ks & 1], acc2[ks >> 1][b]);
      }
    }
  };
  turn(c0);
  bar();
  STAMP(0, 2);
  Fr qa = rdQ(lds + c0 * 16384, 0), pa = rdP(0), pb = rdP(1), qb, pc, pd;
  pa.v[0] ^= sgn; pa.v[1] ^= sgn;
#pragma unroll 1
  for (int cc = 0; cc < 8; ++cc) {
    const int c = (cc + c0) & 7;
    const char* sl = lds + c * 16384;
    const u32 sg = sgn;
    SB();
    qb = rdQ(sl, 1); pc = rdP(2); mm(pa, qa, pb, sg, c, 0);
    qa = rdQ(sl, 2); pd = rdP(3); mm(pb, qb, pc, sg, c, 1);
    turn((c + 1) & 7);
    SB();
    qb = rdQ(sl, 3); pa = rdP(0); mm(pc, qa, pd, sg, c, 2);
    asm volatile("" ::: "memory"); __builtin_amdgcn_s_barrier(); asm volatile("" ::: "memory");
    SB();
    qa = rdQ(lds + ((c + 1) & 7) * 16384, 0); pb = rdP(1); mm(pd, qb, pa, sgn, c, 3);
  }
  STAMP(0, 3);
  if (JOB == 0) {
#pragma unroll
    for (int t = 0; t < 2; ++t)
#pragma unroll
      for (int i = 0; i < 16; i += 2) {
        int col = 64 * nt + 32 * wp + 16 * t + (((i & 3) + 8 * (i >> 2) + 4 * h) >> 1);
        u2 v = {pk(acc[t][0][i], acc[t][0][i + 1]), pk(acc[t][1][i], acc[t][1][i + 1])};
        st2(o0 + col * XI + 128 * qt + 64 * wq + 2 * r, v);
      }
  } else if (JOB == 1) {
#pragma unroll
    for (int b = 0; b < 2; ++b) {
      int tile = 4 * qt + 2 * wq + b;
      if (tile < 86)
#pragma unroll
        for (int t = 0; t < 2; ++t)
#pragma unroll
          for (int s = 0; s < 2; ++s) {
            int ks = 8 * nt + 4 * wp + 2 * t + s;
            u4 v = pack8(acc[t][b], s);
            if (ks == 0 && h == 0) v[0] &= 0xffffu;
            st4(o0 + (tile * 32 + ks) * 256 + lane * 4, v);
          }
    }
  } else {
#pragma unroll
    for (int b = 0; b < 2; ++b)
#pragma unroll
      for (int s = 0; s < 2; ++s) {
        int ks = 8 * qt + 4 * wq + 2 * b + s;
        if (ks < 86)
#pragma unroll
          for (int t = 0; t < 2; ++t) {
            int o = ((4 * nt + 2 * wp + t) * 86 + ks) * 256 + lane * 4;
            st4(o0 + o, pack8(acc[t][b], s));
            st4(o1 + o, pack8(acc2[t][b], s));
          }
      }
  }
}

__global__ __launch_bounds__(512) void k_prep(const u16* __restrict__ x, const u16* __restrict__ wr, const u16* __restrict__ wi,
    const u16* __restrict__ wg, const u16* __restrict__ wu, const u16* __restrict__ wd,
    u32* __restrict__ xh, u32* __restrict__ rstd, u32* __restrict__ wgu, u32* __restrict__ wd1, u32* __restrict__ wd2,
    u32* __restrict__ filt) {
  __shared__ __attribute__((aligned(1024))) char lds[163840];
  const int b = blockIdx.x, nt = (b >> 3) & 3, q = 8 * (b >> 5) + (b & 7), c0 = ROT;
  const int lane = threadIdx.x & 63, w = __builtin_amdgcn_readfirstlane(threadIdx.x >> 6);
  STAMP(0, 0);
  u32 pf = 0;
  if (w >= 4) pf = selfpf(12288);
  if (q < 16) prep_gemm<0>(x, x, q, nt, c0, pf, lds, xh, rstd);
  else if (q < 38) prep_gemm<1>(wg, wu, q - 16, nt, 0, pf, lds, wgu, wgu);
  else if (q < 49) prep_gemm<2>(wd, wd, q - 38, nt, c0, pf, lds, wd1, wd2);
  else {
    const int u = b < 256 ? (q - 49) * 4 + nt : b - 196, k0 = 16 * (u >> 4) + 8 * (u & 1) + 256 * ((u >> 1) & 7);
    const rsrc rs = mkr(w < 4 ? wr : wi);
    const int so = (k0 + 64 * (w & 3)) * 514;
    unroll<4>([&](auto I) { blds<I.v * 1024>(rs, lane * 16, so, lds + w * 4160); });
    if (lane == 0) blds(rs, 0, so + 4096, lds + w * 4160 + 4096);
    blds(mkr((const char*)&R1T + 131072), lane * 16, (u * 8 + w) * 1024, lds + 40960 + w * 1024);
    if (w < 6) {
      u32 lo_, hi_; int nb;
#define PCOF(sym, n) { asm volatile("s_getpc_b64 s[40:41]\n s_add_u32 %0, s40, " sym "@rel32@lo+4\n s_addc_u32 %1, s41, " sym "@rel32@hi+12" : "=s"(lo_), "=s"(hi_) :: "scc", "s40", "s41"); nb = n; }
      if (w == 0) PCOF("_Z5k_fftILi1EEvPKjPKfS1_Pj", 3904)
      else if (w == 1) PCOF("_Z4k_upILi0EEvPKjS1_PjS2_Pf", 4352)
      else if (w == 2) PCOF("_Z6k_downILi0EEvPKjS1_PKfS1_PjPf", 2688)
      else if (w == 3) PCOF("_Z5k_fftILi8EEvPKjPKfS1_Pj", 4352)
      else if (w == 4) PCOF("_Z4k_upILi1EEvPKjS1_PjS2_Pf", 4608)
      else PCOF("_Z6k_downILi1EEvPKjS1_PKfS1_PjPf", 1920)
      const rsrc rc = __builtin_amdgcn_make_buffer_rsrc((void*)(((unsigned long long)hi_ << 32) | lo_), 0, nb, 0x00020000);
      unroll<4>([&](auto I) { blds<I.v * 1024>(rc, lane * 16, 0, lds + 65536 + w * 8192); });
      blds(rc, lane * 16 + 4096, 0, lds + 65536 + w * 8192 + 4096);
    }
    waitv<0>(); bar();
    keep(pf);
    STAMP(0, 1);
#pragma unroll
    for (int it = 0; it < 4; ++it) {
      const int cs = 1 + 32 * w + 8 * it + (lane >> 3);
      const u16* p = (const u16*)(lds + (lane & 7) * 514) + cs;
      u4 v;
#pragma unroll
      for (int j = 0; j < 4; ++j) v[j] = (u32)p[j * 2080] | ((u32)p[(4 + j) * 2080] << 16);
      st4(filt + (cs & 255) * FS + (u >> 3) * 256 + (((u >> 1) & 3) * 16 + 8 * (u & 1) + (lane & 7)) * 4, v);
    }
  }
  STAMPW(0, q < 16 ? 4 : q < 38 ? 5 : q < 49 ? 6 : 7);
}

DI u4 rot(u4 v) { u4 o; for (int j = 0; j < 4; ++j) o[j] = __builtin_amdgcn_alignbit(v[j], v[j], 16) ^ 0x8000u; return o; }
template <int NP>
__global__ __launch_bounds__(256, 2) void k_fft(const u32* __restrict__ xin, const float* __restrict__ st, const u32* __restrict__ filt,
                                                u32* __restrict__ xout) {
  __shared__ __attribute__((aligned(256))) char lds[16384];
  const int col = blockIdx.x, tid = threadIdx.x, lane = tid & 63, w = tid >> 6, l15 = lane & 15, g = lane >> 4;
  constexpr int KI = NP == 1 ? 1 : 4;
  STAMP(KI, 0);
  const u32* xc = xin + col * XI;
  u4 xd[2], xr[2], fw[2];
  f4 sv[NP][2];
#pragma unroll
  for (int i = 0; i < 2; ++i) xd[i] = *(const u4*)(xc + 1024 * i + 4 * tid);
#pragma unroll
  for (int p = 0; p < NP; ++p)
#pragma unroll
    for (int i = 0; i < 2; ++i) sv[p][i] = *(const f4*)(st + p * SS + 1024 * i + 4 * tid);
#pragma unroll
  for (int i = 0; i < 2; ++i) {
    fw[i] = *(const u4*)(filt + col * FS + (2 * w + i) * 256 + lane * 4);
    xr[i] = *(const u4*)(xc + 64 * (16 * i + l15) + 16 * w + 4 * g);
  }
  SB();
  const int k1 = 16 * w + l15;
  const float fk1 = (float)k1, fl = (float)l15, fa = (float)(4 * g);
  u4 F64r[4], F64i[4], F32r[2][2], F32i[2][2];
  float t1c[2][4], t1s[2][4], t2c[2][4], t2s[2][4];
#pragma unroll
  for (int j = 0; j < 4; ++j) {
    float a = (fa + j) * fk1 * (1.f / 64), b = (fa + j) * fl * (1.f / 32);
    F64r[0][j] = pk(__builtin_amdgcn_cosf(a), __builtin_amdgcn_sinf(a));
    F32r[0][0][j] = pk(__builtin_amdgcn_cosf(b), __builtin_amdgcn_sinf(b));
  }
  F64i[0] = rot(F64r[0]); F32i[0][0] = rot(F32r[0][0]);
  const u32 NEG = 0x80008000u, so = (lane & 1) ? NEG : 0u, sr = (lane & 2) ? NEG : 0u, si = ((lane + 1) & 2) ? NEG : 0u;
#pragma unroll
  for (int j = 0; j < 4; ++j) {
    F64r[1][j] = ((lane & 1) ? F64i[0][j] : F64r[0][j]) ^ sr;
    F64i[1][j] = ((lane & 1) ? F64r[0][j] : F64i[0][j]) ^ si;
    F64r[2][j] = F64r[0][j] ^ so; F64i[2][j] = F64i[0][j] ^ so;
    F64r[3][j] = F64r[1][j] ^ so; F64i[3][j] = F64i[1][j] ^ so;
    const u32 sj = (j & 1) ? NEG : 0u;
    F32r[0][1][j] = F32r[0][0][j] ^ so; F32i[0][1][j] = F32i[0][0][j] ^ so;
    F32r[1][0][j] = F32r[0][0][j] ^ sj; F32i[1][0][j] = F32i[0][0][j] ^ sj;
    F32r[1][1][j] = F32r[0][1][j] ^ sj; F32i[1][1][j] = F32i[0][1][j] ^ sj;
  }
#pragma unroll
  for (int t = 0; t < 2; ++t)
#pragma unroll
    for (int j = 0; j < 4; ++j) {
      float a = (fa + (16 * t + j)) * (fk1 * (1.f / 2048)), b = ((fa + (float)(16 * w)) + j) * ((fl + 16 * t) * (1.f / 2048));
      t1c[t][j] = __builtin_amdgcn_cosf(a); t1s[t][j] = __builtin_amdgcn_sinf(a);
      t2c[t][j] = __builtin_amdgcn_cosf(b); t2s[t][j] = __builtin_amdgcn_sinf(b);
    }
  SB();
  STAMP(KI, 1);
  const u32 msk = col == 0 ? 0xffffu : 0xffffffffu;
#pragma unroll
  for (int i = 0; i < 2; ++i) {
    f4 rs = sv[0][i];
    if (NP > 1) {
#pragma unroll
      for (int p = 1; p < NP; ++p) rs += sv[p][i];
#pragma unroll
      for (int j = 0; j < 4; ++j) rs[j] = __builtin_amdgcn_rsqf(rs[j] * (1.f / 512) + EPS) * (1.f / 2048);
    }
    u4 z;
#pragma unroll
    for (int j = 0; j < 4; ++j) z[j] = pk(lo(xd[i][j]) * rs[j], hi(xd[i][j]) * rs[j]) & msk;
    *(u4*)(lds + (16 * i + (tid >> 4)) * 256 + ((l15 ^ (tid >> 4)) << 4)) = z;
  }
  __syncthreads();
  STAMP(KI, 2);
  u4 z[2][4], T[2], Y[2];
  f4 ar[2], ai[2];
  const f4 Z4 = {0, 0, 0, 0};
#pragma unroll
  for (int s = 0; s < 4; ++s)
#pragma unroll
    for (int t = 0; t < 2; ++t) z[t][s] = lds128(lds + (16 * t + l15) * 256 + (((4 * s + g) ^ l15) << 4));
  SB();
#pragma unroll
  for (int t = 0; t < 2; ++t)
#pragma unroll
    for (int s = 0; s < 4; ++s) { ar[t] = mf16(z[t][s], F64r[s], s ? ar[t] : Z4); ai[t] = mf16(z[t][s], F64i[s], s ? ai[t] : Z4); }
#pragma unroll
  for (int t = 0; t < 2; ++t)
#pragma unroll
    for (int j = 0; j < 4; ++j) T[t][j] = pk(ar[t][j] * t1c[t][j] + ai[t][j] * t1s[t][j], ai[t][j] * t1c[t][j] - ar[t][j] * t1s[t][j]);
#pragma unroll
  for (int m = 0; m < 2; ++m)
#pragma unroll
    for (int t = 0; t < 2; ++t) { ar[m] = mf16(F32r[m][t], T[t], t ? ar[m] : Z4); ai[m] = mf16(F32i[m][t], T[t], t ? ai[m] : Z4); }
#pragma unroll
  for (int m = 0; m < 2; ++m)
#pragma unroll
    for (int j = 0; j < 4; ++j) { float a = lo(fw[m][j]), b = hi(fw[m][j]); Y[m][j] = pk(ar[m][j] * a - ai[m][j] * b, -(ar[m][j] * b) - ai[m][j] * a); }
#pragma unroll
  for (int t = 0; t < 2; ++t)
#pragma unroll
    for (int m = 0; m < 2; ++m) { ar[t] = mf16(Y[m], F32r[t][m], m ? ar[t] : Z4); ai[t] = mf16(Y[m], F32i[t][m], m ? ai[t] : Z4); }
#pragma unroll
  for (int t = 0; t < 2; ++t) {
    u4 v;
#pragma unroll
    for (int j = 0; j < 4; ++j) v[j] = pk(ar[t][j] * t2c[t][j] + ai[t][j] * t2s[t][j], ai[t][j] * t2c[t][j] - ar[t][j] * t2s[t][j]);
    *(u4*)(lds + 8192 + (16 * t + l15) * 256 + (((4 * w + g) ^ l15) << 4)) = v;
  }
  __syncthreads();
#pragma unroll
  for (int s = 0; s < 4; ++s)
#pragma unroll
    for (int t = 0; t < 2; ++t) z[t][s] = lds128(lds + 8192 + (16 * t + l15) * 256 + (((4 * s + g) ^ l15) << 4));
  SB();
#pragma unroll
  for (int t = 0; t < 2; ++t)
#pragma unroll
    for (int s = 0; s < 4; ++s) { ar[t] = mf16(F64r[s], z[t][s], s ? ar[t] : Z4); ai[t] = mf16(F64i[s], z[t][s], s ? ai[t] : Z4); }
  const float sg = col == 0 ? 0.f : -1.f;
#pragma unroll
  for (int t = 0; t < 2; ++t) {
    float re[4], im[4];
#pragma unroll
    for (int j = 0; j < 4; ++j) { re[j] = lo(xr[t][j]) + ar[t][j]; im[j] = hi(xr[t][j]) + (col == 0 ? 0.f : sg * ai[t][j]); }
    u32* o = xout + col * (2 * PS) + 32 * (16 * t + l15) + 8 * w + 2 * g;
    *(u2*)o = u2{pk(re[0], re[1]), pk(re[2], re[3])};
    *(u2*)(o + PS) = u2{pk(im[0], im[1]), pk(im[2], im[3])};
  }
  STAMP(KI, 3); STAMPW(KI, 4);
}

template <int LAST>
__global__ __launch_bounds__(512) void k_up(const u32* __restrict__ xh, const u32* __restrict__ wgu,
                                               u32* __restrict__ hb, u32* __restrict__ x3, float* __restrict__ rs2) {
  constexpr int NT = 6 + LAST, CH = NT * 4096 + 16384, NS = LAST ? 3 : 4, NI = NT + 4, PRE = UPPRE;
  __shared__ __attribute__((aligned(1024))) char lds[NS * CH];
  const int xc = blockIdx.x & 7, jb = blockIdx.x >> 3, mt = 8 * (xc & 1) + (jb & 7), ng = 4 * (xc >> 1) + (jb >> 3);
  const int t0 = (ng * 43) >> 3, t1 = ((ng + 1) * 43) >> 3;
  const int lane = threadIdx.x & 63, w8 = __builtin_amdgcn_readfirstlane(threadIdx.x >> 6), w = w8 & 3;
  const int wm = w >> 1, wn = w & 1, r = lane & 31, h = lane >> 5;
  constexpr int KI = LAST ? 5 : 2;
  STAMP(KI, 0);
  if (w8 >= 4) {
    const rsrc rq = mkr(xh), rp = mkr(wgu), rr = mkr((const char*)&R1T + 131072);
    const int vq = (lane >> 4) * (PS * 4) + (((lane & 15) ^ (4 * (lane >> 4))) << 4);
    auto issue = [&](int c, char* sl) {
#pragma unroll
      for (int i = 0; i < 4; ++i) blds(rq, vq, (64 * c + 16 * w + 4 * i) * (PS * 4) + 256 * mt, sl + NT * 4096 + (4 * w + i) * 1024);
#pragma unroll
      for (int i = 0; i < 3; ++i) {
        int e = 6 * w + 2 * i, so = (t0 + (e >> 2)) * 32768 + (4 * c + (e & 3)) * 1024;
        blds(rp, lane * 16, so, sl + e * 1024); blds<1024>(rp, lane * 16, so, sl + e * 1024);
      }
      if (LAST) blds(rr, lane * 16, (ng * 32 + 4 * c + w) * 1024, sl + (24 + w) * 1024);
    };
#pragma unroll
    for (int c = 0; c < PRE; ++c) issue(c, lds + c * CH);
    waitv<(PRE - 1) * NI>(); bar();
#pragma unroll
    for (int c = PRE; c < NS; ++c) issue(c, lds + c * CH);
    const int sOff = NT * 4096 + lane * 256 + ((ng ^ (4 * (lane & 3))) << 4) + 4 * w;
    u32 v;
    asm volatile("ds_read_b32 %0, %1\n s_waitcnt lgkmcnt(0)" : "=v"(v) : "v"((u32)(size_t)(LDSP char*)(lds + sOff)) : "memory");
    v = lane == 1 ? 0u : v;
    float s0 = lo(v) * lo(v), s1 = hi(v) * hi(v);
    int so = 0;
#pragma unroll 1
    for (int c = 0; c < 8; ++c) {
      if (c < 9 - NS) waitv<(NS - 2) * NI>(); else if (NS == 4 && c == 5) waitv<NI>(); else waitv<0>();
      bar();
      if (c < 8 - NS) issue(c + NS, lds + so);
      so = so == (NS - 1) * CH ? 0 : so + CH;
      if (c < 7) {
        asm volatile("ds_read_b32 %0, %1\n s_waitcnt lgkmcnt(0)" : "=v"(v) : "v"((u32)(size_t)(LDSP char*)(lds + so + sOff)) : "memory");
        s0 += lo(v) * lo(v); s1 += hi(v) * hi(v);
      }
    }
#pragma unroll
    for (int o = 1; o < 64; o <<= 1) { s0 += __shfl_xor(s0, o); s1 += __shfl_xor(s1, o); }
    if (lane < 2) rs2[128 * mt + 8 * ng + 2 * w + lane] = 1.f / ((lane ? s1 : s0) * (1.f / 512) + EPS);
    return;
  }
  f16v acc[3][2] = {}, accx = {};
  const int pOff = wn * 12288 + lane * 16;
  int qOff[2];
#pragma unroll
  for (int b = 0; b < 2; ++b)
    qOff[b] = NT * 4096 + (4 * h + ((lane >> 2) & 3)) * 256 + (((4 * (2 * wm + (b ^ wn)) + ((lane >> 3) & 2) + ((lane >> 1) & 1)) ^ (lane & 12)) << 4) + 8 * (lane & 1);
  struct Fr { u4 P[3], Q[2], X; };
  auto rd = [&](const char* sl, int ks) {
    Fr f;
#pragma unroll
    for (int b = 0; b < 2; ++b) f.Q[b] = ldtr(sl + qOff[b] + ks * 4096, 2048);
#pragma unroll
    for (int t = 0; t < 3; ++t) f.P[t] = lds128(sl + pOff + t * 4096 + ks * 1024);
    if (LAST) f.X = lds128(sl + 24576 + ks * 1024 + lane * 16);
    return f;
  };
  auto mm = [&](const Fr& f, int ks) {
#pragma unroll
    for (int t = 0; t < 3; ++t) { acc[t][0] = mf32(f.P[t], f.Q[0], acc[t][0]); acc[t][1] = mf32(f.P[t], f.Q[1], acc[t][1]); }
    if (LAST) accx = mf32(f.X, f.Q[0], accx);
    mix<6 + LAST, 2, 1>();
    SB();
  };
  bar();
  STAMP(KI, 1);
  Fr f0 = rd(lds, 0), f1;
  int so = 0;
#pragma unroll 1
  for (int c = 0; c < 8; ++c) {
    char* sl = lds + so;
    so = so == (NS - 1) * CH ? 0 : so + CH;
    SB();
    f1 = rd(sl, 1); mm(f0, 0);
    f0 = rd(sl, 2); mm(f1, 1);
    f1 = rd(sl, 3); mm(f0, 2);
    bar();
    SB();
    f0 = rd(lds + so, 0); mm(f1, 3);
  }
  STAMP(KI, 3);
#pragma unroll
  for (int t = 0; t < 3; ++t) {
    int tile = t0 + 3 * wn + t;
    if (tile < t1)
#pragma unroll
      for (int b = 0; b < 2; ++b) {
        const f16v& a = acc[t][b];
        u4 o;
#pragma unroll
        for (int j = 0; j < 4; ++j) { int i = 8 * (j >> 1) + 2 * (j & 1); o[j] = pk(fmaxf(a[i], 0.f) * a[i + 4], fmaxf(a[i + 1], 0.f) * a[i + 5]); }
        st4(hb + (((2 * mt + wm) * 2 + (b ^ wn)) * 86 + tile) * 256 + lane * 4, o);
      }
  }
  if (LAST) {
    u32* o = x3 + ((4 * mt + w) * 16 + ng) * 512 + lane * 4;
    st4(o, pack8(accx, 0)); st4(o + 256, pack8(accx, 1));
  }
  STAMP(KI, 4); STAMPW(KI, 5);
}

template <int LAST>
__global__ __launch_bounds__(512) void k_down(const u32* __restrict__ hb, const u32* __restrict__ wd, const float* __restrict__ rs2,
                                              const u32* __restrict__ res, u32* __restrict__ out, float* __restrict__ ssp) {
  __shared__ __attribute__((aligned(1024))) char lds[131072 + 512];
  const int xc = blockIdx.x & 7, jb = blockIdx.x >> 3, rg = 8 * (xc & 3) + (jb & 7), nt = 4 * (xc >> 2) + (jb >> 3);
  const int lane = threadIdx.x & 63, w8 = __builtin_amdgcn_readfirstlane(threadIdx.x >> 6), w = w8 & 3, r = lane & 31, h = lane >> 5;
  constexpr int KI = LAST ? 6 : 3;
  STAMP(KI, 0);
  if (w8 >= 4) {
    const rsrc rs = mkr(w < 2 ? wd + (2 * nt + w) * 86 * 256 : hb + (2 * rg + w - 2) * 86 * 256);
    auto issue = [&](int c) {
      char* sl = lds + (c & 3) * 32768 + w * 8192;
      unroll<4>([&](auto I) { blds<I.v * 1024>(rs, lane * 16, c * 8192, sl); });
      unroll<2>([&](auto I) { blds<I.v * 1024>(rs, lane * 16, c * 8192 + 4096, sl + 4096); });
      if (c < 10) unroll<2>([&](auto I) { blds<I.v * 1024 + 2048>(rs, lane * 16, c * 8192 + 4096, sl + 4096); });
    };
    issue(0); issue(1); issue(2);
    waitv<16>(); bar();
    issue(3);
#pragma unroll 1
    for (int c = 0; c < 10; ++c) {
      if (c < 7) waitv<16>(); else if (c == 7) waitv<14>(); else if (c == 8) waitv<6>(); else waitv<0>();
      bar();
      if (c < 7) issue(c + 4);
    }
    if (!LAST) bar();
    return;
  }
  const int t = w >> 1, b = w & 1, rho = 64 * rg + 32 * b + r;
  u32 rv[8];
  if (LAST) {
    const u32* p = res + ((2 * rg + b) * 16 + 2 * nt + t) * 512 + lane * 4;
    u4 x = *(const u4*)p, y = *(const u4*)(p + 256);
#pragma unroll
    for (int j = 0; j < 4; ++j) { rv[j] = x[j]; rv[4 + j] = y[j]; }
  } else {
    const u16* p = (const u16*)res + (64 * nt + 32 * t + 4 * h) * (2 * PS) + rho;
#pragma unroll
    for (int j = 0; j < 8; ++j) { const u16* e = p + (8 * (j >> 1) + 2 * (j & 1)) * (2 * PS); rv[j] = (u32)e[0] | ((u32)e[2 * PS] << 16); }
  }
  const float rs = rs2[rho];
  struct Fr { u4 A[4], B[4]; };
  const int aOff = t * 8192 + lane * 16, bOff = 16384 + b * 8192 + lane * 16;
  auto rd = [&](const char* sl, auto N) {
    Fr f;
#pragma unroll
    for (int k = 0; k < N.v; ++k) { f.A[k] = lds128(sl + aOff + k * 1024); f.B[k] = lds128(sl + bOff + k * 1024); }
    return f;
  };
  f16v acc = {};
  auto mm = [&](const Fr& f, auto N) {
#pragma unroll
    for (int k = 0; k < N.v; ++k) acc = mf32(f.A[k], f.B[k], acc);
    mix<N.v, 2, 1>();
    SB();
  };
  bar();
  STAMP(KI, 1);
  Fr f0 = rd(lds, IC<4>{}), f1;
#pragma unroll 1
  for (int c = 0; c < 10; ++c) {
    SB();
    f1 = rd(lds + (c & 3) * 32768 + 4096, IC<4>{}); mm(f0, IC<4>{});
    bar();
    SB();
    f0 = rd(lds + ((c + 1) & 3) * 32768, IC<4>{}); mm(f1, IC<4>{});
  }
  f1 = rd(lds + 2 * 32768 + 4096, IC<2>{}); mm(f0, IC<4>{});
  mm(f1, IC<2>{});
  STAMP(KI, 2);
  u32 v[8];
#pragma unroll
  for (int j = 0; j < 8; ++j) v[j] = pk(lo(rv[j]) + rs * acc[2 * j], hi(rv[j]) + rs * acc[2 * j + 1]);
  if (LAST) {
    u32* o = out + (32 * (32 * b + r) + rg) * 256 + 32 * nt + 16 * t + 8 * h;
    *(u4*)o = u4{v[0], v[1], v[2], v[3]}; *(u4*)(o + 4) = u4{v[4], v[5], v[6], v[7]};
  } else {
    const int c0 = 32 * nt + 16 * t + 2 * h;
    float ss = 0.f;
#pragma unroll
    for (int j = 0; j < 8; ++j) {
      st1(out + (c0 + 4 * (j >> 1) + (j & 1)) * XI + rho, v[j]);
      u32 m = (j == 0 && c0 == 0) ? (v[j] & 0xffffu) : v[j];
      ss = dot2(m, m, ss);
    }
    ss += __shfl_xor(ss, 32);
    float* sl = (float*)(lds + 131072);
    if (h == 0) sl[w * 32 + r] = ss;
    bar();
    if (w == 0) ssp[nt * SS + 64 * rg + lane] = sl[lane] + sl[64 + lane];
  }
  STAMP(KI, 3); STAMPW(KI, 4);
}

extern "C" void kernel_launch(void* const* d_in, const int* in_sizes, int n_in, void* d_out, int out_size, void* d_ws, size_t ws_size,
                              hipStream_t stream) {
  const u16 *x = (const u16*)d_in[0], *wr = (const u16*)d_in[1], *wi = (const u16*)d_in[2];
  const u16 *wg = (const u16*)d_in[4], *wu = (const u16*)d_in[5], *wd = (const u16*)d_in[6];
  char* ws = (char*)d_ws;
  const size_t MB = 1 << 20;
  u32 *xh0 = (u32*)ws, *xh1 = (u32*)(ws + 3 * MB), *xh2 = (u32*)(ws + 6 * MB), *xh3 = (u32*)(ws + 9 * MB);
  u32 *filt = (u32*)(ws + 12 * MB), *x3 = (u32*)(ws + 15 * MB), *wgu = (u32*)(ws + 17 * MB), *wd1 = (u32*)(ws + 20 * MB);
  u32 *wd2 = (u32*)(ws + 22 * MB), *hb = (u32*)(ws + 24 * MB), *hb2 = (u32*)(ws + 30 * MB);
  float *rstd = (float*)(ws + 36 * MB), *ssp = rstd + 4096, *rs2 = rstd + 32768, *rs3 = rstd + 65536;
  k_prep<<<260, 512, 0, stream>>>(x, wr, wi, wg, wu, wd, xh0, (u32*)rstd, wgu, wd1, wd2, filt);
  k_fft<1><<<256, 256, 0, stream>>>(xh0, rstd, filt, xh1);
  k_up<0><<<256, 512, 0, stream>>>(xh1, wgu, hb, x3, rs2);
  k_down<0><<<256, 512, 0, stream>>>(hb, wd1, rs2, xh1, xh2, ssp);
  k_fft<8><<<256, 256, 0, stream>>>(xh2, ssp, filt, xh3);
  k_up<1><<<256, 512, 0, stream>>>(xh3, wgu, hb2, x3, rs3);
  k_down<1><<<256, 512, 0, stream>>>(hb2, wd2, rs3, x3, (u32*)d_out, ssp);
}
```

```cpp
#include <hip/hip_runtime.h>
#include <hip/hip_bf16.h>

typedef unsigned u32;
typedef unsigned short u16;
typedef __attribute__((ext_vector_type(8))) __bf16 bh16x8;
typedef __attribute__((ext_vector_type(2))) __bf16 bf2;
typedef __attribute__((ext_vector_type(2))) float f2;
typedef __attribute__((ext_vector_type(4))) float f4;
typedef __attribute__((ext_vector_type(16))) float f16v;
typedef __attribute__((ext_vector_type(2))) u32 u2;
typedef __attribute__((ext_vector_type(4))) u32 u4;
typedef __attribute__((ext_vector_type(4))) short s4;
typedef u4 __attribute__((aligned(2))) u4_ua;
#define DI __device__ __forceinline__
#define LDSP __attribute__((address_space(3)))
#define GLBP __attribute__((address_space(1)))

#define ROT 0
#define UPPRE 2
constexpr int SS = 2048;
constexpr int XI = 2112, PS = 1088, FS = 2080;
constexpr float EPS = 1e-5f;

struct Part { u16 v[16384]; };
#if defined(__HIP_DEVICE_COMPILE__)
constexpr double kPi = 3.14159265358979323846;
constexpr double csin(double x) { double t = x, s = x; for (int i = 1; i < 14; ++i) { t *= -x * x / ((2 * i) * (2 * i + 1)); s += t; } return s; }
constexpr u16 tobf(double x) { float f = (float)x; u32 u = __builtin_bit_cast(u32, f); u += 0x7fffu + ((u >> 16) & 1u); return (u16)(u >> 16); }
struct CosT { u16 v[512]; };
constexpr CosT mkcos() {
  CosT c{};
  for (int i = 0; i < 512; ++i) {
    int q = (i + 128) & 511;
    int y = q <= 128 ? q : (q <= 384 ? 256 - q : q - 512);
    c.v[i] = y == 0 ? (u16)0 : tobf(csin(2 * kPi * y / 512) / 16.0);
  }
  return c;
}
constexpr CosT kCos = mkcos();
constexpr u16 kDC = tobf(0.04419417382415922);
constexpr Part mkR(int pi) {
  Part p{};
  for (int e = 0; e < 16384; ++e) {
    int r = (e >> 3) & 31, h = (e >> 8) & 1, j = e & 7, d = 0, s = 0;
    if (pi < 4) { d = (((e >> 9) & 7) << 4) + 8 * h + j; s = 32 * (4 * pi + (e >> 12)) + r; }
    else { d = 32 * (pi - 4) + (r & 3) + 4 * (r >> 3) + 16 * ((r >> 2) & 1); s = ((e >> 9) << 4) + 8 * (j >> 2) + 4 * h + (j & 3); }
    p.v[e] = s >= 2 ? kCos.v[((s >> 1) * d + ((s & 1) << 7)) & 511] : (u16)(kDC | ((s == 0 && (d & 1)) ? 0x8000 : 0));
  }
  return p;
}
template <int I> struct PR { static constexpr Part v = mkR(I); };
struct Tab { Part p[20]; };
__device__ const __attribute__((aligned(1024))) Tab R1T = {{PR<0>::v, PR<1>::v, PR<2>::v, PR<3>::v, PR<4>::v, PR<5>::v, PR<6>::v, PR<7>::v,
  PR<8>::v, PR<9>::v, PR<10>::v, PR<11>::v, PR<12>::v, PR<13>::v, PR<14>::v, PR<15>::v, PR<16>::v, PR<17>::v, PR<18>::v, PR<19>::v}};
#else
struct Tab { u16 p[8]; };
__device__ const Tab R1T = {};
#endif

DI f16v mf32(u4 a, u4 b, f16v c) { return __builtin_amdgcn_mfma_f32_32x32x16_bf16(__builtin_bit_cast(bh16x8, a), __builtin_bit_cast(bh16x8, b), c, 0, 0, 0); }
DI f4 mf16(u4 a, u4 b, f4 c) { return __builtin_amdgcn_mfma_f32_16x16x32_bf16(__builtin_bit_cast(bh16x8, a), __builtin_bit_cast(bh16x8, b), c, 0, 0, 0); }
DI u32 pk(float a, float b) { f2 v = {a, b}; return __builtin_bit_cast(u32, __builtin_convertvector(v, bf2)); }
DI float lo(u32 u) { return __builtin_bit_cast(float, u << 16); }
DI float hi(u32 u) { return __builtin_bit_cast(float, u & 0xffff0000u); }
DI float dot2(u32 a, u32 b, float c) { return __builtin_amdgcn_fdot2_f32_bf16(__builtin_bit_cast(bf2, a), __builtin_bit_cast(bf2, b), c, false); }
DI void glds(const void* g, char* l) { __builtin_amdgcn_global_load_lds((const GLBP void*)g, (LDSP void*)l, 16, 0, 0); }
typedef __amdgpu_buffer_rsrc_t rsrc;
DI rsrc mkr(const void* p) { return __builtin_amdgcn_make_buffer_rsrc((void*)p, 0, 0x7fffffff, 0x00020000); }
template <int O = 0> DI void blds(rsrc r, int vo, int so, char* l) { __builtin_amdgcn_raw_ptr_buffer_load_lds(r, (LDSP void*)l, 16, vo, so, O, 0); }
DI u32 selfpf(int nb) {
  const rsrc r = __builtin_amdgcn_make_buffer_rsrc((void*)(__builtin_amdgcn_s_getpc() & ~127ll), 0, nb, 0x00020000);
  return __builtin_amdgcn_raw_buffer_load_b32(r, (threadIdx.x & 255) * 128, 0, 0);
}
DI void keep(u32 v) { asm volatile("" ::"v"(v)); }
DI u4 ldtr(const char* a, int d) {
  u2 x = __builtin_bit_cast(u2, __builtin_amdgcn_ds_read_tr16_b64_v4i16((LDSP s4*)a));
  u2 y = __builtin_bit_cast(u2, __builtin_amdgcn_ds_read_tr16_b64_v4i16((LDSP s4*)(a + d)));
  return u4{x[0], x[1], y[0], y[1]};
}
#define WT 2
DI void st4(u32* p, u4 v) {
  if (WT == 2) asm volatile("global_store_dwordx4 %0, %1, off sc0 sc1\n s_nop 1" ::"v"(p), "v"(v) : "memory");
  else if (WT == 1) __builtin_nontemporal_store(v, (u4*)p);
  else *(u4*)p = v;
}
DI void st2(u32* p, u2 v) {
  if (WT == 2) asm volatile("global_store_dwordx2 %0, %1, off sc0 sc1" ::"v"(p), "v"(v) : "memory");
  else if (WT == 1) __builtin_nontemporal_store(v, (u2*)p);
  else *(u2*)p = v;
}
DI void st1(u32* p, u32 v) {
  if (WT == 2) asm volatile("global_store_dword %0, %1, off sc0 sc1" ::"v"(p), "v"(v) : "memory");
  else if (WT == 1) __builtin_nontemporal_store(v, p);
  else *p = v;
}
DI u4 lds128(const char* p) { return *(const u4*)p; }
DI u4 pack8(const f16v& a, int s) { u4 o = {pk(a[8 * s], a[8 * s + 1]), pk(a[8 * s + 2], a[8 * s + 3]), pk(a[8 * s + 4], a[8 * s + 5]), pk(a[8 * s + 6], a[8 * s + 7])}; return o; }
template <int N> DI void waitv() { asm volatile("s_waitcnt vmcnt(%0)" ::"n"(N) : "memory"); }
DI void bar() { asm volatile("s_waitcnt lgkmcnt(0)" ::: "memory"); __builtin_amdgcn_s_barrier(); asm volatile("" ::: "memory"); }
DI int dperm(int m) { return (m & 3) + 4 * (m >> 3) + 16 * ((m >> 2) & 1); }
DI int fperm(int m) { int i = (m & 3) + 4 * (m >> 3); return 16 * (i >> 3) + 8 * ((m >> 2) & 1) + (i & 7); }
#define SB() __builtin_amdgcn_sched_barrier(0)
#define SGB(m, n) __builtin_amdgcn_sched_group_barrier(m, n, 0)
template <int NM, int ND, int NV> DI void mix() { if constexpr (NM > 0) { SGB(0x008, 1); SGB(0x100, ND); SGB(0x002, NV); mix<NM - 1, ND, NV>(); } }
template <int I> struct IC { static constexpr int v = I; };
template <int N, class F> DI void unroll(F&& f) { if constexpr (N > 0) { unroll<N - 1>(f); f(IC<N - 1>{}); } }

template <int JOB>
DI void prep_gemm(const u16* __restrict__ q0, const u16* __restrict__ q1, int qt, int nt, int c0, u32 pf, char* lds,
                  u32* __restrict__ o0, u32* __restrict__ o1) {
  const int lane = threadIdx.x & 63, w8 = __builtin_amdgcn_readfirstlane(threadIdx.x >> 6), w = w8 & 3;
  const int wp = w >> 1, wq = w & 1, r = lane & 31, h = lane >> 5;
  if (w8 >= 4) {
    const rsrc rp = mkr(&R1T), ra = mkr(q0), rb = mkr(q1);
    int qv[4];
#pragma unroll
    for (int i = 0; i < 4; ++i) {
      if (JOB == 1) {
        int d = 16 * w + 8 * (i >> 1) + (lane >> 3), pos = lane & 7;
        int f = 64 * qt + 16 * ((pos >> 1) ^ (d & 3) ^ (i & 1)) + 8 * (pos & 1);
        qv[i] = (d * 1376 + (f > 1368 ? 1368 : f)) * 2;
      } else {
        int u = 32 * w + 8 * i + (lane >> 3), row;
        if (JOB == 0) { int rho = 128 * qt + (u & 64) + 2 * (u & 31) + ((u >> 5) & 1); row = 32 * (rho & 63) + (rho >> 6); }
        else { row = 128 * qt + (u & 96) + fperm(u & 31); row = row > 1375 ? 1375 : row; }
        qv[i] = row * 1024 + (((lane & 7) ^ ((u >> 1) & 7)) << 4);
      }
    }
    constexpr int QSTEP = JOB == 1 ? 64 * 1376 * 2 : 128;
    auto issP = [&](int c) { unroll<4>([&](auto I) { blds<I.v * 1024>(rp, lane * 16, (4 * nt + w) * 8192 + c * 4096, lds + w * 8192 + c * 4096); }); };
    auto issue = [&](int cc) {
      const int c = (cc + c0) & 7;
#pragma unroll
      for (int i = 0; i < 4; ++i) blds((JOB == 1 && (i & 1)) ? rb : ra, qv[i], c * QSTEP, lds + 32768 + c * 16384 + (4 * w + i) * 1024);
    };
    issP(0); issP(1); issue(0); issue(1); issue(2);
    waitv<8>(); bar();
    keep(pf);
#pragma unroll 1
    for (int c = 3; c < 8; ++c) issue(c);
    unroll<8>([&](auto I) { waitv<(I.v < 6 ? 4 * (6 - I.v) : 0)>(); bar(); });
    if (JOB == 0) {
      const int u = 32 * nt + 8 * w + (lane >> 3);
      float s1 = 0.f, s2 = 0.f;
#pragma unroll
      for (int c = 0; c < 8; ++c) {
        u4 v = lds128(lds + 32768 + c * 16384 + u * 128 + (lane & 7) * 16);
#pragma unroll
        for (int j = 0; j < 4; ++j) { s1 = dot2(v[j], 0x3f803f80u, s1); s2 = dot2(v[j], v[j], s2); }
      }
#pragma unroll
      for (int o = 1; o < 8; o <<= 1) { s1 += __shfl_xor(s1, o); s2 += __shfl_xor(s2, o); }
      float m = s1 * (1.f / 512), var = fmaxf(s2 * (1.f / 512) - m * m, 0.f);
      if ((lane & 7) == 0) ((float*)o1)[128 * qt + (u & 64) + 2 * (u & 31) + ((u >> 5) & 1)] = __builtin_amdgcn_rsqf(var + EPS) * (1.f / 2048);
    }
    return;
  }
  f16v acc[2][2] = {};
  int qOff;
  if (JOB == 1) {
    int g = lane >> 4, qr = (lane >> 2) & 3, run = 4 * (g & 1) + (lane & 3), q = run >> 1;
    qOff = 32768 + (g >> 1) * 2048 + qr * 128 + (q & 1) * 1024 + (8 * (run & 1) + 4 * (q >> 1)) * 2;
  } else qOff = 32768 + (64 * wq + r) * 128;
  struct Fr { u4 v[2]; };
  int pOff; u32 sgn;
  auto turn = [&](int c) {
    int n = ((r >> 1) * (c >> 1)) & 3;
    pOff = wp * 16384 + (c & 1) * 4096 + (lane ^ (n & 1)) * 16;
    sgn = (((r & 1) ? n + 1 : n) & 2) ? 0x80008000u : 0u;
  };
  auto rdP = [&](int ks) { return Fr{{lds128(lds + pOff + ks * 1024), lds128(lds + pOff + 8192 + ks * 1024)}}; };
  auto rdQ = [&](const char* sl, int ks) {
    Fr f;
#pragma unroll
    for (int b = 0; b < 2; ++b) {
      if (JOB == 1) {
        f.v[b] = ldtr(sl + qOff + ks * 4096 + (((2 * wq + b) ^ ((lane >> 2) & 3) ^ ((lane >> 1) & 1)) << 5), 512);
      } else f.v[b] = lds128(sl + qOff + b * 4096 + (((2 * ks + h) ^ ((r >> 1) & 7)) << 4));
    }
    return f;
  };
  auto mm = [&](const Fr& P, const Fr& Q, Fr& N, u32 sg, int c, int ks) {
#pragma unroll
    for (int t = 0; t < 2; ++t)
#pragma unroll
      for (int b = 0; b < 2; ++b) acc[t][b] = JOB == 2 ? mf32(Q.v[b], P.v[t], acc[t][b]) : mf32(P.v[t], Q.v[b], acc[t][b]);
    N.v[0] ^= sg; N.v[1] ^= sg;
    SGB(0x008, 1); SGB(0x100, 3); SGB(0x008, 1); SGB(0x100, 3); SGB(0x002, 4); SGB(0x008, 1); SGB(0x002, 4); SGB(0x008, 1);
    asm volatile("" : "+v"(N.v[0]), "+v"(N.v[1]));
    SB();
  };
  turn(c0);
  bar();
  Fr qa = rdQ(lds + c0 * 16384, 0), pa = rdP(0), pb = rdP(1), qb, pc, pd;
  pa.v[0] ^= sgn; pa.v[1] ^= sgn;
#pragma unroll 1
  for (int cc = 0; cc < 8; ++cc) {
    const int c = (cc + c0) & 7;
    const char* sl = lds + c * 16384;
    const u32 sg = sgn;
    SB();
    qb = rdQ(sl, 1); pc = rdP(2); mm(pa, qa, pb, sg, c, 0);
    qa = rdQ(sl, 2); pd = rdP(3); mm(pb, qb, pc, sg, c, 1);
    turn((c + 1) & 7);
    SB();
    qb = rdQ(sl, 3); pa = rdP(0); mm(pc, qa, pd, sg, c, 2);
    asm volatile("" ::: "memory"); __builtin_amdgcn_s_barrier(); asm volatile("" ::: "memory");
    SB();
    qa = rdQ(lds + ((c + 1) & 7) * 16384, 0); pb = rdP(1); mm(pd, qb, pa, sgn, c, 3);
  }
  if (JOB == 0) {
#pragma unroll
    for (int t = 0; t < 2; ++t)
#pragma unroll
      for (int i = 0; i < 16; i += 2) {
        int col = 64 * nt + 32 * wp + 16 * t + (((i & 3) + 8 * (i >> 2) + 4 * h) >> 1);
        u2 v = {pk(acc[t][0][i], acc[t][0][i + 1]), pk(acc[t][1][i], acc[t][1][i + 1])};
        st2(o0 + col * XI + 128 * qt + 64 * wq + 2 * r, v);
      }
  } else if (JOB == 1) {
#pragma unroll
    for (int b = 0; b < 2; ++b) {
      int tile = 4 * qt + 2 * wq + b;
      if (tile < 86)
#pragma unroll
        for (int t = 0; t < 2; ++t)
#pragma unroll
          for (int s = 0; s < 2; ++s) {
            int ks = 8 * nt + 4 * wp + 2 * t + s;
            u4 v = pack8(acc[t][b], s);
            if (ks == 0 && h == 0) v[0] &= 0xffffu;
            st4(o0 + (tile * 32 + ks) * 256 + lane * 4, v);
          }
    }
  } else {
#pragma unroll
    for (int b = 0; b < 2; ++b)
#pragma unroll
      for (int s = 0; s < 2; ++s) {
        int ks = 8 * qt + 4 * wq + 2 * b + s;
        if (ks < 86)
#pragma unroll
          for (int t = 0; t < 2; ++t) {
            int o = ((4 * nt + 2 * wp + t) * 86 + ks) * 256 + lane * 4;
            st4(o0 + o, pack8(acc[t][b], s));
          }
      }
  }
}

__global__ __launch_bounds__(512) void k_prep(const u16* __restrict__ x, const u16* __restrict__ wr, const u16* __restrict__ wi,
    const u16* __restrict__ wg, const u16* __restrict__ wu, const u16* __restrict__ wd,
    u32* __restrict__ xh, u32* __restrict__ rstd, u32* __restrict__ wgu, u32* __restrict__ wd1, u32* __restrict__ wd2,
    u32* __restrict__ filt) {
  __shared__ __attribute__((aligned(1024))) char lds[163840];
  const int b = blockIdx.x, nt = (b >> 3) & 3, q = 8 * (b >> 5) + (b & 7), c0 = ROT;
  const int lane = threadIdx.x & 63, w = __builtin_amdgcn_readfirstlane(threadIdx.x >> 6);
  u32 pf = 0;
  if (w >= 4) pf = selfpf(12288);
  if (b >= 260) {
    const int v = b - 260;
#pragma unroll
    for (int i = 0; i < 4; ++i) blds(mkr(wd), lane * 16, (32 * v + 4 * w + i) * 1024, lds + (4 * w + i) * 1088);
    waitv<0>(); bar();
    keep(pf);
    const int e = lane & 3, a0 = (8 * (lane >> 5) + ((lane >> 2) & 3)) * 1088 + (8 * ((lane >> 4) & 1) + 4 * (e >> 1) + 16 * (e & 1)) * 2;
#pragma unroll
    for (int i = 0; i < 4; ++i) {
      const int dt = 2 * w + (i >> 1), ks = i & 1;
      st4(wd2 + (dt * 86 + 2 * v + ks) * 256 + lane * 4, ldtr(lds + a0 + ks * 16 * 1088 + dt * 64, 4 * 1088));
    }
  } else if (q < 16) prep_gemm<0>(x, x, q, nt, c0, pf, lds, xh, rstd);
  else if (q < 38) prep_gemm<1>(wg, wu, q - 16, nt, 0, pf, lds, wgu, wgu);
  else if (q < 49) prep_gemm<2>(wd, wd, q - 38, nt, c0, pf, lds, wd1, wd1);
  else {
    const int u = b < 256 ? (q - 49) * 4 + nt : b - 196, k0 = 16 * (u >> 4) + 8 * (u & 1) + 256 * ((u >> 1) & 7);
    const rsrc rs = mkr(w < 4 ? wr : wi);
    const int so = (k0 + 64 * (w & 3)) * 514;
    unroll<4>([&](auto I) { blds<I.v * 1024>(rs, lane * 16, so, lds + w * 4160); });
    if (lane == 0) blds(rs, 0, so + 4096, lds + w * 4160 + 4096);
    blds(mkr((const char*)&R1T + 131072), lane * 16, (u * 8 + w) * 1024, lds + 40960 + w * 1024);
    if (w < 6) {
      u32 lo_, hi_; int nb;
#define PCOF(sym, n) { asm volatile("s_getpc_b64 s[40:41]\n s_add_u32 %0, s40, " sym "@rel32@lo+4\n s_addc_u32 %1, s41, " sym "@rel32@hi+12" : "=s"(lo_), "=s"(hi_) :: "scc", "s40", "s41"); nb = n; }
      if (w == 0) PCOF("_Z5k_fftILi1EEvPKjPKfS1_Pj", 3584)
      else if (w == 1) PCOF("_Z4k_upILi0EEvPKjS1_PjS2_Pf", 4352)
      else if (w == 2) PCOF("_Z6k_downILi0EEvPKjS1_PKfS1_PjPf", 2688)
      else if (w == 3) PCOF("_Z5k_fftILi8EEvPKjPKfS1_Pj", 4224)
      else if (w == 4) PCOF("_Z4k_upILi1EEvPKjS1_PjS2_Pf", 4608)
      else PCOF("_Z6k_downILi1EEvPKjS1_PKfS1_PjPf", 1920)
      const rsrc rc = __builtin_amdgcn_make_buffer_rsrc((void*)(((unsigned long long)hi_ << 32) | lo_), 0, nb, 0x00020000);
      unroll<4>([&](auto I) { blds<I.v * 1024>(rc, lane * 16, 0, lds + 65536 + w * 8192); });
      blds(rc, lane * 16 + 4096, 0, lds + 65536 + w * 8192 + 4096);
    }
    waitv<0>(); bar();
    keep(pf);
#pragma unroll
    for (int it = 0; it < 4; ++it) {
      const int cs = 1 + 32 * w + 8 * it + (lane >> 3);
      const u16* p = (const u16*)(lds + (lane & 7) * 514) + cs;
      u4 v;
#pragma unroll
      for (int j = 0; j < 4; ++j) v[j] = (u32)p[j * 2080] | ((u32)p[(4 + j) * 2080] << 16);
      st4(filt + (cs & 255) * FS + (u >> 3) * 256 + (((u >> 1) & 3) * 16 + 8 * (u & 1) + (lane & 7)) * 4, v);
    }
  }
}

DI u4 rot(u4 v) { u4 o; for (int j = 0; j < 4; ++j) o[j] = __builtin_amdgcn_alignbit(v[j], v[j], 16) ^ 0x8000u; return o; }
DI u4 pk4(f4 a, f4 b) { return u4{pk(a[0], b[0]), pk(a[1], b[1]), pk(a[2], b[2]), pk(a[3], b[3])}; }
DI f4 lo4(u4 v) { return __builtin_bit_cast(f4, v << 16); }
DI f4 hi4(u4 v) { return __builtin_bit_cast(f4, v & 0xffff0000u); }
template <int CJ> DI u4 cmulc(f4 re, f4 im, f4 c, f4 s) {
  u4 o;
#pragma unroll
  for (int q = 0; q < 4; q += 2) {
    const f2 r = {re[q], re[q + 1]}, i = {im[q], im[q + 1]}, cc = {c[q], c[q + 1]}, ss = {s[q], s[q + 1]};
    const f2 rc = r * cc, rs = r * ss;
    const f2 x = __builtin_elementwise_fma(CJ ? -i : i, ss, rc), y = __builtin_elementwise_fma(CJ ? -i : i, cc, -rs);
    o[q] = pk(x[0], y[0]); o[q + 1] = pk(x[1], y[1]);
  }
  return o;
}
template <int NP>
__global__ __launch_bounds__(256, 2) void k_fft(const u32* __restrict__ xin, const float* __restrict__ st, const u32* __restrict__ filt,
                                                u32* __restrict__ xout) {
  __shared__ __attribute__((aligned(256))) char lds[16384];
  const int col = blockIdx.x, tid = threadIdx.x, lane = tid & 63, w = tid >> 6, l15 = lane & 15, g = lane >> 4;
  constexpr int KI = NP == 1 ? 1 : 4;
  const u32* xc = xin + col * XI;
  u4 xd[2], xr[2], fw[2];
  f4 sv[NP][2];
#pragma unroll
  for (int i = 0; i < 2; ++i) xd[i] = *(const u4*)(xc + 1024 * i + 4 * tid);
#pragma unroll
  for (int p = 0; p < NP; ++p)
#pragma unroll
    for (int i = 0; i < 2; ++i) sv[p][i] = *(const f4*)(st + p * SS + 1024 * i + 4 * tid);
#pragma unroll
  for (int i = 0; i < 2; ++i) {
    fw[i] = *(const u4*)(filt + col * FS + (2 * w + i) * 256 + lane * 4);
    xr[i] = *(const u4*)(xc + 64 * (16 * i + l15) + 16 * w + 4 * g);
  }
  if (NP == 1) SB();
  const int k1 = 16 * w + l15;
  const float fk1 = (float)k1, fl = (float)l15, fa = (float)(4 * g);
  u4 F64r[4], F64i[4], F32r[2][2], F32i[2][2];
  f4 t1c[2], t1s[2], t2c[2], t2s[2];
#pragma unroll
  for (int j = 0; j < 4; ++j) {
    float a = (fa + j) * fk1 * (1.f / 64), b = (fa + j) * fl * (1.f / 32);
    F64r[0][j] = pk(__builtin_amdgcn_cosf(a), __builtin_amdgcn_sinf(a));
    F32r[0][0][j] = pk(__builtin_amdgcn_cosf(b), __builtin_amdgcn_sinf(b));
  }
  F64i[0] = rot(F64r[0]); F32i[0][0] = rot(F32r[0][0]);
  const u32 NEG = 0x80008000u, so = (lane & 1) ? NEG : 0u, sr = (lane & 2) ? NEG : 0u, si = ((lane + 1) & 2) ? NEG : 0u;
#pragma unroll
  for (int j = 0; j < 4; ++j) {
    F64r[1][j] = ((lane & 1) ? F64i[0][j] : F64r[0][j]) ^ sr;
    F64i[1][j] = ((lane & 1) ? F64r[0][j] : F64i[0][j]) ^ si;
    F64r[2][j] = F64r[0][j] ^ so; F64i[2][j] = F64i[0][j] ^ so;
    F64r[3][j] = F64r[1][j] ^ so; F64i[3][j] = F64i[1][j] ^ so;
    const u32 sj = (j & 1) ? NEG : 0u;
    F32r[0][1][j] = F32r[0][0][j] ^ so; F32i[0][1][j] = F32i[0][0][j] ^ so;
    F32r[1][0][j] = F32r[0][0][j] ^ sj; F32i[1][0][j] = F32i[0][0][j] ^ sj;
    F32r[1][1][j] = F32r[0][1][j] ^ sj; F32i[1][1][j] = F32i[0][1][j] ^ sj;
  }
#pragma unroll
  for (int t = 0; t < 2; ++t)
#pragma unroll
    for (int j = 0; j < 4; ++j) {
      float a = (fa + (16 * t + j)) * (fk1 * (1.f / 2048)), b = ((fa + (float)(16 * w)) + j) * ((fl + 16 * t) * (1.f / 2048));
      t1c[t][j] = __builtin_amdgcn_cosf(a); t1s[t][j] = __builtin_amdgcn_sinf(a);
      t2c[t][j] = __builtin_amdgcn_cosf(b); t2s[t][j] = __builtin_amdgcn_sinf(b);
    }
  if (NP > 1) unroll<22>([&](auto) { SGB(0x020, 1); SGB(0x402, 11); });
  SB();
  const u32 msk = col == 0 ? 0xffffu : 0xffffffffu;
#pragma unroll
  for (int i = 0; i < 2; ++i) {
    f4 rs = sv[0][i];
    if (NP > 1) {
#pragma unroll
      for (int p = 1; p < NP; ++p) rs += sv[p][i];
#pragma unroll
      for (int j = 0; j < 4; ++j) rs[j] = __builtin_amdgcn_rsqf(rs[j] * (1.f / 512) + EPS) * (1.f / 2048);
    }
    const u4 z = pk4(lo4(xd[i]) * rs, hi4(xd[i]) * rs) & msk;
    *(u4*)(lds + (16 * i + (tid >> 4)) * 256 + ((l15 ^ (tid >> 4)) << 4)) = z;
  }
  __syncthreads();
  u4 z[2][4], T[2], Y[2];
  f4 ar[2], ai[2];
  const f4 Z4 = {0, 0, 0, 0};
#pragma unroll
  for (int s = 0; s < 4; ++s)
#pragma unroll
    for (int t = 0; t < 2; ++t) z[t][s] = lds128(lds + (16 * t + l15) * 256 + (((4 * s + g) ^ l15) << 4));
  SB();
#pragma unroll
  for (int t = 0; t < 2; ++t)
#pragma unroll
    for (int s = 0; s < 4; ++s) { ar[t] = mf16(z[t][s], F64r[s], s ? ar[t] : Z4); ai[t] = mf16(z[t][s], F64i[s], s ? ai[t] : Z4); }
#pragma unroll
  for (int t = 0; t < 2; ++t) T[t] = cmulc<0>(ar[t], ai[t], t1c[t], t1s[t]);
#pragma unroll
  for (int m = 0; m < 2; ++m)
#pragma unroll
    for (int t = 0; t < 2; ++t) { ar[m] = mf16(F32r[m][t], T[t], t ? ar[m] : Z4); ai[m] = mf16(F32i[m][t], T[t], t ? ai[m] : Z4); }
#pragma unroll
  for (int m = 0; m < 2; ++m) Y[m] = cmulc<1>(ar[m], ai[m], lo4(fw[m]), hi4(fw[m]));
#pragma unroll
  for (int t = 0; t < 2; ++t)
#pragma unroll
    for (int m = 0; m < 2; ++m) { ar[t] = mf16(Y[m], F32r[t][m], m ? ar[t] : Z4); ai[t] = mf16(Y[m], F32i[t][m], m ? ai[t] : Z4); }
#pragma unroll
  for (int t = 0; t < 2; ++t) {
    const u4 v = cmulc<0>(ar[t], ai[t], t2c[t], t2s[t]);
    *(u4*)(lds + 8192 + (16 * t + l15) * 256 + (((4 * w + g) ^ l15) << 4)) = v;
  }
  __syncthreads();
#pragma unroll
  for (int s = 0; s < 4; ++s)
#pragma unroll
    for (int t = 0; t < 2; ++t) z[t][s] = lds128(lds + 8192 + (16 * t + l15) * 256 + (((4 * s + g) ^ l15) << 4));
  SB();
#pragma unroll
  for (int t = 0; t < 2; ++t)
#pragma unroll
    for (int s = 0; s < 4; ++s) { ar[t] = mf16(F64r[s], z[t][s], s ? ar[t] : Z4); ai[t] = mf16(F64i[s], z[t][s], s ? ai[t] : Z4); }
  const u32 im0 = col == 0 ? 0u : 0xffffffffu;
#pragma unroll
  for (int t = 0; t < 2; ++t) {
    const f4 re = lo4(xr[t]) + ar[t], im = hi4(xr[t]) - __builtin_bit_cast(f4, __builtin_bit_cast(u4, ai[t]) & im0);
    u32* o = xout + col * (2 * PS) + 32 * (16 * t + l15) + 8 * w + 2 * g;
    *(u2*)o = u2{pk(re[0], re[1]), pk(re[2], re[3])};
    *(u2*)(o + PS) = u2{pk(im[0], im[1]), pk(im[2], im[3])};
  }
}

template <int LAST>
__global__ __launch_bounds__(512) void k_up(const u32* __restrict__ xh, const u32* __restrict__ wgu,
                                               u32* __restrict__ hb, u32* __restrict__ x3, float* __restrict__ rs2) {
  constexpr int NT = 6 + LAST, CH = NT * 4096 + 16384, NS = LAST ? 3 : 4, NI = NT + 4, PRE = UPPRE;
  __shared__ __attribute__((aligned(1024))) char lds[NS * CH];
  const int xc = blockIdx.x & 7, jb = blockIdx.x >> 3, mt = 8 * (xc & 1) + (jb & 7), ng = 4 * (xc >> 1) + (jb >> 3);
  const int t0 = (ng * 43) >> 3, t1 = ((ng + 1) * 43) >> 3;
  const int lane = threadIdx.x & 63, w8 = __builtin_amdgcn_readfirstlane(threadIdx.x >> 6), w = w8 & 3;
  const int wm = w >> 1, wn = w & 1, r = lane & 31, h = lane >> 5;
  constexpr int KI = LAST ? 5 : 2;
  if (w8 >= 4) {
    const rsrc rq = mkr(xh), rp = mkr(wgu), rr = mkr((const char*)&R1T + 131072);
    const int vq = (lane >> 4) * (PS * 4) + (((lane & 15) ^ (4 * (lane >> 4))) << 4);
    auto issue = [&](int c, char* sl) {
#pragma unroll
      for (int i = 0; i < 4; ++i) blds(rq, vq, (64 * c + 16 * w + 4 * i) * (PS * 4) + 256 * mt, sl + NT * 4096 + (4 * w + i) * 1024);
#pragma unroll
      for (int i = 0; i < 3; ++i) {
        int e = 6 * w + 2 * i, so = (t0 + (e >> 2)) * 32768 + (4 * c + (e & 3)) * 1024;
        blds(rp, lane * 16, so, sl + e * 1024); blds<1024>(rp, lane * 16, so, sl + e * 1024);
      }
      if (LAST) blds(rr, lane * 16, (ng * 32 + 4 * c + w) * 1024, sl + (24 + w) * 1024);
    };
#pragma unroll
    for (int c = 0; c < PRE; ++c) issue(c, lds + c * CH);
    waitv<(PRE - 1) * NI>(); bar();
#pragma unroll
    for (int c = PRE; c < NS; ++c) issue(c, lds + c * CH);
    const int sOff = NT * 4096 + lane * 256 + ((ng ^ (4 * (lane & 3))) << 4) + 4 * w;
    u32 v;
    asm volatile("ds_read_b32 %0, %1\n s_waitcnt lgkmcnt(0)" : "=v"(v) : "v"((u32)(size_t)(LDSP char*)(lds + sOff)) : "memory");
    v = lane == 1 ? 0u : v;
    float s0 = lo(v) * lo(v), s1 = hi(v) * hi(v);
    int so = 0;
#pragma unroll 1
    for (int c = 0; c < 8; ++c) {
      if (c < 9 - NS) waitv<(NS - 2) * NI>(); else if (NS == 4 && c == 5) waitv<NI>(); else waitv<0>();
      bar();
      if (c < 8 - NS) issue(c + NS, lds + so);
      so = so == (NS - 1) * CH ? 0 : so + CH;
      if (c < 7) {
        asm volatile("ds_read_b32 %0, %1\n s_waitcnt lgkmcnt(0)" : "=v"(v) : "v"((u32)(size_t)(LDSP char*)(lds + so + sOff)) : "memory");
        s0 += lo(v) * lo(v); s1 += hi(v) * hi(v);
      }
    }
#pragma unroll
    for (int o = 1; o < 64; o <<= 1) { s0 += __shfl_xor(s0, o); s1 += __shfl_xor(s1, o); }
    if (lane < 2) rs2[128 * mt + 8 * ng + 2 * w + lane] = 1.f / ((lane ? s1 : s0) * (1.f / 512) + EPS);
    return;
  }
  f16v acc[3][2] = {}, accx = {};
  const int pOff = wn * 12288 + lane * 16;
  int qOff[2];
#pragma unroll
  for (int b = 0; b < 2; ++b)
    qOff[b] = NT * 4096 + (4 * h + ((lane >> 2) & 3)) * 256 + (((4 * (2 * wm + (b ^ wn)) + ((lane >> 3) & 2) + ((lane >> 1) & 1)) ^ (lane & 12)) << 4) + 8 * (lane & 1);
  struct Fr { u4 P[3], Q[2], X; };
  auto rd = [&](const char* sl, int ks) {
    Fr f;
#pragma unroll
    for (int b = 0; b < 2; ++b) f.Q[b] = ldtr(sl + qOff[b] + ks * 4096, 2048);
#pragma unroll
    for (int t = 0; t < 3; ++t) f.P[t] = lds128(sl + pOff + t * 4096 + ks * 1024);
    if (LAST) f.X = lds128(sl + 24576 + ks * 1024 + lane * 16);
    return f;
  };
  auto mm = [&](const Fr& f, int ks) {
#pragma unroll
    for (int t = 0; t < 3; ++t) { acc[t][0] = mf32(f.P[t], f.Q[0], acc[t][0]); acc[t][1] = mf32(f.P[t], f.Q[1], acc[t][1]); }
    if (LAST) accx = mf32(f.X, f.Q[0], accx);
    mix<6 + LAST, 2, 1>();
    SB();
  };
  bar();
  Fr f0 = rd(lds, 0), f1;
  int so = 0;
#pragma unroll 1
  for (int c = 0; c < 8; ++c) {
    char* sl = lds + so;
    so = so == (NS - 1) * CH ? 0 : so + CH;
    SB();
    f1 = rd(sl, 1); mm(f0, 0);
    f0 = rd(sl, 2); mm(f1, 1);
    f1 = rd(sl, 3); mm(f0, 2);
    bar();
    SB();
    f0 = rd(lds + so, 0); mm(f1, 3);
  }
#pragma unroll
  for (int t = 0; t < 3; ++t) {
    int tile = t0 + 3 * wn + t;
    if (tile < t1)
#pragma unroll
      for (int b = 0; b < 2; ++b) {
        const f16v& a = acc[t][b];
        u4 o;
#pragma unroll
        for (int j = 0; j < 4; ++j) { int i = 8 * (j >> 1) + 2 * (j & 1); o[j] = pk(fmaxf(a[i], 0.f) * a[i + 4], fmaxf(a[i + 1], 0.f) * a[i + 5]); }
        st4(hb + (((2 * mt + wm) * 2 + (b ^ wn)) * 86 + tile) * 256 + lane * 4, o);
      }
  }
  if (LAST) {
    u32* o = x3 + ((4 * mt + w) * 16 + ng) * 512 + lane * 4;
    st4(o, pack8(accx, 0)); st4(o + 256, pack8(accx, 1));
  }
}

template <int LAST>
__global__ __launch_bounds__(512) void k_down(const u32* __restrict__ hb, const u32* __restrict__ wd, const float* __restrict__ rs2,
                                              const u32* __restrict__ res, u32* __restrict__ out, float* __restrict__ ssp) {
  __shared__ __attribute__((aligned(1024))) char lds[131072 + 512];
  const int xc = blockIdx.x & 7, jb = blockIdx.x >> 3, rg = 8 * (xc & 3) + (jb & 7), nt = 4 * (xc >> 2) + (jb >> 3);
  const int lane = threadIdx.x & 63, w8 = __builtin_amdgcn_readfirstlane(threadIdx.x >> 6), w = w8 & 3, r = lane & 31, h = lane >> 5;
  constexpr int KI = LAST ? 6 : 3;
  if (w8 >= 4) {
    const rsrc rs = mkr(w < 2 ? wd + (2 * nt + w) * 86 * 256 : hb + (2 * rg + w - 2) * 86 * 256);
    auto issue = [&](int c) {
      char* sl = lds + (c & 3) * 32768 + w * 8192;
      unroll<4>([&](auto I) { blds<I.v * 1024>(rs, lane * 16, c * 8192, sl); });
      unroll<2>([&](auto I) { blds<I.v * 1024>(rs, lane * 16, c * 8192 + 4096, sl + 4096); });
      if (c < 10) unroll<2>([&](auto I) { blds<I.v * 1024 + 2048>(rs, lane * 16, c * 8192 + 4096, sl + 4096); });
    };
    issue(0); issue(1); issue(2);
    waitv<16>(); bar();
    issue(3);
#pragma unroll 1
    for (int c = 0; c < 10; ++c) {
      if (c < 7) waitv<16>(); else if (c == 7) waitv<14>(); else if (c == 8) waitv<6>(); else waitv<0>();
      bar();
      if (c < 7) issue(c + 4);
    }
    if (!LAST) bar();
    return;
  }
  const int t = w >> 1, b = w & 1, rho = 64 * rg + 32 * b + r;
  u32 rv[8];
  if (LAST) {
    const u32* p = res + ((2 * rg + b) * 16 + 2 * nt + t) * 512 + lane * 4;
    u4 x = *(const u4*)p, y = *(const u4*)(p + 256);
#pragma unroll
    for (int j = 0; j < 4; ++j) { rv[j] = x[j]; rv[4 + j] = y[j]; }
  } else {
    const u16* p = (const u16*)res + (64 * nt + 32 * t + 4 * h) * (2 * PS) + rho;
#pragma unroll
    for (int j = 0; j < 8; ++j) { const u16* e = p + (8 * (j >> 1) + 2 * (j & 1)) * (2 * PS); rv[j] = (u32)e[0] | ((u32)e[2 * PS] << 16); }
  }
  const float rs = rs2[rho];
  struct Fr { u4 A[4], B[4]; };
  const int aOff = t * 8192 + lane * 16, bOff = 16384 + b * 8192 + lane * 16;
  auto rd = [&](const char* sl, auto N) {
    Fr f;
#pragma unroll
    for (int k = 0; k < N.v; ++k) { f.A[k] = lds128(sl + aOff + k * 1024); f.B[k] = lds128(sl + bOff + k * 1024); }
    return f;
  };
  f16v acc = {};
  auto mm = [&](const Fr& f, auto N) {
#pragma unroll
    for (int k = 0; k < N.v; ++k) acc = mf32(f.A[k], f.B[k], acc);
    mix<N.v, 2, 1>();
    SB();
  };
  bar();
  Fr f0 = rd(lds, IC<4>{}), f1;
#pragma unroll 1
  for (int c = 0; c < 10; ++c) {
    SB();
    f1 = rd(lds + (c & 3) * 32768 + 4096, IC<4>{}); mm(f0, IC<4>{});
    bar();
    SB();
    f0 = rd(lds + ((c + 1) & 3) * 32768, IC<4>{}); mm(f1, IC<4>{});
  }
  f1 = rd(lds + 2 * 32768 + 4096, IC<2>{}); mm(f0, IC<4>{});
  mm(f1, IC<2>{});
  u32 v[8];
#pragma unroll
  for (int j = 0; j < 8; ++j) v[j] = pk(lo(rv[j]) + rs * acc[2 * j], hi(rv[j]) + rs * acc[2 * j + 1]);
  if (LAST) {
    u32* o = out + (32 * (32 * b + r) + rg) * 256 + 32 * nt + 16 * t + 8 * h;
    *(u4*)o = u4{v[0], v[1], v[2], v[3]}; *(u4*)(o + 4) = u4{v[4], v[5], v[6], v[7]};
  } else {
    const int c0 = 32 * nt + 16 * t + 2 * h;
    float ss = 0.f;
#pragma unroll
    for (int j = 0; j < 8; ++j) {
      st1(out + (c0 + 4 * (j >> 1) + (j & 1)) * XI + rho, v[j]);
      u32 m = (j == 0 && c0 == 0) ? (v[j] & 0xffffu) : v[j];
      ss = dot2(m, m, ss);
    }
    ss += __shfl_xor(ss, 32);
    float* sl = (float*)(lds + 131072);
    if (h == 0) sl[w * 32 + r] = ss;
    bar();
    if (w == 0) ssp[nt * SS + 64 * rg + lane] = sl[lane] + sl[64 + lane];
  }
}

extern "C" void kernel_launch(void* const* d_in, const int* in_sizes, int n_in, void* d_out, int out_size, void* d_ws, size_t ws_size,
                              hipStream_t stream) {
  const u16 *x = (const u16*)d_in[0], *wr = (const u16*)d_in[1], *wi = (const u16*)d_in[2];
  const u16 *wg = (const u16*)d_in[4], *wu = (const u16*)d_in[5], *wd = (const u16*)d_in[6];
  char* ws = (char*)d_ws;
  const size_t MB = 1 << 20;
  u32 *xh0 = (u32*)ws, *xh1 = (u32*)(ws + 3 * MB), *xh2 = (u32*)(ws + 6 * MB), *xh3 = (u32*)(ws + 9 * MB);
  u32 *filt = (u32*)(ws + 12 * MB), *x3 = (u32*)(ws + 15 * MB), *wgu = (u32*)(ws + 17 * MB), *wd1 = (u32*)(ws + 20 * MB);
  u32 *wd2 = (u32*)(ws + 22 * MB), *hb = (u32*)(ws + 24 * MB), *hb2 = (u32*)(ws + 30 * MB);
  float *rstd = (float*)(ws + 36 * MB), *ssp = rstd + 4096, *rs2 = rstd + 32768, *rs3 = rstd + 65536;
  k_prep<<<303, 512, 0, stream>>>(x, wr, wi, wg, wu, wd, xh0, (u32*)rstd, wgu, wd1, wd2, filt);
  k_fft<1><<<256, 256, 0, stream>>>(xh0, rstd, filt, xh1);
  k_up<0><<<256, 512, 0, stream>>>(xh1, wgu, hb, x3, rs2);
  k_down<0><<<256, 512, 0, stream>>>(hb, wd1, rs2, xh1, xh2, ssp);
  k_fft<8><<<256, 256, 0, stream>>>(xh2, ssp, filt, xh3);
  k_up<1><<<256, 512, 0, stream>>>(xh3, wgu, hb2, x3, rs3);
  k_down<1><<<256, 512, 0, stream>>>(hb2, wd2, rs3, x3, (u32*)d_out, ssp);
}
```
